```python
import torch

def _softmax(x, axis=-1):
    return torch.softmax(x, dim=axis)
def _gelu(x):
    return torch.nn.functional.gelu(x, approximate="tanh")
def _silu(x):
    return torch.nn.functional.silu(x)
def _sigmoid(x):
    return torch.sigmoid(x)
def _layernorm(x, axis=-1, eps=1e-5):
    m = x.mean(dim=axis, keepdim=True)
    v = x.var(dim=axis, keepdim=True, unbiased=False)
    return (x - m) / torch.sqrt(v + eps)
def _scatter_add(tgt, idx, src, axis=0):
    return tgt.clone().index_add_(axis, idx, src)
def _segment_sum(src, seg, n):
    out = torch.zeros((n,) + src.shape[1:], dtype=src.dtype)
    return out.index_add_(0, seg, src)
def _pad(x, pw):
    flat = tuple(v for pair in reversed(pw) for v in pair)
    return torch.nn.functional.pad(x, flat)

kernel_name = 'standalone__mha_softmax__near_ridge'

def setup_inputs():

    _w = (lambda a: (lambda t: t.to(torch.bfloat16) if t.is_floating_point() else t)(torch.as_tensor(a)))
    B, H, Sq, Sk, D = 16, 32, 256, 256, 128
    return {
        "q": _w((lambda *s: torch.randn(*s, dtype=torch.bfloat16) * 0.8)(B, H, Sq, D)),
        "k": _w((lambda *s: torch.randn(*s, dtype=torch.bfloat16) * 0.8)(B, H, Sk, D)),
        "v": _w((lambda *s: torch.randn(*s, dtype=torch.bfloat16) * 0.05)(B, H, Sk, D)),
    }

def reference(q, k, v):
    scale = q.shape[-1] ** -0.5
    scores = torch.einsum("bhqd,bhkd->bhqk", q, k) * scale
    p = _softmax(scores, axis=-1)
    return torch.einsum("bhqk,bhkd->bhqd", p, v)
```

```cpp
#include <hip/hip_runtime.h>
#include <hip/hip_bf16.h>

typedef float    f32x16 __attribute__((ext_vector_type(16)));
typedef float    f32x2  __attribute__((ext_vector_type(2)));
typedef __bf16   bf16x8 __attribute__((ext_vector_type(8)));
typedef __bf16   bf16x2 __attribute__((ext_vector_type(2)));
typedef short    s16x4  __attribute__((ext_vector_type(4)));
typedef short    s16x8  __attribute__((ext_vector_type(8)));
typedef unsigned u32x4  __attribute__((ext_vector_type(4)));
typedef unsigned u32x2  __attribute__((ext_vector_type(2)));
typedef __attribute__((address_space(3))) const bf16x8* lds_b8p;
typedef __attribute__((address_space(3))) s16x4* lds_s4p;
typedef __attribute__((address_space(3))) u32x2* lds_u2p;
typedef __attribute__((address_space(3))) const u32x4* lds_u4p;

#define DI __device__ __forceinline__
#define LDPOL ""
#define STAUX 0

constexpr unsigned HEADB = 65536u;
constexpr unsigned KREG = 0u, VREG = 65536u, OREG = 131072u, LDS_BYTES = 163840u;
constexpr int NHEADS = 512, NWG = 256;
constexpr float CEXP = 0.08838834764831845f * 1.4426950408889634f;

DI unsigned cvtpk(float lo, float hi) { f32x2 v = {lo, hi}; bf16x2 b = __builtin_convertvector(v, bf16x2); return __builtin_bit_cast(unsigned, b); }
DI float fmax2(float a, float b) { return __builtin_elementwise_maximum(a, b); }

DI void dma_tile(unsigned m0v, unsigned v0, unsigned v1, __amdgpu_buffer_rsrc_t srd, unsigned soff) {
  asm volatile("s_mov_b32 m0, %0\n\ts_nop 0\n\t"
               "buffer_load_dwordx4 %1, %3, %4 offen" LDPOL " lds\n\t"
               "buffer_load_dwordx4 %2, %3, %4 offen offset:1024" LDPOL " lds"
               :: "s"(m0v), "v"(v0), "v"(v1), "s"(srd), "s"(soff) : "memory");
}
DI void load_q(bf16x8 (&q)[8], unsigned voff, __amdgpu_buffer_rsrc_t srd, unsigned soff) {
  asm volatile("buffer_load_dwordx4 %0, %8, %9, %10 offen" LDPOL "\n\t"
               "buffer_load_dwordx4 %1, %8, %9, %10 offen offset:32" LDPOL "\n\t"
               "buffer_load_dwordx4 %2, %8, %9, %10 offen offset:64" LDPOL "\n\t"
               "buffer_load_dwordx4 %3, %8, %9, %10 offen offset:96" LDPOL "\n\t"
               "buffer_load_dwordx4 %4, %8, %9, %10 offen offset:128" LDPOL "\n\t"
               "buffer_load_dwordx4 %5, %8, %9, %10 offen offset:160" LDPOL "\n\t"
               "buffer_load_dwordx4 %6, %8, %9, %10 offen offset:192" LDPOL "\n\t"
               "buffer_load_dwordx4 %7, %8, %9, %10 offen offset:224" LDPOL
               : "=&v"(q[0]), "=&v"(q[1]), "=&v"(q[2]), "=&v"(q[3]), "=&v"(q[4]), "=&v"(q[5]), "=&v"(q[6]), "=&v"(q[7])
               : "v"(voff), "s"(srd), "s"(soff) : "memory");
}
template <int N> DI void wait_bar() { asm volatile("s_waitcnt vmcnt(%0) lgkmcnt(0)\n\ts_barrier" :: "i"(N) : "memory"); }
template <int N> DI void wait_bar_q(bf16x8 (&q)[8]) {
  asm volatile("s_waitcnt vmcnt(%8) lgkmcnt(0)\n\ts_barrier"
               : "+v"(q[0]), "+v"(q[1]), "+v"(q[2]), "+v"(q[3]), "+v"(q[4]), "+v"(q[5]), "+v"(q[6]), "+v"(q[7]) : "i"(N) : "memory");
}

struct Ctx {
  __amdgpu_buffer_rsrc_t sq, sk, sv, so;
  unsigned wid, lane;
  unsigned vsrc0, vsrc1;
  unsigned vq;
  unsigned ka;
  unsigned va;
  unsigned ow, orr, og;
};

DI void qk_tile(f32x16 (&S)[8], const bf16x8 (&q)[8], unsigned ka, const int t) {
#pragma unroll
  for (int kk = 0; kk < 2; ++kk) {
    const int kb = 2 * t + kk;
    f32x16 acc = {};
#pragma unroll
    for (int d0 = 0; d0 < 8; ++d0) {
      const bf16x8 kf = *(lds_b8p)((ka ^ (32u * d0)) + 8192u * kb);
      acc = __builtin_amdgcn_mfma_f32_32x32x16_bf16(kf, q[d0], acc, 0, 0, 0);
    }
    S[kb] = acc;
  }
}
DI void pv_tile(f32x16 (&o)[4], const u32x4 (&P)[16], unsigned va, const int t) {
#pragma unroll
  for (int kk = 0; kk < 4; ++kk) {
    const int ks = 4 * t + kk;
#pragma unroll
    for (int db = 0; db < 4; ++db) {
      const s16x4 lo = __builtin_amdgcn_ds_read_tr16_b64_v4i16((lds_s4p)((va ^ (64u * db)) + 4096u * ks));
      const s16x4 hi = __builtin_amdgcn_ds_read_tr16_b64_v4i16((lds_s4p)((va ^ (64u * db) ^ 32u) + 4096u * ks + 2048u));
      const s16x8 vf = __builtin_shufflevector(lo, hi, 0, 1, 2, 3, 4, 5, 6, 7);
      o[db] = __builtin_amdgcn_mfma_f32_32x32x16_bf16(__builtin_bit_cast(bf16x8, vf), __builtin_bit_cast(bf16x8, P[ks]), o[db], 0, 0, 0);
    }
  }
}

template <bool FIRST, bool LAST>
DI void head(const Ctx& c, bf16x8 (&q)[8], unsigned hoff, unsigned hnext) {
  const unsigned wk = c.wid * 2048u;
  f32x16 S[8];
  wait_bar_q<FIRST ? 14 : 16>(q);
  if (!FIRST) dma_tile(VREG + 49152u + wk, c.vsrc0, c.vsrc1, c.sv, hoff + 49152u + wk);
  qk_tile(S, q, c.ka, 0);
  wait_bar<FIRST ? 12 : 18>();
  if (!LAST) dma_tile(KREG + wk, c.vsrc0, c.vsrc1, c.sk, hnext + wk);
  qk_tile(S, q, c.ka, 1);
  wait_bar<FIRST ? 12 : (LAST ? 18 : 20)>();
  if (!LAST) dma_tile(KREG + 16384u + wk, c.vsrc0, c.vsrc1, c.sk, hnext + 16384u + wk);
  qk_tile(S, q, c.ka, 2);
  wait_bar<FIRST ? 12 : (LAST ? 16 : 20)>();
  if (!LAST) dma_tile(KREG + 32768u + wk, c.vsrc0, c.vsrc1, c.sk, hnext + 32768u + wk);
  qk_tile(S, q, c.ka, 3);
  if (!LAST) load_q(q, c.vq, c.sq, hnext + c.wid * 8192u);
  float m0 = S[0][0], m1 = S[0][1], m2 = S[0][2], m3 = S[0][3];
#pragma unroll
  for (int kb = 0; kb < 8; ++kb)
#pragma unroll
    for (int r = (kb == 0 ? 4 : 0); r < 16; r += 4) {
      m0 = fmax2(m0, S[kb][r]); m1 = fmax2(m1, S[kb][r + 1]); m2 = fmax2(m2, S[kb][r + 2]); m3 = fmax2(m3, S[kb][r + 3]);
    }
  float m = fmax2(fmax2(m0, m1), fmax2(m2, m3));
  { auto rr = __builtin_amdgcn_permlane32_swap(__float_as_uint(m), __float_as_uint(m), false, false);
    m = fmax2(__uint_as_float(rr[0]), __uint_as_float(rr[1])); }
  const float mc = -m * CEXP;
  float l0 = 0.f, l1 = 0.f, l2 = 0.f, l3 = 0.f;
  u32x4 P[16];
#pragma unroll
  for (int kb = 0; kb < 8; ++kb) {
    float e[16];
#pragma unroll
    for (int r = 0; r < 16; ++r) e[r] = __builtin_amdgcn_exp2f(__builtin_fmaf(S[kb][r], CEXP, mc));
#pragma unroll
    for (int r = 0; r < 16; r += 4) { l0 += e[r]; l1 += e[r + 1]; l2 += e[r + 2]; l3 += e[r + 3]; }
#pragma unroll
    for (int s = 0; s < 2; ++s)
      P[2 * kb + s] = u32x4{cvtpk(e[8 * s], e[8 * s + 1]), cvtpk(e[8 * s + 2], e[8 * s + 3]), cvtpk(e[8 * s + 4], e[8 * s + 5]), cvtpk(e[8 * s + 6], e[8 * s + 7])};
  }
  float l = (l0 + l1) + (l2 + l3);
  { auto rr = __builtin_amdgcn_permlane32_swap(__float_as_uint(l), __float_as_uint(l), false, false);
    l = __uint_as_float(rr[0]) + __uint_as_float(rr[1]); }
  const float il = __builtin_amdgcn_rcpf(l);
  f32x16 o[4] = {};
  wait_bar<FIRST ? 20 : (LAST ? 14 : 28)>();
  if (!LAST) dma_tile(KREG + 49152u + wk, c.vsrc0, c.vsrc1, c.sk, hnext + 49152u + wk);
  pv_tile(o, P, c.va, 0);
  wait_bar<FIRST ? 20 : (LAST ? 12 : 28)>();
  if (!LAST) dma_tile(VREG + wk, c.vsrc0, c.vsrc1, c.sv, hnext + wk);
  pv_tile(o, P, c.va, 1);
  wait_bar<FIRST ? 20 : (LAST ? 10 : 28)>();
  if (!LAST) dma_tile(VREG + 16384u + wk, c.vsrc0, c.vsrc1, c.sv, hnext + 16384u + wk);
  pv_tile(o, P, c.va, 2);
  wait_bar<FIRST ? 20 : (LAST ? 0 : 20)>();
  if (!LAST) dma_tile(VREG + 32768u + wk, c.vsrc0, c.vsrc1, c.sv, hnext + 32768u + wk);
  pv_tile(o, P, c.va, 3);
#pragma unroll
  for (int p = 0; p < 2; ++p) {
#pragma unroll
    for (int dd = 0; dd < 2; ++dd) {
      const int db = 2 * p + dd;
#pragma unroll
      for (int g = 0; g < 4; ++g) {
        u32x2 w = {cvtpk(o[db][4 * g] * il, o[db][4 * g + 1] * il), cvtpk(o[db][4 * g + 2] * il, o[db][4 * g + 3] * il)};
        *(lds_u2p)(c.ow ^ (16u * (4 * dd + g))) = w;
      }
    }
#pragma unroll
    for (int i = 0; i < 4; ++i) {
      const u32x4 w = *(lds_u4p)(c.orr + 1024u * i);
      __builtin_amdgcn_raw_buffer_store_b128(w, c.so, c.og + 2048u * i + 128u * p, hoff + c.wid * 8192u, STAUX);
    }
  }
}

__global__ __launch_bounds__(512, 2) void attn_fwd(const void* __restrict__ Q, const void* __restrict__ K, const void* __restrict__ V, void* __restrict__ O) {
  Ctx c;
  const unsigned tid = threadIdx.x, lane = tid & 63u;
  c.lane = lane;
  c.wid = __builtin_amdgcn_readfirstlane(tid >> 6);
  const int nbytes = NHEADS * (int)HEADB;
  c.sq = __builtin_amdgcn_make_buffer_rsrc((void*)Q, (short)0, nbytes, 0x00020000);
  c.sk = __builtin_amdgcn_make_buffer_rsrc((void*)K, (short)0, nbytes, 0x00020000);
  c.sv = __builtin_amdgcn_make_buffer_rsrc((void*)V, (short)0, nbytes, 0x00020000);
  c.so = __builtin_amdgcn_make_buffer_rsrc((void*)O, (short)0, nbytes, 0x00020000);
  {
    const unsigned r = lane >> 4, p = lane & 15u, w2 = 2u * c.wid;
    c.vsrc0 = r * 256u + 16u * (p ^ ((r << 2) | ((w2 + 0u) & 3u)));
    c.vsrc1 = r * 256u + 16u * (p ^ ((r << 2) | ((w2 + 1u) & 3u)));
  }
  const unsigned l32 = lane & 31u, h = lane >> 5;
  c.vq = l32 * 256u + h * 16u;
  { const unsigned f = ((l32 & 3u) << 2) | ((l32 >> 2) & 3u); c.ka = KREG + l32 * 256u + 16u * (f ^ h); }
  { const unsigned blk = (lane >> 4) & 1u, q4 = (lane & 15u) >> 2, p = lane & 3u;
    c.va = VREG + 256u * (4u * h + q4) + 16u * (4u * q4 + ((2u * blk + (p >> 1)) ^ h)) + 8u * (p & 1u); }
  c.ow = OREG + c.wid * 4096u + l32 * 128u + 16u * (l32 & 7u) + 8u * h;
  { const unsigned rr = lane >> 3, cc = lane & 7u;
    c.orr = OREG + c.wid * 4096u + rr * 128u + 16u * (cc ^ rr);
    c.og = rr * 256u + cc * 16u; }
  const unsigned hA = blockIdx.x * HEADB, hB = (blockIdx.x + NWG) * HEADB;
  const unsigned wk = c.wid * 2048u;
  bf16x8 q[8];
  asm volatile("s_nop 4");
  load_q(q, c.vq, c.sq, hA + c.wid * 8192u);
#pragma unroll
  for (int t = 0; t < 4; ++t) dma_tile(KREG + 16384u * t + wk, c.vsrc0, c.vsrc1, c.sk, hA + 16384u * t + wk);
#pragma unroll
  for (int t = 0; t < 4; ++t) dma_tile(VREG + 16384u * t + wk, c.vsrc0, c.vsrc1, c.sv, hA + 16384u * t + wk);
  head<true, false>(c, q, hA, hB);
  head<false, true>(c, q, hB, 0u);
}

extern "C" void kernel_launch(void* const* d_in, const int* in_sizes, int n_in, void* d_out, int out_size, void* d_ws, size_t ws_size, hipStream_t stream) {
  (void)in_sizes; (void)n_in; (void)out_size; (void)d_ws; (void)ws_size;
  (void)hipFuncSetAttribute((const void*)attn_fwd, hipFuncAttributeMaxDynamicSharedMemorySize, (int)LDS_BYTES);
  hipLaunchKernelGGL(attn_fwd, dim3(NWG), dim3(512), LDS_BYTES, stream, d_in[0], d_in[1], d_in[2], d_out);
}
```

```cpp
#include <hip/hip_runtime.h>
#include <hip/hip_bf16.h>
#include <utility>

typedef float    f32x16 __attribute__((ext_vector_type(16)));
typedef float    f32x2  __attribute__((ext_vector_type(2)));
typedef __bf16   bf16x8 __attribute__((ext_vector_type(8)));
typedef __bf16   bf16x2 __attribute__((ext_vector_type(2)));
typedef short    s16x4  __attribute__((ext_vector_type(4)));
typedef short    s16x8  __attribute__((ext_vector_type(8)));
typedef unsigned u32x4  __attribute__((ext_vector_type(4)));
typedef unsigned u32x2  __attribute__((ext_vector_type(2)));
typedef __attribute__((address_space(3))) const bf16x8* lds_b8p;
typedef __attribute__((address_space(3))) s16x4* lds_s4p;
typedef __attribute__((address_space(3))) u32x2* lds_u2p;
typedef __attribute__((address_space(3))) const u32x4* lds_u4p;

#define DI __device__ __forceinline__
#define GAP() __builtin_amdgcn_sched_barrier(0)
#define PIN(x) asm volatile("" : "+v"(x))
#define LDPOL " nt"
#define STAUX 16
#define BOFF 8

constexpr unsigned HEADB = 65536u;
constexpr unsigned RQO = 0u, RING = 65536u, LDS_BYTES = 163840u;
constexpr int NHEADS = 512, NWG = 256;
constexpr float CEXP = 0.08838834764831845f * 1.4426950408889634f;
constexpr int PFK = 4, PFV = 3;

DI unsigned cvtpk(float lo, float hi) { f32x2 v = {lo, hi}; bf16x2 b = __builtin_convertvector(v, bf16x2); return __builtin_bit_cast(unsigned, b); }
DI float fmax2(float a, float b) { return __builtin_elementwise_maximum(a, b); }
DI float fmax3(float a, float b, float c) { return __builtin_elementwise_maximum(__builtin_elementwise_maximum(a, b), c); }
DI unsigned opaque(unsigned x) { asm volatile("" : "+v"(x)); return x; }

struct Stamps { int dummy; };
#define ST(i) ((void)0)

DI void dma2(unsigned m0v, unsigned v0, unsigned v1, __amdgpu_buffer_rsrc_t srd, unsigned soff) {
  asm volatile("s_mov_b32 m0, %0\n\ts_nop 0\n\t"
               "buffer_load_dwordx4 %1, %3, %4 offen" LDPOL " lds\n\t"
               "buffer_load_dwordx4 %2, %3, %4 offen offset:1024" LDPOL " lds"
               :: "s"(m0v), "v"(v0), "v"(v1), "s"(srd), "s"(soff) : "memory");
}
template <int N> DI void wait_bar() { asm volatile("s_waitcnt vmcnt(%0)\n\ts_barrier" :: "i"(N) : "memory"); }
template <int N> DI void wait_vm() { asm volatile("s_waitcnt vmcnt(%0)" :: "i"(N) : "memory"); }

struct Ctx {
  __amdgpu_buffer_rsrc_t sq, sk, sv, so;
  unsigned wid, lane;
  unsigned v0;
  unsigned hA, hB;
};
constexpr unsigned slot_of(int i) { return RING + 16384u * (unsigned)(i % 6); }

template <int I> DI void dma_ring(const Ctx& c) {
  const unsigned w2 = (32u * c.wid) & 48u;
  dma2(slot_of(I) + c.wid * 2048u, c.v0 ^ w2, c.v0 ^ (w2 + 16u), (I & 4) ? c.sv : c.sk, ((I & 8) ? c.hB : c.hA) + 16384u * (I & 3) + c.wid * 2048u);
}
template <int T> DI void dma_q(const Ctx& c, unsigned hoff) {
  const unsigned o = c.wid * 8192u + 2048u * T;
  dma2(RQO + o, c.v0 ^ (16u * ((2 * T) & 3)), c.v0 ^ (16u * ((2 * T + 1) & 3)), c.sq, hoff + o);
}
DI unsigned row_base(unsigned lane) { const unsigned l32 = lane & 31u, h = lane >> 5, f = ((l32 & 3u) << 2) | ((l32 >> 2) & 3u); return l32 * 256u + 16u * (f ^ h); }
DI void read_q(const Ctx& c, bf16x8 (&q)[8]) {
  const unsigned b = row_base(opaque(c.lane)) + RQO + c.wid * 8192u;
#pragma unroll
  for (int d0 = 0; d0 < 8; ++d0) q[d0] = *(lds_b8p)(b ^ (32u * d0));
}
template <int I0, int I1> DI void store_o(const Ctx& c, unsigned hoff) {
  const unsigned lane = opaque(c.lane), rr = lane >> 4, cc = lane & 15u;
  const unsigned b0 = RQO + c.wid * 8192u + rr * 256u + 16u * (cc ^ rr), b1 = b0 ^ 64u;
  u32x4 w[8];
#pragma unroll
  for (int i = I0; i < I1; ++i) w[i] = *(lds_u4p)(((i & 1) ? b1 : b0) + 1024u * i);
#pragma unroll
  for (int i = I0; i < I1; ++i) __builtin_amdgcn_raw_buffer_store_b128(w[i], c.so, lane * 16u + 1024u * i, hoff + c.wid * 8192u, STAUX);
}

struct Hd {
  f32x16 S[8], o[4];
  bf16x8 kf[64];
  s16x4 vlo[64], vhi[64];
  u32x4 P[2];
  unsigned ka[8], va[8];
  float mx[2], l[2], mc;
};

template <int H, int G> DI void events(const Ctx& c) {
  constexpr int R = 8 * H;
  if constexpr ((G + BOFF) % 16 == 0 && G + BOFF >= 16 && G + BOFF <= 112) {
    constexpr int j = (G + BOFF) / 16;
    constexpr int pos[12] = {-1, 14, 28, 42, 56, 67, 77, 87, 97, 107, 117, 127};
    constexpr int ej = j - 2;
    int n = 0;
    for (int e = ej + 1; e < 12; ++e) if (pos[e] < G) n += (H == 1 && e >= 10) ? 0 : 2;
    constexpr int N = []() constexpr { int n = 0; for (int e = ej + 1; e < 12; ++e) if (pos[e] < G) n += (H == 1 && e >= 10) ? 0 : 2; return n; }();
    (void)n;
    wait_bar<N>();
  }
  if constexpr (G == 14) dma_ring<R + 3>(c);
  if constexpr (G == 28) dma_ring<R + 4>(c);
  if constexpr (G == 42) dma_ring<R + 5>(c);
  if constexpr (G == 56) dma_ring<R + 6>(c);
  if constexpr (G == 67) dma_ring<R + 7>(c);
  if constexpr (H == 0) {
    if constexpr (G == 77) dma_q<0>(c, c.hB);
    if constexpr (G == 87) dma_q<1>(c, c.hB);
    if constexpr (G == 97) dma_q<2>(c, c.hB);
    if constexpr (G == 107) dma_q<3>(c, c.hB);
    if constexpr (G == 117) dma_ring<8>(c);
    if constexpr (G == 127) dma_ring<9>(c);
  } else {
    if constexpr (G == 77) store_o<0, 2>(c, c.hA);
    if constexpr (G == 87) store_o<2, 4>(c, c.hA);
    if constexpr (G == 97) store_o<4, 6>(c, c.hA);
    if constexpr (G == 107) store_o<6, 8>(c, c.hA);
  }
}
template <int H, int G> DI void k_read(Hd& s) {
  constexpr int I = 8 * H + (G >> 4);
  s.kf[G] = *(lds_b8p)(s.ka[G & 7] + 16384u * ((I % 6) % 3) + 8192u * ((G >> 3) & 1));
}
template <int H, int GP> DI void v_read(Hd& s) {
  constexpr int I = 8 * H + 4 + (GP >> 4), ks = (GP >> 2) & 3, db = GP & 3;
  s.vlo[GP] = __builtin_amdgcn_ds_read_tr16_b64_v4i16((lds_s4p)(s.va[2 * db] + 16384u * ((I % 6) % 3) + 4096u * ks));
  s.vhi[GP] = __builtin_amdgcn_ds_read_tr16_b64_v4i16((lds_s4p)(s.va[2 * db + 1] + 16384u * ((I % 6) % 3) + 4096u * ks + 2048u));
}
template <int IPREV, int INEXT> DI void rebase(unsigned (&a)[8]) {
  if constexpr ((IPREV % 6) / 3 != (INEXT % 6) / 3) {
#pragma unroll
    for (int i = 0; i < 8; ++i) a[i] = opaque((INEXT % 6) >= 3 ? a[i] + 49152u : a[i] - 49152u);
  }
}
DI void sm_pair(Hd& s, int ks, int pr) {
  const int kb = ks >> 1, r = 8 * (ks & 1) + 2 * pr;
  const float e0 = __builtin_amdgcn_exp2f(__builtin_fmaf(s.S[kb][r], CEXP, s.mc));
  const float e1 = __builtin_amdgcn_exp2f(__builtin_fmaf(s.S[kb][r + 1], CEXP, s.mc));
  s.l[0] += e0; s.l[1] += e1;
  unsigned w = cvtpk(e0, e1);
  PIN(s.l[0]); PIN(s.l[1]); PIN(w);
  s.P[ks & 1][pr] = w;
}
DI void va_init(const Ctx& c, Hd& s, unsigned base) {
  const unsigned lane = opaque(c.lane), h = lane >> 5, blk = (lane >> 4) & 1u, q4 = (lane & 15u) >> 2, p = lane & 3u;
  const unsigned vb = 256u * (4u * h + q4) + 16u * (4u * q4 + ((2u * blk + (p >> 1)) ^ h)) + 8u * (p & 1u);
#pragma unroll
  for (int i = 0; i < 8; ++i) s.va[i] = opaque((vb ^ (64u * (i >> 1)) ^ (32u * (i & 1))) + base);
}

template <int H, int G> DI void gap(const Ctx& c, Hd& s, const bf16x8 (&q)[8], Stamps& st) {
  constexpr int R = 8 * H;
  events<H, G>(c);
  if constexpr (G < 64) {
    if constexpr (G + PFK < 64) {
      if constexpr (((G + PFK) & 15) == 0) rebase<R + ((G + PFK) >> 4) - 1, R + ((G + PFK) >> 4)>(s.ka);
      k_read<H, G + PFK>(s);
    }
    if constexpr (G == 60) va_init(c, s, RING + (((R + 4) % 6) >= 3 ? 49152u : 0u));
    if constexpr (G + PFV >= 64) v_read<H, G + PFV - 64>(s);
    constexpr int kb = G >> 3, d0 = G & 7;
    if constexpr (d0 == 0) s.S[kb] = __builtin_amdgcn_mfma_f32_32x32x16_bf16(s.kf[G], q[d0], f32x16{}, 0, 0, 0);
    else                   s.S[kb] = __builtin_amdgcn_mfma_f32_32x32x16_bf16(s.kf[G], q[d0], s.S[kb], 0, 0, 0);
    PIN(s.S[kb]);
    if constexpr (kb >= 1 && d0 >= 2 && d0 < 6) {
      constexpr int pb = kb - 1, r = 4 * (d0 - 2);
      if constexpr (pb == 0 && d0 == 2) { s.mx[0] = fmax2(s.S[pb][r], s.S[pb][r + 1]); s.mx[1] = fmax2(s.S[pb][r + 2], s.S[pb][r + 3]); }
      else { s.mx[0] = fmax3(s.mx[0], s.S[pb][r], s.S[pb][r + 1]); s.mx[1] = fmax3(s.mx[1], s.S[pb][r + 2], s.S[pb][r + 3]); }
      PIN(s.mx[0]); PIN(s.mx[1]);
    }
    if constexpr (G == 63) {
#pragma unroll
      for (int r = 0; r < 16; r += 4) { s.mx[0] = fmax3(s.mx[0], s.S[7][r], s.S[7][r + 1]); s.mx[1] = fmax3(s.mx[1], s.S[7][r + 2], s.S[7][r + 3]); }
      float m = fmax2(s.mx[0], s.mx[1]);
      auto rr = __builtin_amdgcn_permlane32_swap(__float_as_uint(m), __float_as_uint(m), false, false);
      m = fmax2(__uint_as_float(rr[0]), __uint_as_float(rr[1]));
      s.mc = -m * CEXP;
      s.l[0] = 0.f; s.l[1] = 0.f;
#pragma unroll
      for (int pr = 0; pr < 4; ++pr) sm_pair(s, 0, pr);
    }
  } else {
    constexpr int GP = G - 64, ks = GP >> 2, db = GP & 3;
    if constexpr (GP + PFV < 64) {
      if constexpr (((GP + PFV) & 15) == 0) rebase<R + 4 + ((GP + PFV) >> 4) - 1, R + 4 + ((GP + PFV) >> 4)>(s.va);
      v_read<H, GP + PFV>(s);
    }
    const s16x8 vf = __builtin_shufflevector(s.vlo[GP], s.vhi[GP], 0, 1, 2, 3, 4, 5, 6, 7);
    if constexpr (ks == 0) s.o[db] = __builtin_amdgcn_mfma_f32_32x32x16_bf16(__builtin_bit_cast(bf16x8, vf), __builtin_bit_cast(bf16x8, s.P[0]), f32x16{}, 0, 0, 0);
    else                   s.o[db] = __builtin_amdgcn_mfma_f32_32x32x16_bf16(__builtin_bit_cast(bf16x8, vf), __builtin_bit_cast(bf16x8, s.P[ks & 1]), s.o[db], 0, 0, 0);
    PIN(s.o[db]);
    if constexpr (ks + 1 < 16) sm_pair(s, ks + 1, db);
  }
  GAP();
}
template <int H, int... Gs> DI void gaps(const Ctx& c, Hd& s, const bf16x8 (&q)[8], Stamps& st, std::integer_sequence<int, Gs...>) { (gap<H, Gs>(c, s, q, st), ...); }

template <int H>
DI void head(const Ctx& c, bf16x8 (&q)[8], Stamps& st) {
  constexpr int R = 8 * H;
  Hd s;
  { const unsigned ra = row_base(opaque(c.lane));
#pragma unroll
    for (int d0 = 0; d0 < 8; ++d0) s.ka[d0] = opaque((ra ^ (32u * d0)) + RING + ((R % 6) >= 3 ? 49152u : 0u)); }
  dma_ring<R + 2>(c);
  GAP();
  wait_bar<4>();
  GAP();
  k_read<H, 0>(s); k_read<H, 1>(s); k_read<H, 2>(s); k_read<H, 3>(s);
  GAP();
  gaps<H>(c, s, q, st, std::make_integer_sequence<int, 128>{});
  ST(1 + 9 * H + 8 + (H ? 1 : 0));
  float ls = s.l[0] + s.l[1];
  { auto rr = __builtin_amdgcn_permlane32_swap(__float_as_uint(ls), __float_as_uint(ls), false, false);
    ls = __uint_as_float(rr[0]) + __uint_as_float(rr[1]); }
  const float il = __builtin_amdgcn_rcpf(ls);
  u32x2 w[16];
#pragma unroll
  for (int db = 0; db < 4; ++db)
#pragma unroll
    for (int g = 0; g < 4; ++g)
      w[4 * db + g] = u32x2{cvtpk(s.o[db][4 * g] * il, s.o[db][4 * g + 1] * il), cvtpk(s.o[db][4 * g + 2] * il, s.o[db][4 * g + 3] * il)};
  GAP();
  if constexpr (H == 0) {
    wait_vm<4>();
    GAP();
    read_q(c, q);
    GAP(); asm volatile("s_waitcnt lgkmcnt(0)" ::: "memory"); GAP();
  }
  {
    const unsigned lane = opaque(c.lane), l32 = lane & 31u, h = lane >> 5;
    const unsigned ow = RQO + c.wid * 8192u + l32 * 256u + 16u * (l32 & 7u) + 8u * h;
#pragma unroll
    for (int i = 0; i < 16; ++i) *(lds_u2p)(ow ^ (16u * i)) = w[i];
  }
  if constexpr (H == 1) store_o<0, 8>(c, c.hB);
  GAP();
}

__global__ __launch_bounds__(512, 2) void attn_fwd(const void* __restrict__ Q, const void* __restrict__ K, const void* __restrict__ V, void* __restrict__ O, unsigned* __restrict__ dbg) {
  Stamps st;
  ST(0);
  Ctx c;
  const unsigned tid = threadIdx.x, lane = tid & 63u;
  c.lane = lane;
  c.wid = __builtin_amdgcn_readfirstlane(tid >> 6);
  const int nbytes = NHEADS * (int)HEADB;
  c.sq = __builtin_amdgcn_make_buffer_rsrc((void*)Q, (short)0, nbytes, 0x00020000);
  c.sk = __builtin_amdgcn_make_buffer_rsrc((void*)K, (short)0, nbytes, 0x00020000);
  c.sv = __builtin_amdgcn_make_buffer_rsrc((void*)V, (short)0, nbytes, 0x00020000);
  c.so = __builtin_amdgcn_make_buffer_rsrc((void*)O, (short)0, nbytes, 0x00020000);
  {
    const unsigned r = lane >> 4, p = lane & 15u;
    c.v0 = r * 256u + 16u * (p ^ (r << 2));
  }
  c.hA = blockIdx.x * HEADB; c.hB = (blockIdx.x + NWG) * HEADB;
  asm volatile("s_nop 4");
  dma_q<0>(c, c.hA); dma_q<1>(c, c.hA); dma_q<2>(c, c.hA); dma_q<3>(c, c.hA);
  dma_ring<0>(c); dma_ring<1>(c);
  bf16x8 q[8];
  GAP();
  wait_vm<4>();
  GAP();
  ST(1);
  read_q(c, q);
  head<0>(c, q, st);
  head<1>(c, q, st);
}

extern "C" void kernel_launch(void* const* d_in, const int* in_sizes, int n_in, void* d_out, int out_size, void* d_ws, size_t ws_size, hipStream_t stream) {
  (void)in_sizes; (void)n_in; (void)out_size; (void)ws_size;
  (void)hipFuncSetAttribute((const void*)attn_fwd, hipFuncAttributeMaxDynamicSharedMemorySize, (int)LDS_BYTES);
  hipLaunchKernelGGL(attn_fwd, dim3(NWG), dim3(512), LDS_BYTES, stream, d_in[0], d_in[1], d_in[2], d_out, (unsigned*)d_ws);
}
```

```cpp
#include <hip/hip_runtime.h>
#include <hip/hip_bf16.h>
#include <utility>
#define NREP 1

typedef float    f32x16 __attribute__((ext_vector_type(16)));
typedef float    f32x2  __attribute__((ext_vector_type(2)));
typedef __bf16   bf16x8 __attribute__((ext_vector_type(8)));
typedef __bf16   bf16x2 __attribute__((ext_vector_type(2)));
typedef short    s16x4  __attribute__((ext_vector_type(4)));
typedef short    s16x8  __attribute__((ext_vector_type(8)));
typedef unsigned u32x4  __attribute__((ext_vector_type(4)));
typedef unsigned u32x2  __attribute__((ext_vector_type(2)));
typedef __attribute__((address_space(3))) const bf16x8* lds_b8p;
typedef __attribute__((address_space(3))) s16x4* lds_s4p;
typedef __attribute__((address_space(3))) u32x2* lds_u2p;
typedef __attribute__((address_space(3))) const u32x4* lds_u4p;

#define DI __device__ __forceinline__
#define GAP() __builtin_amdgcn_sched_barrier(0)
#define PIN(x) asm volatile("" : "+v"(x))
#define LDPOL " nt"
#define STAUX 16
#define BOFF 8

constexpr unsigned HEADB = 65536u;
constexpr unsigned RQO = 0u, RING = 65536u, LDS_BYTES = 163840u;
constexpr int NHEADS = 512, NWG = 256;
constexpr float CEXP = 0.08838834764831845f * 1.4426950408889634f;
constexpr int PFK = 4, PFV = 3;

DI unsigned cvtpk(float lo, float hi) { f32x2 v = {lo, hi}; bf16x2 b = __builtin_convertvector(v, bf16x2); return __builtin_bit_cast(unsigned, b); }
DI float fmax2(float a, float b) { return __builtin_elementwise_maximum(a, b); }
DI float fmax3(float a, float b, float c) { return __builtin_elementwise_maximum(__builtin_elementwise_maximum(a, b), c); }
DI unsigned opaque(unsigned x) { asm volatile("" : "+v"(x)); return x; }

struct Stamps { int dummy; };
#define ST(i) ((void)0)

DI void dma2(unsigned m0v, unsigned v0, unsigned v1, __amdgpu_buffer_rsrc_t srd, unsigned soff) {
  asm volatile("s_mov_b32 m0, %0\n\ts_nop 0\n\t"
               "buffer_load_dwordx4 %1, %3, %4 offen" LDPOL " lds\n\t"
               "buffer_load_dwordx4 %2, %3, %4 offen offset:1024" LDPOL " lds"
               :: "s"(m0v), "v"(v0), "v"(v1), "s"(srd), "s"(soff) : "memory");
}
template <int N> DI void wait_bar() { asm volatile("s_waitcnt vmcnt(%0)\n\ts_barrier" :: "i"(N) : "memory"); }
template <int N> DI void wait_vm() { asm volatile("s_waitcnt vmcnt(%0)" :: "i"(N) : "memory"); }

struct Ctx {
  __amdgpu_buffer_rsrc_t sq, sk, sv, so;
  unsigned wid, lane;
  unsigned v0;
  unsigned hA, hB;
};
constexpr unsigned slot_of(int i) { return RING + 16384u * (unsigned)(i % 6); }

template <int I> DI void dma_ring(const Ctx& c) {
  const unsigned w2 = (32u * c.wid) & 48u;
  dma2(slot_of(I) + c.wid * 2048u, c.v0 ^ w2, c.v0 ^ (w2 + 16u), (I & 4) ? c.sv : c.sk, ((I & 8) ? c.hB : c.hA) + 16384u * (I & 3) + c.wid * 2048u);
}
template <int T> DI void dma_q(const Ctx& c, unsigned hoff) {
  const unsigned o = c.wid * 8192u + 2048u * T;
  dma2(RQO + o, c.v0 ^ (16u * ((2 * T) & 3)), c.v0 ^ (16u * ((2 * T + 1) & 3)), c.sq, hoff + o);
}
DI unsigned row_base(unsigned lane) { const unsigned l32 = lane & 31u, h = lane >> 5, f = ((l32 & 3u) << 2) | ((l32 >> 2) & 3u); return l32 * 256u + 16u * (f ^ h); }
DI void read_q(const Ctx& c, bf16x8 (&q)[8]) {
  const unsigned b = row_base(opaque(c.lane)) + RQO + c.wid * 8192u;
#pragma unroll
  for (int d0 = 0; d0 < 8; ++d0) q[d0] = *(lds_b8p)(b ^ (32u * d0));
}
template <int I0, int I1> DI void store_o(const Ctx& c, unsigned hoff) {
  const unsigned lane = opaque(c.lane), rr = lane >> 4, cc = lane & 15u;
  const unsigned b0 = RQO + c.wid * 8192u + rr * 256u + 16u * (cc ^ rr), b1 = b0 ^ 64u;
  u32x4 w[8];
#pragma unroll
  for (int i = I0; i < I1; ++i) w[i] = *(lds_u4p)(((i & 1) ? b1 : b0) + 1024u * i);
#pragma unroll
  for (int i = I0; i < I1; ++i) __builtin_amdgcn_raw_buffer_store_b128(w[i], c.so, lane * 16u + 1024u * i, hoff + c.wid * 8192u, STAUX);
}

struct Hd {
  f32x16 S[8], o[4];
  bf16x8 kf[64];
  s16x4 vlo[64], vhi[64];
  u32x4 P[2];
  unsigned ka[8], va[8];
  float mx[2], l[2], mc;
};

template <int H, int G> DI void events(const Ctx& c) {
  constexpr int R = 8 * H;
  if constexpr ((G + BOFF) % 16 == 0 && G + BOFF >= 16 && G + BOFF <= 112) {
    constexpr int j = (G + BOFF) / 16;
    constexpr int pos[12] = {-1, 14, 28, 42, 56, 67, 77, 87, 97, 107, 117, 127};
    constexpr int ej = j - 2;
    int n = 0;
    for (int e = ej + 1; e < 12; ++e) if (pos[e] < G) n += (H == 1 && e >= 10) ? 0 : 2;
    constexpr int N = []() constexpr { int n = 0; for (int e = ej + 1; e < 12; ++e) if (pos[e] < G) n += (H == 1 && e >= 10) ? 0 : 2; return n; }();
    (void)n;
    wait_bar<N>();
  }
  if constexpr (G == 14) dma_ring<R + 3>(c);
  if constexpr (G == 28) dma_ring<R + 4>(c);
  if constexpr (G == 42) dma_ring<R + 5>(c);
  if constexpr (G == 56) dma_ring<R + 6>(c);
  if constexpr (G == 67) dma_ring<R + 7>(c);
  if constexpr (H == 0) {
    if constexpr (G == 77) dma_q<0>(c, c.hB);
    if constexpr (G == 87) dma_q<1>(c, c.hB);
    if constexpr (G == 97) dma_q<2>(c, c.hB);
    if constexpr (G == 107) dma_q<3>(c, c.hB);
    if constexpr (G == 117) dma_ring<8>(c);
    if constexpr (G == 127) dma_ring<9>(c);
  } else {
    if constexpr (G == 77) store_o<0, 2>(c, c.hA);
    if constexpr (G == 87) store_o<2, 4>(c, c.hA);
    if constexpr (G == 97) store_o<4, 6>(c, c.hA);
    if constexpr (G == 107) store_o<6, 8>(c, c.hA);
  }
}
template <int H, int G> DI void k_read(Hd& s) {
  constexpr int I = 8 * H + (G >> 4);
  s.kf[G] = *(lds_b8p)(s.ka[G & 7] + 16384u * ((I % 6) % 3) + 8192u * ((G >> 3) & 1));
}
template <int H, int GP> DI void v_read(Hd& s) {
  constexpr int I = 8 * H + 4 + (GP >> 4), ks = (GP >> 2) & 3, db = GP & 3;
  s.vlo[GP] = __builtin_amdgcn_ds_read_tr16_b64_v4i16((lds_s4p)(s.va[2 * db] + 16384u * ((I % 6) % 3) + 4096u * ks));
  s.vhi[GP] = __builtin_amdgcn_ds_read_tr16_b64_v4i16((lds_s4p)(s.va[2 * db + 1] + 16384u * ((I % 6) % 3) + 4096u * ks + 2048u));
}
template <int IPREV, int INEXT> DI void rebase(unsigned (&a)[8]) {
  if constexpr ((IPREV % 6) / 3 != (INEXT % 6) / 3) {
#pragma unroll
    for (int i = 0; i < 8; ++i) a[i] = opaque((INEXT % 6) >= 3 ? a[i] + 49152u : a[i] - 49152u);
  }
}
DI void sm_pair(Hd& s, int ks, int pr) {
  const int kb = ks >> 1, r = 8 * (ks & 1) + 2 * pr;
  const float e0 = __builtin_amdgcn_exp2f(__builtin_fmaf(s.S[kb][r], CEXP, s.mc));
  const float e1 = __builtin_amdgcn_exp2f(__builtin_fmaf(s.S[kb][r + 1], CEXP, s.mc));
  s.l[0] += e0; s.l[1] += e1;
  unsigned w = cvtpk(e0, e1);
  PIN(s.l[0]); PIN(s.l[1]); PIN(w);
  s.P[ks & 1][pr] = w;
}
DI void va_init(const Ctx& c, Hd& s, unsigned base) {
  const unsigned lane = opaque(c.lane), h = lane >> 5, blk = (lane >> 4) & 1u, q4 = (lane & 15u) >> 2, p = lane & 3u;
  const unsigned vb = 256u * (4u * h + q4) + 16u * (4u * q4 + ((2u * blk + (p >> 1)) ^ h)) + 8u * (p & 1u);
#pragma unroll
  for (int i = 0; i < 8; ++i) s.va[i] = opaque((vb ^ (64u * (i >> 1)) ^ (32u * (i & 1))) + base);
}

template <int H, int G> DI void gap(const Ctx& c, Hd& s, const bf16x8 (&q)[8], Stamps& st) {
  constexpr int R = 8 * H;
  events<H, G>(c);
  if constexpr (G < 64) {
    if constexpr (G + PFK < 64) {
      if constexpr (((G + PFK) & 15) == 0) rebase<R + ((G + PFK) >> 4) - 1, R + ((G + PFK) >> 4)>(s.ka);
      k_read<H, G + PFK>(s);
    }
    if constexpr (G == 60) va_init(c, s, RING + (((R + 4) % 6) >= 3 ? 49152u : 0u));
    if constexpr (G + PFV >= 64) v_read<H, G + PFV - 64>(s);
    constexpr int kb = G >> 3, d0 = G & 7;
    if constexpr (d0 == 0) s.S[kb] = __builtin_amdgcn_mfma_f32_32x32x16_bf16(s.kf[G], q[d0], f32x16{}, 0, 0, 0);
    else                   s.S[kb] = __builtin_amdgcn_mfma_f32_32x32x16_bf16(s.kf[G], q[d0], s.S[kb], 0, 0, 0);
    PIN(s.S[kb]);
    if constexpr (kb >= 1 && d0 >= 2 && d0 < 6) {
      constexpr int pb = kb - 1, r = 4 * (d0 - 2);
      if constexpr (pb == 0 && d0 == 2) { s.mx[0] = fmax2(s.S[pb][r], s.S[pb][r + 1]); s.mx[1] = fmax2(s.S[pb][r + 2], s.S[pb][r + 3]); }
      else { s.mx[0] = fmax3(s.mx[0], s.S[pb][r], s.S[pb][r + 1]); s.mx[1] = fmax3(s.mx[1], s.S[pb][r + 2], s.S[pb][r + 3]); }
      PIN(s.mx[0]); PIN(s.mx[1]);
    }
    if constexpr (G == 63) {
#pragma unroll
      for (int r = 0; r < 16; r += 4) { s.mx[0] = fmax3(s.mx[0], s.S[7][r], s.S[7][r + 1]); s.mx[1] = fmax3(s.mx[1], s.S[7][r + 2], s.S[7][r + 3]); }
      float m = fmax2(s.mx[0], s.mx[1]);
      auto rr = __builtin_amdgcn_permlane32_swap(__float_as_uint(m), __float_as_uint(m), false, false);
      m = fmax2(__uint_as_float(rr[0]), __uint_as_float(rr[1]));
      s.mc = -m * CEXP;
      s.l[0] = 0.f; s.l[1] = 0.f;
#pragma unroll
      for (int pr = 0; pr < 4; ++pr) sm_pair(s, 0, pr);
    }
  } else {
    constexpr int GP = G - 64, ks = GP >> 2, db = GP & 3;
    if constexpr (GP + PFV < 64) {
      if constexpr (((GP + PFV) & 15) == 0) rebase<R + 4 + ((GP + PFV) >> 4) - 1, R + 4 + ((GP + PFV) >> 4)>(s.va);
      v_read<H, GP + PFV>(s);
    }
    const s16x8 vf = __builtin_shufflevector(s.vlo[GP], s.vhi[GP], 0, 1, 2, 3, 4, 5, 6, 7);
    if constexpr (ks == 0) s.o[db] = __builtin_amdgcn_mfma_f32_32x32x16_bf16(__builtin_bit_cast(bf16x8, vf), __builtin_bit_cast(bf16x8, s.P[0]), f32x16{}, 0, 0, 0);
    else                   s.o[db] = __builtin_amdgcn_mfma_f32_32x32x16_bf16(__builtin_bit_cast(bf16x8, vf), __builtin_bit_cast(bf16x8, s.P[ks & 1]), s.o[db], 0, 0, 0);
    PIN(s.o[db]);
    if constexpr (ks + 1 < 16) sm_pair(s, ks + 1, db);
  }
  GAP();
}
template <int H, int... Gs> DI void gaps(const Ctx& c, Hd& s, const bf16x8 (&q)[8], Stamps& st, std::integer_sequence<int, Gs...>) { (gap<H, Gs>(c, s, q, st), ...); }

template <int H>
DI void head(const Ctx& c, bf16x8 (&q)[8], Stamps& st) {
  constexpr int R = 8 * H;
  Hd s;
  { const unsigned ra = row_base(opaque(c.lane));
#pragma unroll
    for (int d0 = 0; d0 < 8; ++d0) s.ka[d0] = opaque((ra ^ (32u * d0)) + RING + ((R % 6) >= 3 ? 49152u : 0u)); }
  dma_ring<R + 2>(c);
  GAP();
  wait_bar<4>();
  GAP();
  k_read<H, 0>(s); k_read<H, 1>(s); k_read<H, 2>(s); k_read<H, 3>(s);
  GAP();
  gaps<H>(c, s, q, st, std::make_integer_sequence<int, 128>{});
  ST(1 + 9 * H + 8 + (H ? 1 : 0));
  float ls = s.l[0] + s.l[1];
  { auto rr = __builtin_amdgcn_permlane32_swap(__float_as_uint(ls), __float_as_uint(ls), false, false);
    ls = __uint_as_float(rr[0]) + __uint_as_float(rr[1]); }
  const float il = __builtin_amdgcn_rcpf(ls);
  u32x2 w[16];
#pragma unroll
  for (int db = 0; db < 4; ++db)
#pragma unroll
    for (int g = 0; g < 4; ++g)
      w[4 * db + g] = u32x2{cvtpk(s.o[db][4 * g] * il, s.o[db][4 * g + 1] * il), cvtpk(s.o[db][4 * g + 2] * il, s.o[db][4 * g + 3] * il)};
  GAP();
  if constexpr (H == 0) {
    wait_vm<4>();
    GAP();
    read_q(c, q);
    GAP(); asm volatile("s_waitcnt lgkmcnt(0)" ::: "memory"); GAP();
  }
  {
    const unsigned lane = opaque(c.lane), l32 = lane & 31u, h = lane >> 5;
    const unsigned ow = RQO + c.wid * 8192u + l32 * 256u + 16u * (l32 & 7u) + 8u * h;
#pragma unroll
    for (int i = 0; i < 16; ++i) *(lds_u2p)(ow ^ (16u * i)) = w[i];
  }
  if constexpr (H == 1) store_o<0, 8>(c, c.hB);
  GAP();
}

__global__ __launch_bounds__(512, 2) void attn_fwd(const void* __restrict__ Q, const void* __restrict__ K, const void* __restrict__ V, void* __restrict__ O, unsigned* __restrict__ dbg) {
  Stamps st;
  ST(0);
  Ctx c;
  const unsigned tid = threadIdx.x, lane = tid & 63u;
  c.lane = lane;
  c.wid = __builtin_amdgcn_readfirstlane(tid >> 6);
  const int nbytes = NHEADS * (int)HEADB;
  c.sq = __builtin_amdgcn_make_buffer_rsrc((void*)Q, (short)0, nbytes, 0x00020000);
  c.sk = __builtin_amdgcn_make_buffer_rsrc((void*)K, (short)0, nbytes, 0x00020000);
  c.sv = __builtin_amdgcn_make_buffer_rsrc((void*)V, (short)0, nbytes, 0x00020000);
  c.so = __builtin_amdgcn_make_buffer_rsrc((void*)O, (short)0, nbytes, 0x00020000);
  {
    const unsigned r = lane >> 4, p = lane & 15u;
    c.v0 = r * 256u + 16u * (p ^ (r << 2));
  }
  c.hA = blockIdx.x * HEADB; c.hB = (blockIdx.x + NWG) * HEADB;
  asm volatile("s_nop 4");
  int nrep = NREP; asm volatile("" : "+s"(nrep));
#pragma nounroll
  for (int rep = 0; rep < nrep; ++rep) {
  dma_q<0>(c, c.hA); dma_q<1>(c, c.hA); dma_q<2>(c, c.hA); dma_q<3>(c, c.hA);
  dma_ring<0>(c); dma_ring<1>(c);
  bf16x8 q[8];
  GAP();
  wait_vm<4>();
  GAP();
  ST(1);
  read_q(c, q);
  head<0>(c, q, st);
  head<1>(c, q, st);
  asm volatile("s_waitcnt vmcnt(0) lgkmcnt(0)\n\ts_barrier" ::: "memory");
  }
}

extern "C" void kernel_launch(void* const* d_in, const int* in_sizes, int n_in, void* d_out, int out_size, void* d_ws, size_t ws_size, hipStream_t stream) {
  (void)in_sizes; (void)n_in; (void)out_size; (void)ws_size;
  (void)hipFuncSetAttribute((const void*)attn_fwd, hipFuncAttributeMaxDynamicSharedMemorySize, (int)LDS_BYTES);
  hipLaunchKernelGGL(attn_fwd, dim3(NWG), dim3(512), LDS_BYTES, stream, d_in[0], d_in[1], d_in[2], d_out, (unsigned*)d_ws);
}
```

```cpp
#include <hip/hip_runtime.h>
#include <hip/hip_bf16.h>
#include <utility>
#define STAMPS
#define RSTAMP
#define SIDE 0
#define ONLYWG 7

typedef float    f32x16 __attribute__((ext_vector_type(16)));
typedef float    f32x2  __attribute__((ext_vector_type(2)));
typedef __bf16   bf16x8 __attribute__((ext_vector_type(8)));
typedef __bf16   bf16x2 __attribute__((ext_vector_type(2)));
typedef short    s16x4  __attribute__((ext_vector_type(4)));
typedef short    s16x8  __attribute__((ext_vector_type(8)));
typedef unsigned u32x4  __attribute__((ext_vector_type(4)));
typedef unsigned u32x2  __attribute__((ext_vector_type(2)));
typedef __attribute__((address_space(3))) const bf16x8* lds_b8p;
typedef __attribute__((address_space(3))) s16x4* lds_s4p;
typedef __attribute__((address_space(3))) u32x2* lds_u2p;
typedef __attribute__((address_space(3))) const u32x4* lds_u4p;

#define DI __device__ __forceinline__
#define GAP() __builtin_amdgcn_sched_barrier(0)
#define PIN(x) asm volatile("" : "+v"(x))
#define LDPOL " nt"
#define STAUX 16
#define BOFF 8

constexpr unsigned HEADB = 65536u;
constexpr unsigned RQO = 0u, RING = 65536u, LDS_BYTES = 163840u;
constexpr int NHEADS = 512, NWG = 256;
constexpr float CEXP = 0.08838834764831845f * 1.4426950408889634f;
constexpr int PFK = 4, PFV = 3;

DI unsigned cvtpk(float lo, float hi) { f32x2 v = {lo, hi}; bf16x2 b = __builtin_convertvector(v, bf16x2); return __builtin_bit_cast(unsigned, b); }
DI float fmax2(float a, float b) { return __builtin_elementwise_maximum(a, b); }
DI float fmax3(float a, float b, float c) { return __builtin_elementwise_maximum(__builtin_elementwise_maximum(a, b), c); }
DI unsigned opaque(unsigned x) { asm volatile("" : "+v"(x)); return x; }

#define NST 40
struct Stamps { unsigned t[NST]; };
DI unsigned stamp_now() { unsigned long long t; GAP();
  asm volatile("s_memrealtime %0\n\ts_waitcnt lgkmcnt(0)" : "=s"(t) :: "memory");
  GAP(); return (unsigned)t; }
#define ST(i) (st.t[i] = stamp_now())

DI void dma2(unsigned m0v, unsigned v0, unsigned v1, __amdgpu_buffer_rsrc_t srd, unsigned soff) {
  asm volatile("s_mov_b32 m0, %0\n\ts_nop 0\n\t"
               "buffer_load_dwordx4 %1, %3, %4 offen" LDPOL " lds\n\t"
               "buffer_load_dwordx4 %2, %3, %4 offen offset:1024" LDPOL " lds"
               :: "s"(m0v), "v"(v0), "v"(v1), "s"(srd), "s"(soff) : "memory");
}
template <int N> DI void wait_bar() { asm volatile("s_waitcnt vmcnt(%0)\n\ts_barrier" :: "i"(N) : "memory"); }
template <int N> DI void wait_vm() { asm volatile("s_waitcnt vmcnt(%0)" :: "i"(N) : "memory"); }

struct Ctx {
  __amdgpu_buffer_rsrc_t sq, sk, sv, so;
  unsigned wid, lane;
  unsigned v0;
  unsigned hA, hB;
};
constexpr unsigned slot_of(int i) { return RING + 16384u * (unsigned)(i % 6); }

template <int I> DI void dma_ring(const Ctx& c) {
  const unsigned w2 = (32u * c.wid) & 48u;
  dma2(slot_of(I) + c.wid * 2048u, c.v0 ^ w2, c.v0 ^ (w2 + 16u), (I & 4) ? c.sv : c.sk, ((I & 8) ? c.hB : c.hA) + 16384u * (I & 3) + c.wid * 2048u);
}
template <int T> DI void dma_q(const Ctx& c, unsigned hoff) {
  const unsigned o = c.wid * 8192u + 2048u * T;
  dma2(RQO + o, c.v0 ^ (16u * ((2 * T) & 3)), c.v0 ^ (16u * ((2 * T + 1) & 3)), c.sq, hoff + o);
}
DI unsigned row_base(unsigned lane) { const unsigned l32 = lane & 31u, h = lane >> 5, f = ((l32 & 3u) << 2) | ((l32 >> 2) & 3u); return l32 * 256u + 16u * (f ^ h); }
DI void read_q(const Ctx& c, bf16x8 (&q)[8]) {
  const unsigned b = row_base(opaque(c.lane)) + RQO + c.wid * 8192u;
#pragma unroll
  for (int d0 = 0; d0 < 8; ++d0) q[d0] = *(lds_b8p)(b ^ (32u * d0));
}
template <int I0, int I1> DI void store_o(const Ctx& c, unsigned hoff) {
  const unsigned lane = opaque(c.lane), rr = lane >> 4, cc = lane & 15u;
  const unsigned b0 = RQO + c.wid * 8192u + rr * 256u + 16u * (cc ^ rr), b1 = b0 ^ 64u;
  u32x4 w[8];
#pragma unroll
  for (int i = I0; i < I1; ++i) w[i] = *(lds_u4p)(((i & 1) ? b1 : b0) + 1024u * i);
#pragma unroll
  for (int i = I0; i < I1; ++i) __builtin_amdgcn_raw_buffer_store_b128(w[i], c.so, lane * 16u + 1024u * i, hoff + c.wid * 8192u, STAUX);
}

struct Hd {
  f32x16 S[8], o[4];
  bf16x8 kf[64];
  s16x4 vlo[64], vhi[64];
  u32x4 P[2];
  unsigned ka[8], va[8];
  float mx[2], l[2], mc;
};

template <int H, int G> DI void events(const Ctx& c) {
  constexpr int R = 8 * H;
  if constexpr ((G + BOFF) % 16 == 0 && G + BOFF >= 16 && G + BOFF <= 112) {
    constexpr int j = (G + BOFF) / 16;
    constexpr int pos[12] = {-1, 14, 28, 42, 56, 67, 77, 87, 97, 107, 117, 127};
    constexpr int ej = j - 2;
    int n = 0;
    for (int e = ej + 1; e < 12; ++e) if (pos[e] < G) n += (H == 1 && e >= 10) ? 0 : 2;
    constexpr int N = []() constexpr { int n = 0; for (int e = ej + 1; e < 12; ++e) if (pos[e] < G) n += (H == 1 && e >= 10) ? 0 : 2; return n; }();
    (void)n;
    wait_bar<N>();
  }
  if constexpr (G == 14) dma_ring<R + 3>(c);
  if constexpr (G == 28) dma_ring<R + 4>(c);
  if constexpr (G == 42) dma_ring<R + 5>(c);
  if constexpr (G == 56) dma_ring<R + 6>(c);
  if constexpr (G == 67) dma_ring<R + 7>(c);
  if constexpr (H == 0) {
    if constexpr (G == 77) dma_q<0>(c, c.hB);
    if constexpr (G == 87) dma_q<1>(c, c.hB);
    if constexpr (G == 97) dma_q<2>(c, c.hB);
    if constexpr (G == 107) dma_q<3>(c, c.hB);
    if constexpr (G == 117) dma_ring<8>(c);
    if constexpr (G == 127) dma_ring<9>(c);
  } else {
    if constexpr (G == 77) store_o<0, 2>(c, c.hA);
    if constexpr (G == 87) store_o<2, 4>(c, c.hA);
    if constexpr (G == 97) store_o<4, 6>(c, c.hA);
    if constexpr (G == 107) store_o<6, 8>(c, c.hA);
  }
}
template <int H, int G> DI void k_read(Hd& s) {
  constexpr int I = 8 * H + (G >> 4);
  s.kf[G] = *(lds_b8p)(s.ka[G & 7] + 16384u * ((I % 6) % 3) + 8192u * ((G >> 3) & 1));
}
template <int H, int GP> DI void v_read(Hd& s) {
  constexpr int I = 8 * H + 4 + (GP >> 4), ks = (GP >> 2) & 3, db = GP & 3;
  s.vlo[GP] = __builtin_amdgcn_ds_read_tr16_b64_v4i16((lds_s4p)(s.va[2 * db] + 16384u * ((I % 6) % 3) + 4096u * ks));
  s.vhi[GP] = __builtin_amdgcn_ds_read_tr16_b64_v4i16((lds_s4p)(s.va[2 * db + 1] + 16384u * ((I % 6) % 3) + 4096u * ks + 2048u));
}
template <int IPREV, int INEXT> DI void rebase(unsigned (&a)[8]) {
  if constexpr ((IPREV % 6) / 3 != (INEXT % 6) / 3) {
#pragma unroll
    for (int i = 0; i < 8; ++i) a[i] = opaque((INEXT % 6) >= 3 ? a[i] + 49152u : a[i] - 49152u);
  }
}
DI void sm_pair(Hd& s, int ks, int pr) {
  const int kb = ks >> 1, r = 8 * (ks & 1) + 2 * pr;
  const float e0 = __builtin_amdgcn_exp2f(__builtin_fmaf(s.S[kb][r], CEXP, s.mc));
  const float e1 = __builtin_amdgcn_exp2f(__builtin_fmaf(s.S[kb][r + 1], CEXP, s.mc));
  s.l[0] += e0; s.l[1] += e1;
  unsigned w = cvtpk(e0, e1);
  PIN(s.l[0]); PIN(s.l[1]); PIN(w);
  s.P[ks & 1][pr] = w;
}
DI void va_init(const Ctx& c, Hd& s, unsigned base) {
  const unsigned lane = opaque(c.lane), h = lane >> 5, blk = (lane >> 4) & 1u, q4 = (lane & 15u) >> 2, p = lane & 3u;
  const unsigned vb = 256u * (4u * h + q4) + 16u * (4u * q4 + ((2u * blk + (p >> 1)) ^ h)) + 8u * (p & 1u);
#pragma unroll
  for (int i = 0; i < 8; ++i) s.va[i] = opaque((vb ^ (64u * (i >> 1)) ^ (32u * (i & 1))) + base);
}

template <int H, int G> DI void gap(const Ctx& c, Hd& s, const bf16x8 (&q)[8], Stamps& st) {
  constexpr int R = 8 * H;
  if constexpr (G % 16 == 0) ST(1 + 9 * H + G / 16 + (H ? 1 : 0));
  events<H, G>(c);
  if constexpr (G < 64) {
    if constexpr (G + PFK < 64) {
      if constexpr (((G + PFK) & 15) == 0) rebase<R + ((G + PFK) >> 4) - 1, R + ((G + PFK) >> 4)>(s.ka);
      k_read<H, G + PFK>(s);
    }
    if constexpr (G == 60) va_init(c, s, RING + (((R + 4) % 6) >= 3 ? 49152u : 0u));
    if constexpr (G + PFV >= 64) v_read<H, G + PFV - 64>(s);
    constexpr int kb = G >> 3, d0 = G & 7;
    if constexpr (d0 == 0) s.S[kb] = __builtin_amdgcn_mfma_f32_32x32x16_bf16(s.kf[G], q[d0], f32x16{}, 0, 0, 0);
    else                   s.S[kb] = __builtin_amdgcn_mfma_f32_32x32x16_bf16(s.kf[G], q[d0], s.S[kb], 0, 0, 0);
    PIN(s.S[kb]);
    if constexpr (kb >= 1 && d0 >= 2 && d0 < 6) {
      constexpr int pb = kb - 1, r = 4 * (d0 - 2);
      if constexpr (pb == 0 && d0 == 2) { s.mx[0] = fmax2(s.S[pb][r], s.S[pb][r + 1]); s.mx[1] = fmax2(s.S[pb][r + 2], s.S[pb][r + 3]); }
      else { s.mx[0] = fmax3(s.mx[0], s.S[pb][r], s.S[pb][r + 1]); s.mx[1] = fmax3(s.mx[1], s.S[pb][r + 2], s.S[pb][r + 3]); }
      PIN(s.mx[0]); PIN(s.mx[1]);
    }
    if constexpr (G == 63) {
#pragma unroll
      for (int r = 0; r < 16; r += 4) { s.mx[0] = fmax3(s.mx[0], s.S[7][r], s.S[7][r + 1]); s.mx[1] = fmax3(s.mx[1], s.S[7][r + 2], s.S[7][r + 3]); }
      float m = fmax2(s.mx[0], s.mx[1]);
      auto rr = __builtin_amdgcn_permlane32_swap(__float_as_uint(m), __float_as_uint(m), false, false);
      m = fmax2(__uint_as_float(rr[0]), __uint_as_float(rr[1]));
      s.mc = -m * CEXP;
      s.l[0] = 0.f; s.l[1] = 0.f;
#pragma unroll
      for (int pr = 0; pr < 4; ++pr) sm_pair(s, 0, pr);
    }
  } else {
    constexpr int GP = G - 64, ks = GP >> 2, db = GP & 3;
    if constexpr (GP + PFV < 64) {
      if constexpr (((GP + PFV) & 15) == 0) rebase<R + 4 + ((GP + PFV) >> 4) - 1, R + 4 + ((GP + PFV) >> 4)>(s.va);
      v_read<H, GP + PFV>(s);
    }
    const s16x8 vf = __builtin_shufflevector(s.vlo[GP], s.vhi[GP], 0, 1, 2, 3, 4, 5, 6, 7);
    if constexpr (ks == 0) s.o[db] = __builtin_amdgcn_mfma_f32_32x32x16_bf16(__builtin_bit_cast(bf16x8, vf), __builtin_bit_cast(bf16x8, s.P[0]), f32x16{}, 0, 0, 0);
    else                   s.o[db] = __builtin_amdgcn_mfma_f32_32x32x16_bf16(__builtin_bit_cast(bf16x8, vf), __builtin_bit_cast(bf16x8, s.P[ks & 1]), s.o[db], 0, 0, 0);
    PIN(s.o[db]);
    if constexpr (ks + 1 < 16) sm_pair(s, ks + 1, db);
  }
  GAP();
}
template <int H, int... Gs> DI void gaps(const Ctx& c, Hd& s, const bf16x8 (&q)[8], Stamps& st, std::integer_sequence<int, Gs...>) { (gap<H, Gs>(c, s, q, st), ...); }

template <int H>
DI void head(const Ctx& c, bf16x8 (&q)[8], Stamps& st) {
  constexpr int R = 8 * H;
  Hd s;
  { const unsigned ra = row_base(opaque(c.lane));
#pragma unroll
    for (int d0 = 0; d0 < 8; ++d0) s.ka[d0] = opaque((ra ^ (32u * d0)) + RING + ((R % 6) >= 3 ? 49152u : 0u)); }
  dma_ring<R + 2>(c);
  GAP();
  wait_bar<4>();
  GAP();
  k_read<H, 0>(s); k_read<H, 1>(s); k_read<H, 2>(s); k_read<H, 3>(s);
  GAP();
  gaps<H>(c, s, q, st, std::make_integer_sequence<int, 128>{});
  ST(1 + 9 * H + 8 + (H ? 1 : 0));
  float ls = s.l[0] + s.l[1];
  { auto rr = __builtin_amdgcn_permlane32_swap(__float_as_uint(ls), __float_as_uint(ls), false, false);
    ls = __uint_as_float(rr[0]) + __uint_as_float(rr[1]); }
  const float il = __builtin_amdgcn_rcpf(ls);
  u32x2 w[16];
#pragma unroll
  for (int db = 0; db < 4; ++db)
#pragma unroll
    for (int g = 0; g < 4; ++g)
      w[4 * db + g] = u32x2{cvtpk(s.o[db][4 * g] * il, s.o[db][4 * g + 1] * il), cvtpk(s.o[db][4 * g + 2] * il, s.o[db][4 * g + 3] * il)};
  GAP();
  if constexpr (H == 0) {
    wait_vm<4>();
    GAP();
    read_q(c, q);
    GAP(); asm volatile("s_waitcnt lgkmcnt(0)" ::: "memory"); GAP();
  }
  {
    const unsigned lane = opaque(c.lane), l32 = lane & 31u, h = lane >> 5;
    const unsigned ow = RQO + c.wid * 8192u + l32 * 256u + 16u * (l32 & 7u) + 8u * h;
#pragma unroll
    for (int i = 0; i < 16; ++i) *(lds_u2p)(ow ^ (16u * i)) = w[i];
  }
  if constexpr (H == 1) store_o<0, 8>(c, c.hB);
  GAP();
}

__global__ __launch_bounds__(512, 2) void attn_fwd(const void* __restrict__ Q, const void* __restrict__ K, const void* __restrict__ V, void* __restrict__ O, unsigned* __restrict__ dbg) {
  Stamps st;
  unsigned long long rt0; asm volatile("s_memrealtime %0\n\ts_waitcnt lgkmcnt(0)" : "=s"(rt0) :: "memory");
  for (int i = 0; i < NST; ++i) st.t[i] = 0;
  ST(0);
  Ctx c;
  const unsigned tid = threadIdx.x, lane = tid & 63u;
  c.lane = lane;
  c.wid = __builtin_amdgcn_readfirstlane(tid >> 6);
  const int nbytes = NHEADS * (int)HEADB;
  c.sq = __builtin_amdgcn_make_buffer_rsrc((void*)Q, (short)0, nbytes, 0x00020000);
  c.sk = __builtin_amdgcn_make_buffer_rsrc((void*)K, (short)0, nbytes, 0x00020000);
  c.sv = __builtin_amdgcn_make_buffer_rsrc((void*)V, (short)0, nbytes, 0x00020000);
  c.so = __builtin_amdgcn_make_buffer_rsrc((void*)O, (short)0, nbytes, 0x00020000);
  {
    const unsigned r = lane >> 4, p = lane & 15u;
    c.v0 = r * 256u + 16u * (p ^ (r << 2));
  }
  c.hA = blockIdx.x * HEADB; c.hB = (blockIdx.x + NWG) * HEADB;
  asm volatile("s_nop 4");
  dma_q<0>(c, c.hA); dma_q<1>(c, c.hA); dma_q<2>(c, c.hA); dma_q<3>(c, c.hA);
  dma_ring<0>(c); dma_ring<1>(c);
  bf16x8 q[8];
  GAP();
  wait_vm<4>();
  GAP();
  ST(1);
  read_q(c, q);
  head<0>(c, q, st);
  head<1>(c, q, st);
  ST(21);
  asm volatile("s_waitcnt vmcnt(0)" ::: "memory");
  ST(22);
  unsigned long long rt1; asm volatile("s_memrealtime %0\n\ts_waitcnt lgkmcnt(0)" : "=s"(rt1) :: "memory");
  st.t[38] = (unsigned)rt0; st.t[39] = (unsigned)rt1;
  { const unsigned target = 6000u + (SIDE == 0 ? 0u : st.t[SIDE] - (unsigned)rt0);
    if (blockIdx.x == ONLYWG)
    for (;;) { unsigned long long now; asm volatile("s_memrealtime %0\n\ts_waitcnt lgkmcnt(0)" : "=s"(now) :: "memory"); if ((unsigned)now - (unsigned)rt0 >= target) break; __builtin_amdgcn_s_sleep(2); } }
  if (false) {
#pragma unroll
    for (int i = 0; i < NST; ++i) dbg[(blockIdx.x * 8 + c.wid) * NST + i] = st.t[i];
  }
}

extern "C" void kernel_launch(void* const* d_in, const int* in_sizes, int n_in, void* d_out, int out_size, void* d_ws, size_t ws_size, hipStream_t stream) {
  (void)in_sizes; (void)n_in; (void)out_size; (void)ws_size;
  (void)hipFuncSetAttribute((const void*)attn_fwd, hipFuncAttributeMaxDynamicSharedMemorySize, (int)LDS_BYTES);
  hipLaunchKernelGGL(attn_fwd, dim3(NWG), dim3(512), LDS_BYTES, stream, d_in[0], d_in[1], d_in[2], d_out, (unsigned*)d_ws);
}
```

```cpp
#include <hip/hip_runtime.h>
#include <hip/hip_bf16.h>
#include <utility>

typedef float    f32x16 __attribute__((ext_vector_type(16)));
typedef float    f32x2  __attribute__((ext_vector_type(2)));
typedef __bf16   bf16x8 __attribute__((ext_vector_type(8)));
typedef __bf16   bf16x2 __attribute__((ext_vector_type(2)));
typedef short    s16x4  __attribute__((ext_vector_type(4)));
typedef short    s16x8  __attribute__((ext_vector_type(8)));
typedef unsigned u32x4  __attribute__((ext_vector_type(4)));
typedef unsigned u32x2  __attribute__((ext_vector_type(2)));
typedef __attribute__((address_space(3))) const bf16x8* lds_b8p;
typedef __attribute__((address_space(3))) s16x4* lds_s4p;
typedef __attribute__((address_space(3))) u32x2* lds_u2p;
typedef __attribute__((address_space(3))) const u32x4* lds_u4p;

#define DI __device__ __forceinline__
#define GAP() __builtin_amdgcn_sched_barrier(0)
#define PIN(x) asm volatile("" : "+v"(x))
#define LDPOL " nt"
#define STAUX 18
#define BOFF 8

constexpr unsigned HEADB = 65536u;
constexpr unsigned RQO = 0u, RING = 65536u, LDS_BYTES = 163840u;
constexpr int NHEADS = 512, NWG = 256;
constexpr float CEXP = 0.08838834764831845f * 1.4426950408889634f;
constexpr int PFK = 4, PFV = 3;

DI unsigned cvtpk(float lo, float hi) { f32x2 v = {lo, hi}; bf16x2 b = __builtin_convertvector(v, bf16x2); return __builtin_bit_cast(unsigned, b); }
DI float fmax2(float a, float b) { return __builtin_elementwise_maximum(a, b); }
DI float fmax3(float a, float b, float c) { return __builtin_elementwise_maximum(__builtin_elementwise_maximum(a, b), c); }
DI unsigned opaque(unsigned x) { asm volatile("" : "+v"(x)); return x; }

struct Stamps { int dummy; };
#define ST(i) ((void)0)

DI void dma2(unsigned m0v, unsigned v0, unsigned v1, __amdgpu_buffer_rsrc_t srd, unsigned soff) {
  asm volatile("s_mov_b32 m0, %0\n\ts_nop 0\n\t"
               "buffer_load_dwordx4 %1, %3, %4 offen" LDPOL " lds\n\t"
               "buffer_load_dwordx4 %2, %3, %4 offen offset:1024" LDPOL " lds"
               :: "s"(m0v), "v"(v0), "v"(v1), "s"(srd), "s"(soff) : "memory");
}
template <int N> DI void wait_bar() { asm volatile("s_waitcnt vmcnt(%0)\n\ts_barrier" :: "i"(N) : "memory"); }
template <int N> DI void wait_vm() { asm volatile("s_waitcnt vmcnt(%0)" :: "i"(N) : "memory"); }

struct Ctx {
  __amdgpu_buffer_rsrc_t sq, sk, sv, so;
  unsigned wid, lane;
  unsigned v0;
  unsigned hA, hB;
};
constexpr unsigned slot_of(int i) { return RING + 16384u * (unsigned)(i % 6); }

template <int I> DI void dma_ring(const Ctx& c) {
  const unsigned w2 = (32u * c.wid) & 48u;
  dma2(slot_of(I) + c.wid * 2048u, c.v0 ^ w2, c.v0 ^ (w2 + 16u), (I & 4) ? c.sv : c.sk, ((I & 8) ? c.hB : c.hA) + 16384u * (I & 3) + c.wid * 2048u);
}
template <int T> DI void dma_q(const Ctx& c, unsigned hoff) {
  const unsigned o = c.wid * 8192u + 2048u * T;
  dma2(RQO + o, c.v0 ^ (16u * ((2 * T) & 3)), c.v0 ^ (16u * ((2 * T + 1) & 3)), c.sq, hoff + o);
}
DI unsigned row_base(unsigned lane) { const unsigned l32 = lane & 31u, h = lane >> 5, f = ((l32 & 3u) << 2) | ((l32 >> 2) & 3u); return l32 * 256u + 16u * (f ^ h); }
DI void read_q(const Ctx& c, bf16x8 (&q)[8]) {
  const unsigned b = row_base(opaque(c.lane)) + RQO + c.wid * 8192u;
#pragma unroll
  for (int d0 = 0; d0 < 8; ++d0) q[d0] = *(lds_b8p)(b ^ (32u * d0));
}
template <int I0, int I1> DI void store_o(const Ctx& c, unsigned hoff) {
  const unsigned lane = opaque(c.lane), rr = lane >> 4, cc = lane & 15u;
  const unsigned b0 = RQO + c.wid * 8192u + rr * 256u + 16u * (cc ^ rr), b1 = b0 ^ 64u;
  u32x4 w[8];
#pragma unroll
  for (int i = I0; i < I1; ++i) w[i] = *(lds_u4p)(((i & 1) ? b1 : b0) + 1024u * i);
#pragma unroll
  for (int i = I0; i < I1; ++i) __builtin_amdgcn_raw_buffer_store_b128(w[i], c.so, lane * 16u + 1024u * i, hoff + c.wid * 8192u, STAUX);
}

struct Hd {
  f32x16 S[8], o[4];
  bf16x8 kf[64];
  s16x4 vlo[64], vhi[64];
  u32x4 P[2];
  unsigned ka[8], va[8];
  float mx[2], l[2], mc;
};

template <int H, int G> DI void events(const Ctx& c) {
  constexpr int R = 8 * H;
  if constexpr ((G + BOFF) % 16 == 0 && G + BOFF >= 16 && G + BOFF <= 112) {
    constexpr int j = (G + BOFF) / 16;
    constexpr int pos[12] = {-1, 14, 28, 42, 56, 67, 77, 87, 97, 107, 117, 127};
    constexpr int ej = j - 2;
    int n = 0;
    for (int e = ej + 1; e < 12; ++e) if (pos[e] < G) n += (H == 1 && e >= 10) ? 0 : 2;
    constexpr int N = []() constexpr { int n = 0; for (int e = ej + 1; e < 12; ++e) if (pos[e] < G) n += (H == 1 && e >= 10) ? 0 : 2; return n; }();
    (void)n;
    wait_bar<N>();
  }
  if constexpr (G == 14) dma_ring<R + 3>(c);
  if constexpr (G == 28) dma_ring<R + 4>(c);
  if constexpr (G == 42) dma_ring<R + 5>(c);
  if constexpr (G == 56) dma_ring<R + 6>(c);
  if constexpr (G == 67) dma_ring<R + 7>(c);
  if constexpr (H == 0) {
    if constexpr (G == 77) dma_q<0>(c, c.hB);
    if constexpr (G == 87) dma_q<1>(c, c.hB);
    if constexpr (G == 97) dma_q<2>(c, c.hB);
    if constexpr (G == 107) dma_q<3>(c, c.hB);
    if constexpr (G == 117) dma_ring<8>(c);
    if constexpr (G == 127) dma_ring<9>(c);
  } else {
    if constexpr (G == 77) store_o<0, 2>(c, c.hA);
    if constexpr (G == 87) store_o<2, 4>(c, c.hA);
    if constexpr (G == 97) store_o<4, 6>(c, c.hA);
    if constexpr (G == 107) store_o<6, 8>(c, c.hA);
  }
}
template <int H, int G> DI void k_read(Hd& s) {
  constexpr int I = 8 * H + (G >> 4);
  s.kf[G] = *(lds_b8p)(s.ka[G & 7] + 16384u * ((I % 6) % 3) + 8192u * ((G >> 3) & 1));
}
template <int H, int GP> DI void v_read(Hd& s) {
  constexpr int I = 8 * H + 4 + (GP >> 4), ks = (GP >> 2) & 3, db = GP & 3;
  s.vlo[GP] = __builtin_amdgcn_ds_read_tr16_b64_v4i16((lds_s4p)(s.va[2 * db] + 16384u * ((I % 6) % 3) + 4096u * ks));
  s.vhi[GP] = __builtin_amdgcn_ds_read_tr16_b64_v4i16((lds_s4p)(s.va[2 * db + 1] + 16384u * ((I % 6) % 3) + 4096u * ks + 2048u));
}
template <int IPREV, int INEXT> DI void rebase(unsigned (&a)[8]) {
  if constexpr ((IPREV % 6) / 3 != (INEXT % 6) / 3) {
#pragma unroll
    for (int i = 0; i < 8; ++i) a[i] = opaque((INEXT % 6) >= 3 ? a[i] + 49152u : a[i] - 49152u);
  }
}
DI void sm_pair(Hd& s, int ks, int pr) {
  const int kb = ks >> 1, r = 8 * (ks & 1) + 2 * pr;
  const float e0 = __builtin_amdgcn_exp2f(__builtin_fmaf(s.S[kb][r], CEXP, s.mc));
  const float e1 = __builtin_amdgcn_exp2f(__builtin_fmaf(s.S[kb][r + 1], CEXP, s.mc));
  s.l[0] += e0; s.l[1] += e1;
  unsigned w = cvtpk(e0, e1);
  PIN(s.l[0]); PIN(s.l[1]); PIN(w);
  s.P[ks & 1][pr] = w;
}
DI void va_init(const Ctx& c, Hd& s, unsigned base) {
  const unsigned lane = opaque(c.lane), h = lane >> 5, blk = (lane >> 4) & 1u, q4 = (lane & 15u) >> 2, p = lane & 3u;
  const unsigned vb = 256u * (4u * h + q4) + 16u * (4u * q4 + ((2u * blk + (p >> 1)) ^ h)) + 8u * (p & 1u);
#pragma unroll
  for (int i = 0; i < 8; ++i) s.va[i] = opaque((vb ^ (64u * (i >> 1)) ^ (32u * (i & 1))) + base);
}

template <int H, int G> DI void gap(const Ctx& c, Hd& s, const bf16x8 (&q)[8], Stamps& st) {
  constexpr int R = 8 * H;
  events<H, G>(c);
  if constexpr (G < 64) {
    if constexpr (G + PFK < 64) {
      if constexpr (((G + PFK) & 15) == 0) rebase<R + ((G + PFK) >> 4) - 1, R + ((G + PFK) >> 4)>(s.ka);
      k_read<H, G + PFK>(s);
    }
    if constexpr (G == 60) va_init(c, s, RING + (((R + 4) % 6) >= 3 ? 49152u : 0u));
    if constexpr (G + PFV >= 64) v_read<H, G + PFV - 64>(s);
    constexpr int kb = G >> 3, d0 = G & 7;
    if constexpr (d0 == 0) s.S[kb] = __builtin_amdgcn_mfma_f32_32x32x16_bf16(s.kf[G], q[d0], f32x16{}, 0, 0, 0);
    else                   s.S[kb] = __builtin_amdgcn_mfma_f32_32x32x16_bf16(s.kf[G], q[d0], s.S[kb], 0, 0, 0);
    PIN(s.S[kb]);
    if constexpr (kb >= 1 && d0 >= 2 && d0 < 6) {
      constexpr int pb = kb - 1, r = 4 * (d0 - 2);
      if constexpr (pb == 0 && d0 == 2) { s.mx[0] = fmax2(s.S[pb][r], s.S[pb][r + 1]); s.mx[1] = fmax2(s.S[pb][r + 2], s.S[pb][r + 3]); }
      else { s.mx[0] = fmax3(s.mx[0], s.S[pb][r], s.S[pb][r + 1]); s.mx[1] = fmax3(s.mx[1], s.S[pb][r + 2], s.S[pb][r + 3]); }
      PIN(s.mx[0]); PIN(s.mx[1]);
    }
    if constexpr (G == 63) {
#pragma unroll
      for (int r = 0; r < 16; r += 4) { s.mx[0] = fmax3(s.mx[0], s.S[7][r], s.S[7][r + 1]); s.mx[1] = fmax3(s.mx[1], s.S[7][r + 2], s.S[7][r + 3]); }
      float m = fmax2(s.mx[0], s.mx[1]);
      auto rr = __builtin_amdgcn_permlane32_swap(__float_as_uint(m), __float_as_uint(m), false, false);
      m = fmax2(__uint_as_float(rr[0]), __uint_as_float(rr[1]));
      s.mc = -m * CEXP;
      s.l[0] = 0.f; s.l[1] = 0.f;
#pragma unroll
      for (int pr = 0; pr < 4; ++pr) sm_pair(s, 0, pr);
    }
  } else {
    constexpr int GP = G - 64, ks = GP >> 2, db = GP & 3;
    if constexpr (GP + PFV < 64) {
      if constexpr (((GP + PFV) & 15) == 0) rebase<R + 4 + ((GP + PFV) >> 4) - 1, R + 4 + ((GP + PFV) >> 4)>(s.va);
      v_read<H, GP + PFV>(s);
    }
    const s16x8 vf = __builtin_shufflevector(s.vlo[GP], s.vhi[GP], 0, 1, 2, 3, 4, 5, 6, 7);
    if constexpr (ks == 0) s.o[db] = __builtin_amdgcn_mfma_f32_32x32x16_bf16(__builtin_bit_cast(bf16x8, vf), __builtin_bit_cast(bf16x8, s.P[0]), f32x16{}, 0, 0, 0);
    else                   s.o[db] = __builtin_amdgcn_mfma_f32_32x32x16_bf16(__builtin_bit_cast(bf16x8, vf), __builtin_bit_cast(bf16x8, s.P[ks & 1]), s.o[db], 0, 0, 0);
    PIN(s.o[db]);
    if constexpr (ks + 1 < 16) sm_pair(s, ks + 1, db);
  }
  GAP();
}
template <int H, int... Gs> DI void gaps(const Ctx& c, Hd& s, const bf16x8 (&q)[8], Stamps& st, std::integer_sequence<int, Gs...>) { (gap<H, Gs>(c, s, q, st), ...); }

template <int H>
DI void head(const Ctx& c, bf16x8 (&q)[8], Stamps& st) {
  constexpr int R = 8 * H;
  Hd s;
  { const unsigned ra = row_base(opaque(c.lane));
#pragma unroll
    for (int d0 = 0; d0 < 8; ++d0) s.ka[d0] = opaque((ra ^ (32u * d0)) + RING + ((R % 6) >= 3 ? 49152u : 0u)); }
  dma_ring<R + 2>(c);
  GAP();
  wait_bar<4>();
  GAP();
  k_read<H, 0>(s); k_read<H, 1>(s); k_read<H, 2>(s); k_read<H, 3>(s);
  GAP();
  gaps<H>(c, s, q, st, std::make_integer_sequence<int, 128>{});
  ST(1 + 9 * H + 8 + (H ? 1 : 0));
  float ls = s.l[0] + s.l[1];
  { auto rr = __builtin_amdgcn_permlane32_swap(__float_as_uint(ls), __float_as_uint(ls), false, false);
    ls = __uint_as_float(rr[0]) + __uint_as_float(rr[1]); }
  const float il = __builtin_amdgcn_rcpf(ls);
  u32x2 w[16];
#pragma unroll
  for (int db = 0; db < 4; ++db)
#pragma unroll
    for (int g = 0; g < 4; ++g)
      w[4 * db + g] = u32x2{cvtpk(s.o[db][4 * g] * il, s.o[db][4 * g + 1] * il), cvtpk(s.o[db][4 * g + 2] * il, s.o[db][4 * g + 3] * il)};
  GAP();
  if constexpr (H == 0) {
    wait_vm<4>();
    GAP();
    read_q(c, q);
    GAP(); asm volatile("s_waitcnt lgkmcnt(0)" ::: "memory"); GAP();
  }
  {
    const unsigned lane = opaque(c.lane), l32 = lane & 31u, h = lane >> 5;
    const unsigned ow = RQO + c.wid * 8192u + l32 * 256u + 16u * (l32 & 7u) + 8u * h;
#pragma unroll
    for (int i = 0; i < 16; ++i) *(lds_u2p)(ow ^ (16u * i)) = w[i];
  }
  if constexpr (H == 1) store_o<0, 8>(c, c.hB);
  GAP();
}

__global__ __launch_bounds__(512, 2) void attn_fwd(const void* __restrict__ Q, const void* __restrict__ K, const void* __restrict__ V, void* __restrict__ O, unsigned* __restrict__ dbg) {
  Stamps st;
  ST(0);
  Ctx c;
  const unsigned tid = threadIdx.x, lane = tid & 63u;
  c.lane = lane;
  c.wid = __builtin_amdgcn_readfirstlane(tid >> 6);
  const int nbytes = NHEADS * (int)HEADB;
  c.sq = __builtin_amdgcn_make_buffer_rsrc((void*)Q, (short)0, nbytes, 0x00020000);
  c.sk = __builtin_amdgcn_make_buffer_rsrc((void*)K, (short)0, nbytes, 0x00020000);
  c.sv = __builtin_amdgcn_make_buffer_rsrc((void*)V, (short)0, nbytes, 0x00020000);
  c.so = __builtin_amdgcn_make_buffer_rsrc((void*)O, (short)0, nbytes, 0x00020000);
  {
    const unsigned r = lane >> 4, p = lane & 15u;
    c.v0 = r * 256u + 16u * (p ^ (r << 2));
  }
  c.hA = blockIdx.x * HEADB; c.hB = (blockIdx.x + NWG) * HEADB;
  asm volatile("s_nop 4");
  dma_q<0>(c, c.hA); dma_q<1>(c, c.hA); dma_q<2>(c, c.hA); dma_q<3>(c, c.hA);
  dma_ring<0>(c); dma_ring<1>(c);
  bf16x8 q[8];
  GAP();
  wait_vm<4>();
  GAP();
  ST(1);
  read_q(c, q);
  head<0>(c, q, st);
  head<1>(c, q, st);
}

extern "C" void kernel_launch(void* const* d_in, const int* in_sizes, int n_in, void* d_out, int out_size, void* d_ws, size_t ws_size, hipStream_t stream) {
  (void)in_sizes; (void)n_in; (void)out_size; (void)ws_size;
  (void)hipFuncSetAttribute((const void*)attn_fwd, hipFuncAttributeMaxDynamicSharedMemorySize, (int)LDS_BYTES);
  hipLaunchKernelGGL(attn_fwd, dim3(NWG), dim3(512), LDS_BYTES, stream, d_in[0], d_in[1], d_in[2], d_out, (unsigned*)d_ws);
}
```

```cpp
#include <hip/hip_runtime.h>
#include <hip/hip_bf16.h>
#include <utility>
#define STAMPS
#define RSTAMP
#define SPINBASE 10000u
#define SIDE 0
#define MEMPRE 1

typedef float    f32x16 __attribute__((ext_vector_type(16)));
typedef float    f32x2  __attribute__((ext_vector_type(2)));
typedef __bf16   bf16x8 __attribute__((ext_vector_type(8)));
typedef __bf16   bf16x2 __attribute__((ext_vector_type(2)));
typedef short    s16x4  __attribute__((ext_vector_type(4)));
typedef short    s16x8  __attribute__((ext_vector_type(8)));
typedef unsigned u32x4  __attribute__((ext_vector_type(4)));
typedef unsigned u32x2  __attribute__((ext_vector_type(2)));
typedef __attribute__((address_space(3))) const bf16x8* lds_b8p;
typedef __attribute__((address_space(3))) s16x4* lds_s4p;
typedef __attribute__((address_space(3))) u32x2* lds_u2p;
typedef __attribute__((address_space(3))) const u32x4* lds_u4p;

#define DI __device__ __forceinline__
#define GAP() __builtin_amdgcn_sched_barrier(0)
#define PIN(x) asm volatile("" : "+v"(x))
#define LDPOL " nt"
#define STAUX 16
#define BOFF 8

constexpr unsigned HEADB = 65536u;
constexpr unsigned RQO = 0u, RING = 65536u, LDS_BYTES = 163840u;
constexpr int NHEADS = 512, NWG = 256;
constexpr float CEXP = 0.08838834764831845f * 1.4426950408889634f;
constexpr int PFK = 4, PFV = 3;

DI unsigned cvtpk(float lo, float hi) { f32x2 v = {lo, hi}; bf16x2 b = __builtin_convertvector(v, bf16x2); return __builtin_bit_cast(unsigned, b); }
DI float fmax2(float a, float b) { return __builtin_elementwise_maximum(a, b); }
DI float fmax3(float a, float b, float c) { return __builtin_elementwise_maximum(__builtin_elementwise_maximum(a, b), c); }
DI unsigned opaque(unsigned x) { asm volatile("" : "+v"(x)); return x; }

#define NST 40
struct Stamps { unsigned t[NST]; };
DI unsigned stamp_now() { unsigned long long t; GAP();
  asm volatile("s_memrealtime %0\n\ts_waitcnt lgkmcnt(0)" : "=s"(t) :: "memory");
  GAP(); return (unsigned)t; }
#define ST(i) (st.t[i] = stamp_now())

DI void dma2(unsigned m0v, unsigned v0, unsigned v1, __amdgpu_buffer_rsrc_t srd, unsigned soff) {
  asm volatile("s_mov_b32 m0, %0\n\ts_nop 0\n\t"
               "buffer_load_dwordx4 %1, %3, %4 offen" LDPOL " lds\n\t"
               "buffer_load_dwordx4 %2, %3, %4 offen offset:1024" LDPOL " lds"
               :: "s"(m0v), "v"(v0), "v"(v1), "s"(srd), "s"(soff) : "memory");
}
template <int N> DI void wait_bar() { asm volatile("s_waitcnt vmcnt(%0)\n\ts_barrier" :: "i"(N) : "memory"); }
template <int N> DI void wait_vm() { asm volatile("s_waitcnt vmcnt(%0)" :: "i"(N) : "memory"); }

struct Ctx {
  __amdgpu_buffer_rsrc_t sq, sk, sv, so;
  unsigned wid, lane;
  unsigned v0;
  unsigned hA, hB;
};
constexpr unsigned slot_of(int i) { return RING + 16384u * (unsigned)(i % 6); }

template <int I> DI void dma_ring(const Ctx& c) {
  const unsigned w2 = (32u * c.wid) & 48u;
  dma2(slot_of(I) + c.wid * 2048u, c.v0 ^ w2, c.v0 ^ (w2 + 16u), (I & 4) ? c.sv : c.sk, ((I & 8) ? c.hB : c.hA) + 16384u * (I & 3) + c.wid * 2048u);
}
template <int T> DI void dma_q(const Ctx& c, unsigned hoff) {
  const unsigned o = c.wid * 8192u + 2048u * T;
  dma2(RQO + o, c.v0 ^ (16u * ((2 * T) & 3)), c.v0 ^ (16u * ((2 * T + 1) & 3)), c.sq, hoff + o);
}
DI unsigned row_base(unsigned lane) { const unsigned l32 = lane & 31u, h = lane >> 5, f = ((l32 & 3u) << 2) | ((l32 >> 2) & 3u); return l32 * 256u + 16u * (f ^ h); }
DI void read_q(const Ctx& c, bf16x8 (&q)[8]) {
  const unsigned b = row_base(opaque(c.lane)) + RQO + c.wid * 8192u;
#pragma unroll
  for (int d0 = 0; d0 < 8; ++d0) q[d0] = *(lds_b8p)(b ^ (32u * d0));
}
template <int I0, int I1> DI void store_o(const Ctx& c, unsigned hoff) {
  const unsigned lane = opaque(c.lane), rr = lane >> 4, cc = lane & 15u;
  const unsigned b0 = RQO + c.wid * 8192u + rr * 256u + 16u * (cc ^ rr), b1 = b0 ^ 64u;
  u32x4 w[8];
#pragma unroll
  for (int i = I0; i < I1; ++i) w[i] = *(lds_u4p)(((i & 1) ? b1 : b0) + 1024u * i);
#pragma unroll
  for (int i = I0; i < I1; ++i) __builtin_amdgcn_raw_buffer_store_b128(w[i], c.so, lane * 16u + 1024u * i, hoff + c.wid * 8192u, STAUX);
}

struct Hd {
  f32x16 S[8], o[4];
  bf16x8 kf[64];
  s16x4 vlo[64], vhi[64];
  u32x4 P[2];
  unsigned ka[8], va[8];
  float mx[2], l[2], mc;
};

template <int H, int G> DI void events(const Ctx& c) {
  constexpr int R = 8 * H;
  if constexpr ((G + BOFF) % 16 == 0 && G + BOFF >= 16 && G + BOFF <= 112) {
    constexpr int j = (G + BOFF) / 16;
    constexpr int pos[12] = {-1, 14, 28, 42, 56, 67, 77, 87, 97, 107, 117, 127};
    constexpr int ej = j - 2;
    int n = 0;
    for (int e = ej + 1; e < 12; ++e) if (pos[e] < G) n += (H == 1 && e >= 10) ? 0 : 2;
    constexpr int N = []() constexpr { int n = 0; for (int e = ej + 1; e < 12; ++e) if (pos[e] < G) n += (H == 1 && e >= 10) ? 0 : 2; return n; }();
    (void)n;
    wait_bar<N>();
  }
  if constexpr (G == 14) dma_ring<R + 3>(c);
  if constexpr (G == 28) dma_ring<R + 4>(c);
  if constexpr (G == 42) dma_ring<R + 5>(c);
  if constexpr (G == 56) dma_ring<R + 6>(c);
  if constexpr (G == 67) dma_ring<R + 7>(c);
  if constexpr (H == 0) {
    if constexpr (G == 77) dma_q<0>(c, c.hB);
    if constexpr (G == 87) dma_q<1>(c, c.hB);
    if constexpr (G == 97) dma_q<2>(c, c.hB);
    if constexpr (G == 107) dma_q<3>(c, c.hB);
    if constexpr (G == 117) dma_ring<8>(c);
    if constexpr (G == 127) dma_ring<9>(c);
  } else {
    if constexpr (G == 77) store_o<0, 2>(c, c.hA);
    if constexpr (G == 87) store_o<2, 4>(c, c.hA);
    if constexpr (G == 97) store_o<4, 6>(c, c.hA);
    if constexpr (G == 107) store_o<6, 8>(c, c.hA);
  }
}
template <int H, int G> DI void k_read(Hd& s) {
  constexpr int I = 8 * H + (G >> 4);
  s.kf[G] = *(lds_b8p)(s.ka[G & 7] + 16384u * ((I % 6) % 3) + 8192u * ((G >> 3) & 1));
}
template <int H, int GP> DI void v_read(Hd& s) {
  constexpr int I = 8 * H + 4 + (GP >> 4), ks = (GP >> 2) & 3, db = GP & 3;
  s.vlo[GP] = __builtin_amdgcn_ds_read_tr16_b64_v4i16((lds_s4p)(s.va[2 * db] + 16384u * ((I % 6) % 3) + 4096u * ks));
  s.vhi[GP] = __builtin_amdgcn_ds_read_tr16_b64_v4i16((lds_s4p)(s.va[2 * db + 1] + 16384u * ((I % 6) % 3) + 4096u * ks + 2048u));
}
template <int IPREV, int INEXT> DI void rebase(unsigned (&a)[8]) {
  if constexpr ((IPREV % 6) / 3 != (INEXT % 6) / 3) {
#pragma unroll
    for (int i = 0; i < 8; ++i) a[i] = opaque((INEXT % 6) >= 3 ? a[i] + 49152u : a[i] - 49152u);
  }
}
DI void sm_pair(Hd& s, int ks, int pr) {
  const int kb = ks >> 1, r = 8 * (ks & 1) + 2 * pr;
  const float e0 = __builtin_amdgcn_exp2f(__builtin_fmaf(s.S[kb][r], CEXP, s.mc));
  const float e1 = __builtin_amdgcn_exp2f(__builtin_fmaf(s.S[kb][r + 1], CEXP, s.mc));
  s.l[0] += e0; s.l[1] += e1;
  unsigned w = cvtpk(e0, e1);
  PIN(s.l[0]); PIN(s.l[1]); PIN(w);
  s.P[ks & 1][pr] = w;
}
DI void va_init(const Ctx& c, Hd& s, unsigned base) {
  const unsigned lane = opaque(c.lane), h = lane >> 5, blk = (lane >> 4) & 1u, q4 = (lane & 15u) >> 2, p = lane & 3u;
  const unsigned vb = 256u * (4u * h + q4) + 16u * (4u * q4 + ((2u * blk + (p >> 1)) ^ h)) + 8u * (p & 1u);
#pragma unroll
  for (int i = 0; i < 8; ++i) s.va[i] = opaque((vb ^ (64u * (i >> 1)) ^ (32u * (i & 1))) + base);
}

template <int H, int G> DI void gap(const Ctx& c, Hd& s, const bf16x8 (&q)[8], Stamps& st) {
  constexpr int R = 8 * H;
  if constexpr (G % 16 == 0) ST(1 + 9 * H + G / 16 + (H ? 1 : 0));
  events<H, G>(c);
  if constexpr (G < 64) {
    if constexpr (G + PFK < 64) {
      if constexpr (((G + PFK) & 15) == 0) rebase<R + ((G + PFK) >> 4) - 1, R + ((G + PFK) >> 4)>(s.ka);
      k_read<H, G + PFK>(s);
    }
    if constexpr (G == 60) va_init(c, s, RING + (((R + 4) % 6) >= 3 ? 49152u : 0u));
    if constexpr (G + PFV >= 64) v_read<H, G + PFV - 64>(s);
    constexpr int kb = G >> 3, d0 = G & 7;
    if constexpr (d0 == 0) s.S[kb] = __builtin_amdgcn_mfma_f32_32x32x16_bf16(s.kf[G], q[d0], f32x16{}, 0, 0, 0);
    else                   s.S[kb] = __builtin_amdgcn_mfma_f32_32x32x16_bf16(s.kf[G], q[d0], s.S[kb], 0, 0, 0);
    PIN(s.S[kb]);
    if constexpr (kb >= 1 && d0 >= 2 && d0 < 6) {
      constexpr int pb = kb - 1, r = 4 * (d0 - 2);
      if constexpr (pb == 0 && d0 == 2) { s.mx[0] = fmax2(s.S[pb][r], s.S[pb][r + 1]); s.mx[1] = fmax2(s.S[pb][r + 2], s.S[pb][r + 3]); }
      else { s.mx[0] = fmax3(s.mx[0], s.S[pb][r], s.S[pb][r + 1]); s.mx[1] = fmax3(s.mx[1], s.S[pb][r + 2], s.S[pb][r + 3]); }
      PIN(s.mx[0]); PIN(s.mx[1]);
    }
    if constexpr (G == 63) {
#pragma unroll
      for (int r = 0; r < 16; r += 4) { s.mx[0] = fmax3(s.mx[0], s.S[7][r], s.S[7][r + 1]); s.mx[1] = fmax3(s.mx[1], s.S[7][r + 2], s.S[7][r + 3]); }
      float m = fmax2(s.mx[0], s.mx[1]);
      auto rr = __builtin_amdgcn_permlane32_swap(__float_as_uint(m), __float_as_uint(m), false, false);
      m = fmax2(__uint_as_float(rr[0]), __uint_as_float(rr[1]));
      s.mc = -m * CEXP;
      s.l[0] = 0.f; s.l[1] = 0.f;
#pragma unroll
      for (int pr = 0; pr < 4; ++pr) sm_pair(s, 0, pr);
    }
  } else {
    constexpr int GP = G - 64, ks = GP >> 2, db = GP & 3;
    if constexpr (GP + PFV < 64) {
      if constexpr (((GP + PFV) & 15) == 0) rebase<R + 4 + ((GP + PFV) >> 4) - 1, R + 4 + ((GP + PFV) >> 4)>(s.va);
      v_read<H, GP + PFV>(s);
    }
    const s16x8 vf = __builtin_shufflevector(s.vlo[GP], s.vhi[GP], 0, 1, 2, 3, 4, 5, 6, 7);
    if constexpr (ks == 0) s.o[db] = __builtin_amdgcn_mfma_f32_32x32x16_bf16(__builtin_bit_cast(bf16x8, vf), __builtin_bit_cast(bf16x8, s.P[0]), f32x16{}, 0, 0, 0);
    else                   s.o[db] = __builtin_amdgcn_mfma_f32_32x32x16_bf16(__builtin_bit_cast(bf16x8, vf), __builtin_bit_cast(bf16x8, s.P[ks & 1]), s.o[db], 0, 0, 0);
    PIN(s.o[db]);
    if constexpr (ks + 1 < 16) sm_pair(s, ks + 1, db);
  }
  GAP();
}
template <int H, int... Gs> DI void gaps(const Ctx& c, Hd& s, const bf16x8 (&q)[8], Stamps& st, std::integer_sequence<int, Gs...>) { (gap<H, Gs>(c, s, q, st), ...); }

template <int H>
DI void head(const Ctx& c, bf16x8 (&q)[8], Stamps& st) {
  constexpr int R = 8 * H;
  Hd s;
  { const unsigned ra = row_base(opaque(c.lane));
#pragma unroll
    for (int d0 = 0; d0 < 8; ++d0) s.ka[d0] = opaque((ra ^ (32u * d0)) + RING + ((R % 6) >= 3 ? 49152u : 0u)); }
  dma_ring<R + 2>(c);
  GAP();
  wait_bar<4>();
  GAP();
  k_read<H, 0>(s); k_read<H, 1>(s); k_read<H, 2>(s); k_read<H, 3>(s);
  GAP();
  gaps<H>(c, s, q, st, std::make_integer_sequence<int, 128>{});
  ST(1 + 9 * H + 8 + (H ? 1 : 0));
  float ls = s.l[0] + s.l[1];
  { auto rr = __builtin_amdgcn_permlane32_swap(__float_as_uint(ls), __float_as_uint(ls), false, false);
    ls = __uint_as_float(rr[0]) + __uint_as_float(rr[1]); }
  const float il = __builtin_amdgcn_rcpf(ls);
  u32x2 w[16];
#pragma unroll
  for (int db = 0; db < 4; ++db)
#pragma unroll
    for (int g = 0; g < 4; ++g)
      w[4 * db + g] = u32x2{cvtpk(s.o[db][4 * g] * il, s.o[db][4 * g + 1] * il), cvtpk(s.o[db][4 * g + 2] * il, s.o[db][4 * g + 3] * il)};
  GAP();
  if constexpr (H == 0) {
    wait_vm<4>();
    GAP();
    read_q(c, q);
    GAP(); asm volatile("s_waitcnt lgkmcnt(0)" ::: "memory"); GAP();
  }
  {
    const unsigned lane = opaque(c.lane), l32 = lane & 31u, h = lane >> 5;
    const unsigned ow = RQO + c.wid * 8192u + l32 * 256u + 16u * (l32 & 7u) + 8u * h;
#pragma unroll
    for (int i = 0; i < 16; ++i) *(lds_u2p)(ow ^ (16u * i)) = w[i];
  }
  if constexpr (H == 1) store_o<0, 8>(c, c.hB);
  GAP();
}

__global__ __launch_bounds__(512, 2) void attn_fwd(const void* __restrict__ Q, const void* __restrict__ K, const void* __restrict__ V, void* __restrict__ O, unsigned* __restrict__ dbg) {
  Stamps st;
  unsigned long long rt0; asm volatile("s_memrealtime %0\n\ts_waitcnt lgkmcnt(0)" : "=s"(rt0) :: "memory");
  for (int i = 0; i < NST; ++i) st.t[i] = 0;
  ST(0);
  Ctx c;
  const unsigned tid = threadIdx.x, lane = tid & 63u;
  c.lane = lane;
  c.wid = __builtin_amdgcn_readfirstlane(tid >> 6);
  const int nbytes = NHEADS * (int)HEADB;
  c.sq = __builtin_amdgcn_make_buffer_rsrc((void*)Q, (short)0, nbytes, 0x00020000);
  c.sk = __builtin_amdgcn_make_buffer_rsrc((void*)K, (short)0, nbytes, 0x00020000);
  c.sv = __builtin_amdgcn_make_buffer_rsrc((void*)V, (short)0, nbytes, 0x00020000);
  c.so = __builtin_amdgcn_make_buffer_rsrc((void*)O, (short)0, nbytes, 0x00020000);
  {
    const unsigned r = lane >> 4, p = lane & 15u;
    c.v0 = r * 256u + 16u * (p ^ (r << 2));
  }
  c.hA = blockIdx.x * HEADB; c.hB = (blockIdx.x + NWG) * HEADB;
  asm volatile("s_nop 4");
  {
    u32x4 junk = {lane, lane, lane, lane};
#pragma unroll
    for (int hh = 0; hh < 2; ++hh) {
      const unsigned ho = hh ? c.hB : c.hA;
#pragma unroll
      for (int t = 0; t < 12; ++t) {
        const unsigned o = 16384u * (t & 3) + c.wid * 2048u;
        dma2((16384u * (t % 10) + c.wid * 2048u), c.v0, c.v0, t < 4 ? c.sq : (t < 8 ? c.sk : c.sv), ho + o);
      }
      if (MEMPRE == 1) {
        asm volatile("s_waitcnt vmcnt(0)" ::: "memory");
#pragma unroll
        for (int i = 0; i < 8; ++i) __builtin_amdgcn_raw_buffer_store_b128(junk, c.so, lane * 16u + 1024u * i, ho + c.wid * 8192u, STAUX);
      }
    }
    if (MEMPRE == 2) {
      asm volatile("s_waitcnt vmcnt(0)" ::: "memory");
#pragma unroll
      for (int hh = 0; hh < 2; ++hh)
#pragma unroll
        for (int i = 0; i < 8; ++i) __builtin_amdgcn_raw_buffer_store_b128(junk, c.so, lane * 16u + 1024u * i, (hh ? c.hB : c.hA) + c.wid * 8192u, STAUX);
    }
    asm volatile("s_waitcnt vmcnt(0)\n\ts_barrier" ::: "memory");
    ST(30);
  }
  dma_q<0>(c, c.hA); dma_q<1>(c, c.hA); dma_q<2>(c, c.hA); dma_q<3>(c, c.hA);
  dma_ring<0>(c); dma_ring<1>(c);
  bf16x8 q[8];
  GAP();
  wait_vm<4>();
  GAP();
  ST(1);
  read_q(c, q);
  head<0>(c, q, st);
  head<1>(c, q, st);
  ST(21);
  asm volatile("s_waitcnt vmcnt(0)" ::: "memory");
  ST(22);
  unsigned long long rt1; asm volatile("s_memrealtime %0\n\ts_waitcnt lgkmcnt(0)" : "=s"(rt1) :: "memory");
  st.t[38] = (unsigned)rt0; st.t[39] = (unsigned)rt1;
  { const unsigned target = SPINBASE + (SIDE == 0 ? 0u : st.t[SIDE] - (unsigned)rt0);
    for (;;) { unsigned long long now; asm volatile("s_memrealtime %0\n\ts_waitcnt lgkmcnt(0)" : "=s"(now) :: "memory"); if ((unsigned)now - (unsigned)rt0 >= target) break; __builtin_amdgcn_s_sleep(2); } }
  if (false) {
#pragma unroll
    for (int i = 0; i < NST; ++i) dbg[(blockIdx.x * 8 + c.wid) * NST + i] = st.t[i];
  }
}

extern "C" void kernel_launch(void* const* d_in, const int* in_sizes, int n_in, void* d_out, int out_size, void* d_ws, size_t ws_size, hipStream_t stream) {
  (void)in_sizes; (void)n_in; (void)out_size; (void)ws_size;
  (void)hipFuncSetAttribute((const void*)attn_fwd, hipFuncAttributeMaxDynamicSharedMemorySize, (int)LDS_BYTES);
  hipLaunchKernelGGL(attn_fwd, dim3(NWG), dim3(512), LDS_BYTES, stream, d_in[0], d_in[1], d_in[2], d_out, (unsigned*)d_ws);
}
```

```cpp
#include <hip/hip_runtime.h>
#include <hip/hip_bf16.h>
#include <utility>
#define STAMPS
#define RSTAMP
#define SIDE 31

typedef float    f32x16 __attribute__((ext_vector_type(16)));
typedef float    f32x2  __attribute__((ext_vector_type(2)));
typedef __bf16   bf16x8 __attribute__((ext_vector_type(8)));
typedef __bf16   bf16x2 __attribute__((ext_vector_type(2)));
typedef short    s16x4  __attribute__((ext_vector_type(4)));
typedef short    s16x8  __attribute__((ext_vector_type(8)));
typedef unsigned u32x4  __attribute__((ext_vector_type(4)));
typedef unsigned u32x2  __attribute__((ext_vector_type(2)));
typedef __attribute__((address_space(3))) const bf16x8* lds_b8p;
typedef __attribute__((address_space(3))) s16x4* lds_s4p;
typedef __attribute__((address_space(3))) u32x2* lds_u2p;
typedef __attribute__((address_space(3))) const u32x4* lds_u4p;

#define DI __device__ __forceinline__
#define SPINBASE 6000u
#define GAP() __builtin_amdgcn_sched_barrier(0)
#define PIN(x) asm volatile("" : "+v"(x))
#define LDPOL " nt"
#define STAUX 16
#define BOFF 8

constexpr unsigned HEADB = 65536u;
constexpr unsigned RQO = 0u, RING = 65536u, LDS_BYTES = 163840u;
constexpr int NHEADS = 512, NWG = 256;
constexpr float CEXP = 0.08838834764831845f * 1.4426950408889634f;
constexpr int PFK = 4, PFV = 3;

DI unsigned cvtpk(float lo, float hi) { f32x2 v = {lo, hi}; bf16x2 b = __builtin_convertvector(v, bf16x2); return __builtin_bit_cast(unsigned, b); }
DI float fmax2(float a, float b) { return __builtin_elementwise_maximum(a, b); }
DI float fmax3(float a, float b, float c) { return __builtin_elementwise_maximum(__builtin_elementwise_maximum(a, b), c); }
DI unsigned opaque(unsigned x) { asm volatile("" : "+v"(x)); return x; }

#define NST 40
struct Stamps { unsigned t[NST]; };
DI unsigned stamp_now() { unsigned long long t; GAP();
  asm volatile("s_memrealtime %0\n\ts_waitcnt lgkmcnt(0)" : "=s"(t) :: "memory");
  GAP(); return (unsigned)t; }
#define ST(i) (st.t[i] = stamp_now())

DI void dma2(unsigned m0v, unsigned v0, unsigned v1, __amdgpu_buffer_rsrc_t srd, unsigned soff) {
  asm volatile("s_mov_b32 m0, %0\n\ts_nop 0\n\t"
               "buffer_load_dwordx4 %1, %3, %4 offen" LDPOL " lds\n\t"
               "buffer_load_dwordx4 %2, %3, %4 offen offset:1024" LDPOL " lds"
               :: "s"(m0v), "v"(v0), "v"(v1), "s"(srd), "s"(soff) : "memory");
}
template <int N> DI void wait_bar() { asm volatile("s_waitcnt vmcnt(%0)\n\ts_barrier" :: "i"(N) : "memory"); }
template <int N> DI void wait_vm() { asm volatile("s_waitcnt vmcnt(%0)" :: "i"(N) : "memory"); }

struct Ctx {
  __amdgpu_buffer_rsrc_t sq, sk, sv, so;
  unsigned wid, lane;
  unsigned v0;
  unsigned hA, hB;
};
constexpr unsigned slot_of(int i) { return RING + 16384u * (unsigned)(i % 6); }

template <int I> DI void dma_ring(const Ctx& c) {
  const unsigned w2 = (32u * c.wid) & 48u;
  dma2(slot_of(I) + c.wid * 2048u, c.v0 ^ w2, c.v0 ^ (w2 + 16u), (I & 4) ? c.sv : c.sk, ((I & 8) ? c.hB : c.hA) + 16384u * (I & 3) + c.wid * 2048u);
}
template <int T> DI void dma_q(const Ctx& c, unsigned hoff) {
  const unsigned o = c.wid * 8192u + 2048u * T;
  dma2(RQO + o, c.v0 ^ (16u * ((2 * T) & 3)), c.v0 ^ (16u * ((2 * T + 1) & 3)), c.sq, hoff + o);
}
DI unsigned row_base(unsigned lane) { const unsigned l32 = lane & 31u, h = lane >> 5, f = ((l32 & 3u) << 2) | ((l32 >> 2) & 3u); return l32 * 256u + 16u * (f ^ h); }
DI void read_q(const Ctx& c, bf16x8 (&q)[8]) {
  const unsigned b = row_base(opaque(c.lane)) + RQO + c.wid * 8192u;
#pragma unroll
  for (int d0 = 0; d0 < 8; ++d0) q[d0] = *(lds_b8p)(b ^ (32u * d0));
}
template <int I0, int I1> DI void store_o(const Ctx& c, unsigned hoff) {
  const unsigned lane = opaque(c.lane), rr = lane >> 4, cc = lane & 15u;
  const unsigned b0 = RQO + c.wid * 8192u + rr * 256u + 16u * (cc ^ rr), b1 = b0 ^ 64u;
  u32x4 w[8];
#pragma unroll
  for (int i = I0; i < I1; ++i) w[i] = *(lds_u4p)(((i & 1) ? b1 : b0) + 1024u * i);
#pragma unroll
  for (int i = I0; i < I1; ++i) __builtin_amdgcn_raw_buffer_store_b128(w[i], c.so, lane * 16u + 1024u * i, hoff + c.wid * 8192u, STAUX);
}

struct Hd {
  f32x16 S[8], o[4];
  bf16x8 kf[64];
  s16x4 vlo[64], vhi[64];
  u32x4 P[2];
  unsigned ka[8], va[8];
  float mx[2], l[2], mc;
};

template <int H, int G> DI void events(const Ctx& c) {
  constexpr int R = 8 * H;
  if constexpr ((G + BOFF) % 16 == 0 && G + BOFF >= 16 && G + BOFF <= 112) {
    constexpr int j = (G + BOFF) / 16;
    constexpr int pos[12] = {-1, 14, 28, 42, 56, 67, 77, 87, 97, 107, 117, 127};
    constexpr int ej = j - 2;
    int n = 0;
    for (int e = ej + 1; e < 12; ++e) if (pos[e] < G) n += (H == 1 && e >= 10) ? 0 : 2;
    constexpr int N = []() constexpr { int n = 0; for (int e = ej + 1; e < 12; ++e) if (pos[e] < G) n += (H == 1 && e >= 10) ? 0 : 2; return n; }();
    (void)n;
    wait_bar<N>();
  }
  if constexpr (G == 14) dma_ring<R + 3>(c);
  if constexpr (G == 28) dma_ring<R + 4>(c);
  if constexpr (G == 42) dma_ring<R + 5>(c);
  if constexpr (G == 56) dma_ring<R + 6>(c);
  if constexpr (G == 67) dma_ring<R + 7>(c);
  if constexpr (H == 0) {
    if constexpr (G == 77) dma_q<0>(c, c.hB);
    if constexpr (G == 87) dma_q<1>(c, c.hB);
    if constexpr (G == 97) dma_q<2>(c, c.hB);
    if constexpr (G == 107) dma_q<3>(c, c.hB);
    if constexpr (G == 117) dma_ring<8>(c);
    if constexpr (G == 127) dma_ring<9>(c);
  } else {
    if constexpr (G == 77) store_o<0, 2>(c, c.hA);
    if constexpr (G == 87) store_o<2, 4>(c, c.hA);
    if constexpr (G == 97) store_o<4, 6>(c, c.hA);
    if constexpr (G == 107) store_o<6, 8>(c, c.hA);
  }
}
template <int H, int G> DI void k_read(Hd& s) {
  constexpr int I = 8 * H + (G >> 4);
  s.kf[G] = *(lds_b8p)(s.ka[G & 7] + 16384u * ((I % 6) % 3) + 8192u * ((G >> 3) & 1));
}
template <int H, int GP> DI void v_read(Hd& s) {
  constexpr int I = 8 * H + 4 + (GP >> 4), ks = (GP >> 2) & 3, db = GP & 3;
  s.vlo[GP] = __builtin_amdgcn_ds_read_tr16_b64_v4i16((lds_s4p)(s.va[2 * db] + 16384u * ((I % 6) % 3) + 4096u * ks));
  s.vhi[GP] = __builtin_amdgcn_ds_read_tr16_b64_v4i16((lds_s4p)(s.va[2 * db + 1] + 16384u * ((I % 6) % 3) + 4096u * ks + 2048u));
}
template <int IPREV, int INEXT> DI void rebase(unsigned (&a)[8]) {
  if constexpr ((IPREV % 6) / 3 != (INEXT % 6) / 3) {
#pragma unroll
    for (int i = 0; i < 8; ++i) a[i] = opaque((INEXT % 6) >= 3 ? a[i] + 49152u : a[i] - 49152u);
  }
}
DI void sm_pair(Hd& s, int ks, int pr) {
  const int kb = ks >> 1, r = 8 * (ks & 1) + 2 * pr;
  const float e0 = __builtin_amdgcn_exp2f(__builtin_fmaf(s.S[kb][r], CEXP, s.mc));
  const float e1 = __builtin_amdgcn_exp2f(__builtin_fmaf(s.S[kb][r + 1], CEXP, s.mc));
  s.l[0] += e0; s.l[1] += e1;
  unsigned w = cvtpk(e0, e1);
  PIN(s.l[0]); PIN(s.l[1]); PIN(w);
  s.P[ks & 1][pr] = w;
}
DI void va_init(const Ctx& c, Hd& s, unsigned base) {
  const unsigned lane = opaque(c.lane), h = lane >> 5, blk = (lane >> 4) & 1u, q4 = (lane & 15u) >> 2, p = lane & 3u;
  const unsigned vb = 256u * (4u * h + q4) + 16u * (4u * q4 + ((2u * blk + (p >> 1)) ^ h)) + 8u * (p & 1u);
#pragma unroll
  for (int i = 0; i < 8; ++i) s.va[i] = opaque((vb ^ (64u * (i >> 1)) ^ (32u * (i & 1))) + base);
}

template <int H, int G> DI void gap(const Ctx& c, Hd& s, const bf16x8 (&q)[8], Stamps& st) {
  constexpr int R = 8 * H;
  if constexpr (G % 16 == 0) ST(1 + 9 * H + G / 16 + (H ? 1 : 0));
  events<H, G>(c);
  if constexpr (G < 64) {
    if constexpr (G + PFK < 64) {
      if constexpr (((G + PFK) & 15) == 0) rebase<R + ((G + PFK) >> 4) - 1, R + ((G + PFK) >> 4)>(s.ka);
      k_read<H, G + PFK>(s);
    }
    if constexpr (G == 60) va_init(c, s, RING + (((R + 4) % 6) >= 3 ? 49152u : 0u));
    if constexpr (G + PFV >= 64) v_read<H, G + PFV - 64>(s);
    constexpr int kb = G >> 3, d0 = G & 7;
    if constexpr (d0 == 0) s.S[kb] = __builtin_amdgcn_mfma_f32_32x32x16_bf16(s.kf[G], q[d0], f32x16{}, 0, 0, 0);
    else                   s.S[kb] = __builtin_amdgcn_mfma_f32_32x32x16_bf16(s.kf[G], q[d0], s.S[kb], 0, 0, 0);
    PIN(s.S[kb]);
    if constexpr (kb >= 1 && d0 >= 2 && d0 < 6) {
      constexpr int pb = kb - 1, r = 4 * (d0 - 2);
      if constexpr (pb == 0 && d0 == 2) { s.mx[0] = fmax2(s.S[pb][r], s.S[pb][r + 1]); s.mx[1] = fmax2(s.S[pb][r + 2], s.S[pb][r + 3]); }
      else { s.mx[0] = fmax3(s.mx[0], s.S[pb][r], s.S[pb][r + 1]); s.mx[1] = fmax3(s.mx[1], s.S[pb][r + 2], s.S[pb][r + 3]); }
      PIN(s.mx[0]); PIN(s.mx[1]);
    }
    if constexpr (G == 63) {
#pragma unroll
      for (int r = 0; r < 16; r += 4) { s.mx[0] = fmax3(s.mx[0], s.S[7][r], s.S[7][r + 1]); s.mx[1] = fmax3(s.mx[1], s.S[7][r + 2], s.S[7][r + 3]); }
      float m = fmax2(s.mx[0], s.mx[1]);
      auto rr = __builtin_amdgcn_permlane32_swap(__float_as_uint(m), __float_as_uint(m), false, false);
      m = fmax2(__uint_as_float(rr[0]), __uint_as_float(rr[1]));
      s.mc = -m * CEXP;
      s.l[0] = 0.f; s.l[1] = 0.f;
#pragma unroll
      for (int pr = 0; pr < 4; ++pr) sm_pair(s, 0, pr);
    }
  } else {
    constexpr int GP = G - 64, ks = GP >> 2, db = GP & 3;
    if constexpr (GP + PFV < 64) {
      if constexpr (((GP + PFV) & 15) == 0) rebase<R + 4 + ((GP + PFV) >> 4) - 1, R + 4 + ((GP + PFV) >> 4)>(s.va);
      v_read<H, GP + PFV>(s);
    }
    const s16x8 vf = __builtin_shufflevector(s.vlo[GP], s.vhi[GP], 0, 1, 2, 3, 4, 5, 6, 7);
    if constexpr (ks == 0) s.o[db] = __builtin_amdgcn_mfma_f32_32x32x16_bf16(__builtin_bit_cast(bf16x8, vf), __builtin_bit_cast(bf16x8, s.P[0]), f32x16{}, 0, 0, 0);
    else                   s.o[db] = __builtin_amdgcn_mfma_f32_32x32x16_bf16(__builtin_bit_cast(bf16x8, vf), __builtin_bit_cast(bf16x8, s.P[ks & 1]), s.o[db], 0, 0, 0);
    PIN(s.o[db]);
    if constexpr (ks + 1 < 16) sm_pair(s, ks + 1, db);
  }
  GAP();
}
template <int H, int... Gs> DI void gaps(const Ctx& c, Hd& s, const bf16x8 (&q)[8], Stamps& st, std::integer_sequence<int, Gs...>) { (gap<H, Gs>(c, s, q, st), ...); }

template <int H>
DI void head(const Ctx& c, bf16x8 (&q)[8], Stamps& st) {
  constexpr int R = 8 * H;
  Hd s;
  { const unsigned ra = row_base(opaque(c.lane));
#pragma unroll
    for (int d0 = 0; d0 < 8; ++d0) s.ka[d0] = opaque((ra ^ (32u * d0)) + RING + ((R % 6) >= 3 ? 49152u : 0u)); }
  dma_ring<R + 2>(c);
  GAP();
  wait_bar<4>();
  GAP();
  k_read<H, 0>(s); k_read<H, 1>(s); k_read<H, 2>(s); k_read<H, 3>(s);
  GAP();
  gaps<H>(c, s, q, st, std::make_integer_sequence<int, 128>{});
  ST(1 + 9 * H + 8 + (H ? 1 : 0));
  float ls = s.l[0] + s.l[1];
  { auto rr = __builtin_amdgcn_permlane32_swap(__float_as_uint(ls), __float_as_uint(ls), false, false);
    ls = __uint_as_float(rr[0]) + __uint_as_float(rr[1]); }
  const float il = __builtin_amdgcn_rcpf(ls);
  u32x2 w[16];
#pragma unroll
  for (int db = 0; db < 4; ++db)
#pragma unroll
    for (int g = 0; g < 4; ++g)
      w[4 * db + g] = u32x2{cvtpk(s.o[db][4 * g] * il, s.o[db][4 * g + 1] * il), cvtpk(s.o[db][4 * g + 2] * il, s.o[db][4 * g + 3] * il)};
  GAP();
  if constexpr (H == 0) {
    wait_vm<4>();
    GAP();
    read_q(c, q);
    GAP(); asm volatile("s_waitcnt lgkmcnt(0)" ::: "memory"); GAP();
  }
  {
    const unsigned lane = opaque(c.lane), l32 = lane & 31u, h = lane >> 5;
    const unsigned ow = RQO + c.wid * 8192u + l32 * 256u + 16u * (l32 & 7u) + 8u * h;
#pragma unroll
    for (int i = 0; i < 16; ++i) *(lds_u2p)(ow ^ (16u * i)) = w[i];
  }
  if constexpr (H == 1) store_o<0, 8>(c, c.hB);
  GAP();
}

__global__ __launch_bounds__(512, 2) void attn_fwd(const void* __restrict__ Q, const void* __restrict__ K, const void* __restrict__ V, void* __restrict__ O, unsigned* __restrict__ dbg) {
  Stamps st;
  unsigned long long rt0; asm volatile("s_memrealtime %0\n\ts_waitcnt lgkmcnt(0)" : "=s"(rt0) :: "memory");
  for (int i = 0; i < NST; ++i) st.t[i] = 0;
  ST(0);
  Ctx c;
  const unsigned tid = threadIdx.x, lane = tid & 63u;
  c.lane = lane;
  c.wid = __builtin_amdgcn_readfirstlane(tid >> 6);
  const int nbytes = NHEADS * (int)HEADB;
  c.sq = __builtin_amdgcn_make_buffer_rsrc((void*)Q, (short)0, nbytes, 0x00020000);
  c.sk = __builtin_amdgcn_make_buffer_rsrc((void*)K, (short)0, nbytes, 0x00020000);
  c.sv = __builtin_amdgcn_make_buffer_rsrc((void*)V, (short)0, nbytes, 0x00020000);
  c.so = __builtin_amdgcn_make_buffer_rsrc((void*)O, (short)0, nbytes, 0x00020000);
  {
    const unsigned r = lane >> 4, p = lane & 15u;
    c.v0 = r * 256u + 16u * (p ^ (r << 2));
  }
  c.hA = blockIdx.x * HEADB; c.hB = (blockIdx.x + NWG) * HEADB;
  asm volatile("s_nop 4");
  asm volatile("" :: "s"(Q), "s"(K), "s"(V), "s"(O));
  ST(31);
  dma_q<0>(c, c.hA); dma_q<1>(c, c.hA); dma_q<2>(c, c.hA); dma_q<3>(c, c.hA);
  dma_ring<0>(c); dma_ring<1>(c);
  bf16x8 q[8];
  GAP();
  wait_vm<4>();
  GAP();
  ST(32);
  read_q(c, q);
  head<0>(c, q, st);
  head<1>(c, q, st);
  ST(21);
  asm volatile("s_waitcnt vmcnt(0)" ::: "memory");
  ST(22);
  unsigned long long rt1; asm volatile("s_memrealtime %0\n\ts_waitcnt lgkmcnt(0)" : "=s"(rt1) :: "memory");
  st.t[38] = (unsigned)rt0; st.t[39] = (unsigned)rt1;
  { const unsigned target = SPINBASE + (SIDE == 0 ? 0u : st.t[SIDE] - (unsigned)rt0);
    for (;;) { unsigned long long now; asm volatile("s_memrealtime %0\n\ts_waitcnt lgkmcnt(0)" : "=s"(now) :: "memory"); if ((unsigned)now - (unsigned)rt0 >= target) break; __builtin_amdgcn_s_sleep(2); } }
  if (false) {
#pragma unroll
    for (int i = 0; i < NST; ++i) dbg[(blockIdx.x * 8 + c.wid) * NST + i] = st.t[i];
  }
}

extern "C" void kernel_launch(void* const* d_in, const int* in_sizes, int n_in, void* d_out, int out_size, void* d_ws, size_t ws_size, hipStream_t stream) {
  (void)in_sizes; (void)n_in; (void)out_size; (void)ws_size;
  (void)hipFuncSetAttribute((const void*)attn_fwd, hipFuncAttributeMaxDynamicSharedMemorySize, (int)LDS_BYTES);
  hipLaunchKernelGGL(attn_fwd, dim3(NWG), dim3(512), LDS_BYTES, stream, d_in[0], d_in[1], d_in[2], d_out, (unsigned*)d_ws);
}
```

```cpp
#include <hip/hip_runtime.h>
#include <hip/hip_bf16.h>
#include <utility>

typedef float    f32x16 __attribute__((ext_vector_type(16)));
typedef float    f32x2  __attribute__((ext_vector_type(2)));
typedef __bf16   bf16x8 __attribute__((ext_vector_type(8)));
typedef __bf16   bf16x2 __attribute__((ext_vector_type(2)));
typedef short    s16x4  __attribute__((ext_vector_type(4)));
typedef short    s16x8  __attribute__((ext_vector_type(8)));
typedef unsigned u32x4  __attribute__((ext_vector_type(4)));
typedef unsigned u32x2  __attribute__((ext_vector_type(2)));
typedef __attribute__((address_space(3))) const bf16x8* lds_b8p;
typedef __attribute__((address_space(3))) s16x4* lds_s4p;
typedef __attribute__((address_space(3))) u32x2* lds_u2p;
typedef __attribute__((address_space(3))) const u32x4* lds_u4p;

#define DI __device__ __forceinline__
#define GAP() __builtin_amdgcn_sched_barrier(0)
#define PIN(x) asm volatile("" : "+v"(x))
#define LDPOL " nt"
#define STAUX 18
#define BOFF 8
#define HMAP 2
#define LIMLOG 30

constexpr unsigned HEADB = 65536u;
constexpr unsigned RQO = 0u, RING = 65536u, LDS_BYTES = 163840u;
constexpr int NHEADS = 512, NWG = 256;
constexpr float CEXP = 0.08838834764831845f * 1.4426950408889634f;
constexpr int PFK = 4, PFV = 3;

DI unsigned cvtpk(float lo, float hi) { f32x2 v = {lo, hi}; bf16x2 b = __builtin_convertvector(v, bf16x2); return __builtin_bit_cast(unsigned, b); }
DI float fmax2(float a, float b) { return __builtin_elementwise_maximum(a, b); }
DI float fmax3(float a, float b, float c) { return __builtin_elementwise_maximum(__builtin_elementwise_maximum(a, b), c); }
DI unsigned opaque(unsigned x) { asm volatile("" : "+v"(x)); return x; }
DI float xhalf_max(float m) { auto rr = __builtin_amdgcn_permlane32_swap(__float_as_uint(m), __float_as_uint(m), false, false); return fmax2(__uint_as_float(rr[0]), __uint_as_float(rr[1])); }
DI float xhalf_sum(float m) { auto rr = __builtin_amdgcn_permlane32_swap(__float_as_uint(m), __float_as_uint(m), false, false); return __uint_as_float(rr[0]) + __uint_as_float(rr[1]); }

struct Stamps { int dummy; };
#define ST(i) ((void)0)

DI void dma2(unsigned m0v, unsigned v0, unsigned v1, __amdgpu_buffer_rsrc_t srd, unsigned soff) {
  asm volatile("s_mov_b32 m0, %0\n\ts_nop 0\n\t"
               "buffer_load_dwordx4 %1, %3, %4 offen" LDPOL " lds\n\t"
               "buffer_load_dwordx4 %2, %3, %4 offen offset:1024" LDPOL " lds"
               :: "s"(m0v), "v"(v0), "v"(v1), "s"(srd), "s"(soff) : "memory");
}
template <int N> DI void wait_bar() { asm volatile("s_waitcnt vmcnt(%0)\n\ts_barrier" :: "i"(N) : "memory"); }
template <int N> DI void wait_vm() { asm volatile("s_waitcnt vmcnt(%0)" :: "i"(N) : "memory"); }

struct Ctx {
  __amdgpu_buffer_rsrc_t sq, sk, sv, so;
  const char *pq, *pk, *pv;
  unsigned wid, lane;
  unsigned v0;
  unsigned hA, hB;
};
constexpr unsigned slot_of(int i) { return RING + 16384u * (unsigned)(i % 6); }

struct Ev { int pos, kind, idx; };
#define EVTAB \
  {0, 0, 2}, {8 + 1, 0, 3}, {24 + 1, 0, 4}, {40 + 1, 0, 5}, {56 + 1, 0, 6}, {64 + 1, 1, 0}, {72 + 1, 0, 7}, {80 + 1, 1, 1}, {88 + 1, 1, 2}, {96 + 1, 1, 3}, \
  {104 + 1, 0, 8}, {112 + 1, 0, 9}, {120 + 1, 0, 10}, \
  {1000, 0, 11}, {1000 + 0 + 1, 0, 12}, {1000 + 8 + 1, 0, 13}, {1000 + 32 + 1, 0, 14}, {1000 + 52 + 1, 0, 15}, \
  {1000 + 64 + 1, 2, 0}, {1000 + 76 + 1, 2, 1}, {1000 + 88 + 1, 2, 2}, {1000 + 100 + 1, 2, 3}
constexpr Ev EV[] = {EVTAB};
constexpr int NEV = sizeof(EV) / sizeof(EV[0]);
constexpr int younger(int kind, int idx, int pos) {
  int e0 = -1;
  for (int e = 0; e < NEV; ++e) if (EV[e].kind == kind && EV[e].idx == idx) e0 = e;
  int n = 0;
  if (e0 < 0) n = (kind == 0 && idx == 0) ? 2 : 0;
  for (int e = e0 + 1; e < NEV; ++e) if (EV[e].pos < pos) n += 2;
  return n;
}

template <int I> DI void dma_ring(const Ctx& c) {
  const unsigned w2 = (32u * c.wid) & 48u;
  dma2(slot_of(I) + c.wid * 2048u, c.v0 ^ w2, c.v0 ^ (w2 + 16u), (I & 4) ? c.sv : c.sk, ((I & 8) ? c.hB : c.hA) + 16384u * (I & 3) + c.wid * 2048u);
}
template <int T> DI void dma_q(const Ctx& c, unsigned hoff) {
  const unsigned o = c.wid * 8192u + 2048u * T;
  dma2(RQO + o, c.v0 ^ (16u * ((2 * T) & 3)), c.v0 ^ (16u * ((2 * T + 1) & 3)), c.sq, hoff + o);
}
DI unsigned row_base(unsigned lane) { const unsigned l32 = lane & 31u, h = lane >> 5, f = ((l32 & 3u) << 2) | ((l32 >> 2) & 3u); return l32 * 256u + 16u * (f ^ h); }
DI void read_q(const Ctx& c, bf16x8 (&q)[8]) {
  const unsigned b = row_base(opaque(c.lane)) + RQO + c.wid * 8192u;
#pragma unroll
  for (int d0 = 0; d0 < 8; ++d0) q[d0] = *(lds_b8p)(b ^ (32u * d0));
}
template <int I0, int I1> DI void store_o(const Ctx& c, unsigned hoff) {
  const unsigned lane = opaque(c.lane), rr = lane >> 4, cc = lane & 15u;
  const unsigned b0 = RQO + c.wid * 8192u + rr * 256u + 16u * (cc ^ rr), b1 = b0 ^ 64u;
  u32x4 w[8];
#pragma unroll
  for (int i = I0; i < I1; ++i) w[i] = *(lds_u4p)(((i & 1) ? b1 : b0) + 1024u * i);
#pragma unroll
  for (int i = I0; i < I1; ++i) __builtin_amdgcn_raw_buffer_store_b128(w[i], c.so, lane * 16u + 1024u * i, hoff + c.wid * 8192u, STAUX);
}
template <int E> DI void run_event(const Ctx& c) {
  if constexpr (EV[E].kind == 0) dma_ring<EV[E].idx>(c);
  else if constexpr (EV[E].kind == 1) dma_q<EV[E].idx>(c, c.hB);
  else store_o<2 * EV[E].idx, 2 * EV[E].idx + 2>(c, c.hA);
}
template <int POS, int... Es> DI void events_at(const Ctx& c, std::integer_sequence<int, Es...>) {
  ([&]() { if constexpr (EV[Es].pos == POS) run_event<Es>(c); }(), ...);
}

struct Hd {
  f32x16 S[8], o[4];
  bf16x8 kf[64];
  s16x4 vlo[64], vhi[64];
  u32x4 P[16];
  unsigned ka[8], va[8];
  float mx[2], l[2], mc;
  float e[64][2];
};
struct Prev { f32x16 o[4]; float il; unsigned ow; };

template <int H, int G> DI void k_read(Hd& s) {
  constexpr int I = 8 * H + (G >> 4);
  s.kf[G] = *(lds_b8p)(s.ka[G & 7] + 16384u * ((I % 6) % 3) + 8192u * ((G >> 3) & 1));
}
template <int H, int GP> DI void v_read(Hd& s) {
  constexpr int I = 8 * H + 4 + (GP >> 4), ks = (GP >> 2) & 3, db = GP & 3;
  s.vlo[GP] = __builtin_amdgcn_ds_read_tr16_b64_v4i16((lds_s4p)(s.va[2 * db] + 16384u * ((I % 6) % 3) + 4096u * ks));
  s.vhi[GP] = __builtin_amdgcn_ds_read_tr16_b64_v4i16((lds_s4p)(s.va[2 * db + 1] + 16384u * ((I % 6) % 3) + 4096u * ks + 2048u));
}
template <int IPREV, int INEXT> DI void rebase(unsigned (&a)[8]) {
  if constexpr ((IPREV % 6) / 3 != (INEXT % 6) / 3) {
#pragma unroll
    for (int i = 0; i < 8; ++i) a[i] = opaque((INEXT % 6) >= 3 ? a[i] + 49152u : a[i] - 49152u);
  }
}
DI void va_init(const Ctx& c, Hd& s, unsigned base) {
  const unsigned lane = opaque(c.lane), h = lane >> 5, blk = (lane >> 4) & 1u, q4 = (lane & 15u) >> 2, p = lane & 3u;
  const unsigned vb = 256u * (4u * h + q4) + 16u * (4u * q4 + ((2u * blk + (p >> 1)) ^ h)) + 8u * (p & 1u);
#pragma unroll
  for (int i = 0; i < 8; ++i) s.va[i] = opaque((vb ^ (64u * (i >> 1)) ^ (32u * (i & 1))) + base);
}
template <int N> DI void pair_a(Hd& s) {
  constexpr int ks = N >> 2, kb = ks >> 1, r = 8 * (ks & 1) + 2 * (N & 3);
  s.e[N][0] = __builtin_amdgcn_exp2f(__builtin_fmaf(s.S[kb][r], CEXP, s.mc));
  s.e[N][1] = __builtin_amdgcn_exp2f(__builtin_fmaf(s.S[kb][r + 1], CEXP, s.mc));
  PIN(s.e[N][0]); PIN(s.e[N][1]);
}
template <int N> DI void pair_b(Hd& s) {
  if constexpr (N == 0) { s.l[0] = s.e[N][0]; s.l[1] = s.e[N][1]; }
  else { s.l[0] += s.e[N][0]; s.l[1] += s.e[N][1]; }
  unsigned w = cvtpk(s.e[N][0], s.e[N][1]);
  PIN(s.l[0]); PIN(s.l[1]); PIN(w);
  s.P[N >> 2][N & 3] = w;
}
constexpr int pair_gap(int n) { return n < 32 ? 16 + (3 * n) / 2 : 64 + ((n - 32) * 57) / 32; }
constexpr int pair_at(int g) { for (int n = 0; n < 64; ++n) if (pair_gap(n) == g) return n; return -1; }
template <int I> DI void pack_item(const f32x16 (&o)[4], float il, unsigned ow) {
  constexpr int db = I >> 2, g = I & 3;
  const u32x2 w = {cvtpk(o[db][4 * g] * il, o[db][4 * g + 1] * il), cvtpk(o[db][4 * g + 2] * il, o[db][4 * g + 3] * il)};
  *(lds_u2p)(ow ^ (16u * I)) = w;
}
DI unsigned stage_base(const Ctx& c) { const unsigned lane = opaque(c.lane), l32 = lane & 31u, h = lane >> 5; return RQO + c.wid * 8192u + l32 * 256u + 16u * (l32 & 7u) + 8u * h; }

template <int H, int G> DI void gap(const Ctx& c, Hd& s, const bf16x8 (&q)[8], const Prev& pv, Stamps& st) {
  constexpr int R = 8 * H;
  if constexpr ((G + BOFF) % 16 == 0 && G + BOFF >= 16 && G + BOFF <= 112) wait_bar<younger(0, R + (G + BOFF) / 16, 1000 * H + G + 1)>();
  events_at<1000 * H + G + 1>(c, std::make_integer_sequence<int, NEV>{});
  if constexpr (G < 64) {
    if constexpr (G + PFK < 64) {
      if constexpr (((G + PFK) & 15) == 0) rebase<R + ((G + PFK) >> 4) - 1, R + ((G + PFK) >> 4)>(s.ka);
      k_read<H, G + PFK>(s);
    }
    if constexpr (G == 60) va_init(c, s, RING + (((R + 4) % 6) >= 3 ? 49152u : 0u));
    if constexpr (G + PFV >= 64) v_read<H, G + PFV - 64>(s);
    constexpr int kb = G >> 3, d0 = G & 7;
    if constexpr (d0 == 0) s.S[kb] = __builtin_amdgcn_mfma_f32_32x32x16_bf16(s.kf[G], q[d0], f32x16{}, 0, 0, 0);
    else                   s.S[kb] = __builtin_amdgcn_mfma_f32_32x32x16_bf16(s.kf[G], q[d0], s.S[kb], 0, 0, 0);
    PIN(s.S[kb]);
    if constexpr (G == 10) { s.mx[0] = fmax3(s.S[0][0], s.S[0][1], s.S[0][2]); s.mx[1] = fmax3(s.S[0][3], s.S[0][4], s.S[0][5]); PIN(s.mx[0]); PIN(s.mx[1]); }
    if constexpr (G == 11) { s.mx[0] = fmax3(s.mx[0], s.S[0][6], s.S[0][7]); s.mx[1] = fmax3(s.mx[1], s.S[0][8], s.S[0][9]); PIN(s.mx[0]); PIN(s.mx[1]); }
    if constexpr (G == 12) { s.mx[0] = fmax3(s.mx[0], s.S[0][10], s.S[0][11]); s.mx[1] = fmax3(s.mx[1], s.S[0][12], s.S[0][13]); PIN(s.mx[0]); PIN(s.mx[1]); }
    if constexpr (G == 13) { s.mx[0] = fmax3(s.mx[0], s.S[0][14], s.S[0][15]); s.mx[0] = fmax2(s.mx[0], s.mx[1]); PIN(s.mx[0]); }
    if constexpr (G == 14) { s.mc = -xhalf_max(s.mx[0]) * CEXP; PIN(s.mc); }
    if constexpr (H == 1 && G < 16) pack_item<G>(pv.o, pv.il, pv.ow);
  } else {
    constexpr int GP = G - 64, ks = GP >> 2, db = GP & 3;
    if constexpr (GP + PFV < 64) {
      if constexpr (((GP + PFV) & 15) == 0) rebase<R + 4 + ((GP + PFV) >> 4) - 1, R + 4 + ((GP + PFV) >> 4)>(s.va);
      v_read<H, GP + PFV>(s);
    }
    const s16x8 vf = __builtin_shufflevector(s.vlo[GP], s.vhi[GP], 0, 1, 2, 3, 4, 5, 6, 7);
    if constexpr (ks == 0) s.o[db] = __builtin_amdgcn_mfma_f32_32x32x16_bf16(__builtin_bit_cast(bf16x8, vf), __builtin_bit_cast(bf16x8, s.P[0]), f32x16{}, 0, 0, 0);
    else                   s.o[db] = __builtin_amdgcn_mfma_f32_32x32x16_bf16(__builtin_bit_cast(bf16x8, vf), __builtin_bit_cast(bf16x8, s.P[ks]), s.o[db], 0, 0, 0);
    PIN(s.o[db]);
  }
  if constexpr (pair_at(G - 1) >= 0) pair_b<pair_at(G - 1)>(s);
  if constexpr (pair_at(G) >= 0) pair_a<pair_at(G)>(s);
  GAP();
}
template <int H, int... Gs> DI void gaps(const Ctx& c, Hd& s, const bf16x8 (&q)[8], const Prev& pv, Stamps& st, std::integer_sequence<int, Gs...>) { (gap<H, Gs>(c, s, q, pv, st), ...); }

__device__ __forceinline__ void fallback(const Ctx& c, unsigned hoff, f32x16 (&o)[4], float& l) {
  const unsigned lane = c.lane, l32 = lane & 31u, h = lane >> 5;
  const char* qp = c.pq + hoff + (c.wid * 32u + l32) * 256u + h * 16u;
  const char* kp = c.pk + hoff + l32 * 256u + h * 16u;
  const short* vp = (const short*)(c.pv + hoff) + l32;
  bf16x8 qf[8];
#pragma unroll
  for (int d = 0; d < 8; ++d) qf[d] = *(const bf16x8*)(qp + 32 * d);
  float m = -__builtin_inff();
#pragma nounroll
  for (int kb = 0; kb < 8; ++kb) {
    f32x16 a = {};
#pragma unroll
    for (int d = 0; d < 8; ++d) a = __builtin_amdgcn_mfma_f32_32x32x16_bf16(*(const bf16x8*)(kp + kb * 8192 + 32 * d), qf[d], a, 0, 0, 0);
#pragma unroll
    for (int r = 0; r < 16; ++r) m = fmax2(m, a[r]);
  }
  m = xhalf_max(m);
  float ls = 0.f;
#pragma unroll
  for (int db = 0; db < 4; ++db) o[db] = f32x16{};
#pragma nounroll
  for (int kb = 0; kb < 8; ++kb) {
    f32x16 a = {};
#pragma unroll
    for (int d = 0; d < 8; ++d) a = __builtin_amdgcn_mfma_f32_32x32x16_bf16(*(const bf16x8*)(kp + kb * 8192 + 32 * d), qf[d], a, 0, 0, 0);
#pragma unroll
    for (int sx = 0; sx < 2; ++sx) {
      float e[8];
#pragma unroll
      for (int j = 0; j < 8; ++j) { e[j] = __builtin_amdgcn_exp2f((a[8 * sx + j] - m) * CEXP); ls += e[j]; }
      const u32x4 p = {cvtpk(e[0], e[1]), cvtpk(e[2], e[3]), cvtpk(e[4], e[5]), cvtpk(e[6], e[7])};
#pragma unroll
      for (int db = 0; db < 4; ++db) {
        s16x8 vf;
#pragma unroll
        for (int j = 0; j < 8; ++j) vf[j] = vp[(kb * 32 + 16 * sx + 8 * (j >> 2) + 4 * h + (j & 3)) * 128 + 32 * db];
        o[db] = __builtin_amdgcn_mfma_f32_32x32x16_bf16(__builtin_bit_cast(bf16x8, vf), __builtin_bit_cast(bf16x8, p), o[db], 0, 0, 0);
      }
    }
  }
  l = xhalf_sum(ls);
}

template <int H>
DI void head(const Ctx& c, bf16x8 (&q)[8], Prev& pv, Stamps& st) {
  constexpr int R = 8 * H;
  Hd s;
  { const unsigned ra = row_base(opaque(c.lane));
#pragma unroll
    for (int d0 = 0; d0 < 8; ++d0) s.ka[d0] = opaque((ra ^ (32u * d0)) + RING + ((R % 6) >= 3 ? 49152u : 0u)); }
  events_at<1000 * H>(c, std::make_integer_sequence<int, NEV>{});
  GAP();
  wait_bar<younger(0, R, 1000 * H + 1)>();
  GAP();
  k_read<H, 0>(s); k_read<H, 1>(s); k_read<H, 2>(s); k_read<H, 3>(s);
  GAP();
  gaps<H>(c, s, q, pv, st, std::make_integer_sequence<int, 128>{});
  ST(1 + 10 * H + 8);
  float l = xhalf_sum(s.l[0] + s.l[1]);
  constexpr float LIM_HI = (float)(1ull << LIMLOG), LIM_LO = 1.0f / LIM_HI;
  if (__builtin_expect(__any(!(l <= LIM_HI && l >= LIM_LO)), 0)) fallback(c, H ? c.hB : c.hA, s.o, l);
  const float il = __builtin_amdgcn_rcpf(l);
  if constexpr (H == 0) {
#pragma unroll
    for (int db = 0; db < 4; ++db) pv.o[db] = s.o[db];
    pv.il = il; pv.ow = stage_base(c);
    GAP();
    wait_vm<younger(1, 3, 1000)>();
    GAP();
    read_q(c, q);
  } else {
    const unsigned ow = stage_base(c);
    pack_item<0>(s.o, il, ow); pack_item<1>(s.o, il, ow); pack_item<2>(s.o, il, ow); pack_item<3>(s.o, il, ow);
    pack_item<4>(s.o, il, ow); pack_item<5>(s.o, il, ow); pack_item<6>(s.o, il, ow); pack_item<7>(s.o, il, ow);
    pack_item<8>(s.o, il, ow); pack_item<9>(s.o, il, ow); pack_item<10>(s.o, il, ow); pack_item<11>(s.o, il, ow);
    pack_item<12>(s.o, il, ow); pack_item<13>(s.o, il, ow); pack_item<14>(s.o, il, ow); pack_item<15>(s.o, il, ow);
    store_o<0, 8>(c, c.hB);
  }
  GAP();
}

__global__ __launch_bounds__(512, 2) void attn_fwd(const void* __restrict__ Q, const void* __restrict__ K, const void* __restrict__ V, void* __restrict__ O, unsigned* __restrict__ dbg) {
  Stamps st;
  ST(0);
  Ctx c;
  const unsigned tid = threadIdx.x, lane = tid & 63u;
  c.lane = lane;
  c.wid = __builtin_amdgcn_readfirstlane(tid >> 6);
  const int nbytes = NHEADS * (int)HEADB;
  c.sq = __builtin_amdgcn_make_buffer_rsrc((void*)Q, (short)0, nbytes, 0x00020000);
  c.sk = __builtin_amdgcn_make_buffer_rsrc((void*)K, (short)0, nbytes, 0x00020000);
  c.sv = __builtin_amdgcn_make_buffer_rsrc((void*)V, (short)0, nbytes, 0x00020000);
  c.so = __builtin_amdgcn_make_buffer_rsrc((void*)O, (short)0, nbytes, 0x00020000);
  c.pq = (const char*)Q; c.pk = (const char*)K; c.pv = (const char*)V;
  {
    const unsigned r = lane >> 4, p = lane & 15u;
    c.v0 = r * 256u + 16u * (p ^ (r << 2));
  }
  { const unsigned b = blockIdx.x;
    const unsigned h0 = HMAP == 0 ? b : (HMAP == 1 ? 2u * b : (b & 7u) * 64u + (b >> 3) * 2u);
    c.hA = h0 * HEADB; c.hB = (h0 + (HMAP == 0 ? (unsigned)NWG : 1u)) * HEADB; }
  asm volatile("s_nop 4");
  dma_q<0>(c, c.hA); dma_q<1>(c, c.hA); dma_q<2>(c, c.hA); dma_q<3>(c, c.hA);
  dma_ring<0>(c); dma_ring<1>(c);
  bf16x8 q[8];
  GAP();
  wait_vm<4>();
  GAP();
  read_q(c, q);
  Prev pv;
  head<0>(c, q, pv, st);
  head<1>(c, q, pv, st);
}

extern "C" void kernel_launch(void* const* d_in, const int* in_sizes, int n_in, void* d_out, int out_size, void* d_ws, size_t ws_size, hipStream_t stream) {
  (void)in_sizes; (void)n_in; (void)out_size; (void)ws_size;
  (void)hipFuncSetAttribute((const void*)attn_fwd, hipFuncAttributeMaxDynamicSharedMemorySize, (int)LDS_BYTES);
  hipLaunchKernelGGL(attn_fwd, dim3(NWG), dim3(512), LDS_BYTES, stream, d_in[0], d_in[1], d_in[2], d_out, (unsigned*)d_ws);
}
```

```cpp
#include <hip/hip_runtime.h>
#include <hip/hip_bf16.h>
#include <utility>
#define STAMPS
#define RSTAMP
#define SIDE 0

typedef float    f32x16 __attribute__((ext_vector_type(16)));
typedef float    f32x2  __attribute__((ext_vector_type(2)));
typedef __bf16   bf16x8 __attribute__((ext_vector_type(8)));
typedef __bf16   bf16x2 __attribute__((ext_vector_type(2)));
typedef short    s16x4  __attribute__((ext_vector_type(4)));
typedef short    s16x8  __attribute__((ext_vector_type(8)));
typedef unsigned u32x4  __attribute__((ext_vector_type(4)));
typedef unsigned u32x2  __attribute__((ext_vector_type(2)));
typedef __attribute__((address_space(3))) const bf16x8* lds_b8p;
typedef __attribute__((address_space(3))) s16x4* lds_s4p;
typedef __attribute__((address_space(3))) u32x2* lds_u2p;
typedef __attribute__((address_space(3))) const u32x4* lds_u4p;

#define DI __device__ __forceinline__
#define GAP() __builtin_amdgcn_sched_barrier(0)
#define PIN(x) asm volatile("" : "+v"(x))
#define LDPOL " nt"
#define STAUX 18
#define BOFF 8
#define HMAP 2
#define LIMLOG 30

constexpr unsigned HEADB = 65536u;
constexpr unsigned RQO = 0u, RING = 65536u, LDS_BYTES = 163840u;
constexpr int NHEADS = 512, NWG = 256;
constexpr float CEXP = 0.08838834764831845f * 1.4426950408889634f;
constexpr int PFK = 4, PFV = 3;

DI unsigned cvtpk(float lo, float hi) { f32x2 v = {lo, hi}; bf16x2 b = __builtin_convertvector(v, bf16x2); return __builtin_bit_cast(unsigned, b); }
DI float fmax2(float a, float b) { return __builtin_elementwise_maximum(a, b); }
DI float fmax3(float a, float b, float c) { return __builtin_elementwise_maximum(__builtin_elementwise_maximum(a, b), c); }
DI unsigned opaque(unsigned x) { asm volatile("" : "+v"(x)); return x; }
DI float xhalf_max(float m) { auto rr = __builtin_amdgcn_permlane32_swap(__float_as_uint(m), __float_as_uint(m), false, false); return fmax2(__uint_as_float(rr[0]), __uint_as_float(rr[1])); }
DI float xhalf_sum(float m) { auto rr = __builtin_amdgcn_permlane32_swap(__float_as_uint(m), __float_as_uint(m), false, false); return __uint_as_float(rr[0]) + __uint_as_float(rr[1]); }

#define NST 40
struct Stamps { unsigned t[NST]; };
DI unsigned stamp_now() { unsigned long long t; GAP();
  asm volatile("s_memrealtime %0\n\ts_waitcnt lgkmcnt(0)" : "=s"(t) :: "memory");
  GAP(); return (unsigned)t; }
#define ST(i) (st.t[i] = stamp_now())

DI void dma2(unsigned m0v, unsigned v0, unsigned v1, __amdgpu_buffer_rsrc_t srd, unsigned soff) {
  asm volatile("s_mov_b32 m0, %0\n\ts_nop 0\n\t"
               "buffer_load_dwordx4 %1, %3, %4 offen" LDPOL " lds\n\t"
               "buffer_load_dwordx4 %2, %3, %4 offen offset:1024" LDPOL " lds"
               :: "s"(m0v), "v"(v0), "v"(v1), "s"(srd), "s"(soff) : "memory");
}
template <int N> DI void wait_bar() { asm volatile("s_waitcnt vmcnt(%0)\n\ts_barrier" :: "i"(N) : "memory"); }
template <int N> DI void wait_vm() { asm volatile("s_waitcnt vmcnt(%0)" :: "i"(N) : "memory"); }

struct Ctx {
  __amdgpu_buffer_rsrc_t sq, sk, sv, so;
  const char *pq, *pk, *pv;
  unsigned wid, lane;
  unsigned v0;
  unsigned hA, hB;
};
constexpr unsigned slot_of(int i) { return RING + 16384u * (unsigned)(i % 6); }

struct Ev { int pos, kind, idx; };
#define EVTAB \
  {0, 0, 2}, {8 + 1, 0, 3}, {24 + 1, 0, 4}, {40 + 1, 0, 5}, {56 + 1, 0, 6}, {64 + 1, 1, 0}, {72 + 1, 0, 7}, {80 + 1, 1, 1}, {88 + 1, 1, 2}, {96 + 1, 1, 3}, \
  {104 + 1, 0, 8}, {112 + 1, 0, 9}, {120 + 1, 0, 10}, \
  {1000, 0, 11}, {1000 + 0 + 1, 0, 12}, {1000 + 8 + 1, 0, 13}, {1000 + 32 + 1, 0, 14}, {1000 + 52 + 1, 0, 15}, \
  {1000 + 64 + 1, 2, 0}, {1000 + 76 + 1, 2, 1}, {1000 + 88 + 1, 2, 2}, {1000 + 100 + 1, 2, 3}
constexpr Ev EV[] = {EVTAB};
constexpr int NEV = sizeof(EV) / sizeof(EV[0]);
constexpr int younger(int kind, int idx, int pos) {
  int e0 = -1;
  for (int e = 0; e < NEV; ++e) if (EV[e].kind == kind && EV[e].idx == idx) e0 = e;
  int n = 0;
  if (e0 < 0) n = (kind == 0 && idx == 0) ? 2 : 0;
  for (int e = e0 + 1; e < NEV; ++e) if (EV[e].pos < pos) n += 2;
  return n;
}

template <int I> DI void dma_ring(const Ctx& c) {
  const unsigned w2 = (32u * c.wid) & 48u;
  dma2(slot_of(I) + c.wid * 2048u, c.v0 ^ w2, c.v0 ^ (w2 + 16u), (I & 4) ? c.sv : c.sk, ((I & 8) ? c.hB : c.hA) + 16384u * (I & 3) + c.wid * 2048u);
}
template <int T> DI void dma_q(const Ctx& c, unsigned hoff) {
  const unsigned o = c.wid * 8192u + 2048u * T;
  dma2(RQO + o, c.v0 ^ (16u * ((2 * T) & 3)), c.v0 ^ (16u * ((2 * T + 1) & 3)), c.sq, hoff + o);
}
DI unsigned row_base(unsigned lane) { const unsigned l32 = lane & 31u, h = lane >> 5, f = ((l32 & 3u) << 2) | ((l32 >> 2) & 3u); return l32 * 256u + 16u * (f ^ h); }
DI void read_q(const Ctx& c, bf16x8 (&q)[8]) {
  const unsigned b = row_base(opaque(c.lane)) + RQO + c.wid * 8192u;
#pragma unroll
  for (int d0 = 0; d0 < 8; ++d0) q[d0] = *(lds_b8p)(b ^ (32u * d0));
}
template <int I0, int I1> DI void store_o(const Ctx& c, unsigned hoff) {
  const unsigned lane = opaque(c.lane), rr = lane >> 4, cc = lane & 15u;
  const unsigned b0 = RQO + c.wid * 8192u + rr * 256u + 16u * (cc ^ rr), b1 = b0 ^ 64u;
  u32x4 w[8];
#pragma unroll
  for (int i = I0; i < I1; ++i) w[i] = *(lds_u4p)(((i & 1) ? b1 : b0) + 1024u * i);
#pragma unroll
  for (int i = I0; i < I1; ++i) __builtin_amdgcn_raw_buffer_store_b128(w[i], c.so, lane * 16u + 1024u * i, hoff + c.wid * 8192u, STAUX);
}
template <int E> DI void run_event(const Ctx& c) {
  if constexpr (EV[E].kind == 0) dma_ring<EV[E].idx>(c);
  else if constexpr (EV[E].kind == 1) dma_q<EV[E].idx>(c, c.hB);
  else store_o<2 * EV[E].idx, 2 * EV[E].idx + 2>(c, c.hA);
}
template <int POS, int... Es> DI void events_at(const Ctx& c, std::integer_sequence<int, Es...>) {
  ([&]() { if constexpr (EV[Es].pos == POS) run_event<Es>(c); }(), ...);
}

struct Hd {
  f32x16 S[8], o[4];
  bf16x8 kf[64];
  s16x4 vlo[64], vhi[64];
  u32x4 P[16];
  unsigned ka[8], va[8];
  float mx[2], l[2], mc;
  float e[64][2];
};
struct Prev { f32x16 o[4]; float il; unsigned ow; };

template <int H, int G> DI void k_read(Hd& s) {
  constexpr int I = 8 * H + (G >> 4);
  s.kf[G] = *(lds_b8p)(s.ka[G & 7] + 16384u * ((I % 6) % 3) + 8192u * ((G >> 3) & 1));
}
template <int H, int GP> DI void v_read(Hd& s) {
  constexpr int I = 8 * H + 4 + (GP >> 4), ks = (GP >> 2) & 3, db = GP & 3;
  s.vlo[GP] = __builtin_amdgcn_ds_read_tr16_b64_v4i16((lds_s4p)(s.va[2 * db] + 16384u * ((I % 6) % 3) + 4096u * ks));
  s.vhi[GP] = __builtin_amdgcn_ds_read_tr16_b64_v4i16((lds_s4p)(s.va[2 * db + 1] + 16384u * ((I % 6) % 3) + 4096u * ks + 2048u));
}
template <int IPREV, int INEXT> DI void rebase(unsigned (&a)[8]) {
  if constexpr ((IPREV % 6) / 3 != (INEXT % 6) / 3) {
#pragma unroll
    for (int i = 0; i < 8; ++i) a[i] = opaque((INEXT % 6) >= 3 ? a[i] + 49152u : a[i] - 49152u);
  }
}
DI void va_init(const Ctx& c, Hd& s, unsigned base) {
  const unsigned lane = opaque(c.lane), h = lane >> 5, blk = (lane >> 4) & 1u, q4 = (lane & 15u) >> 2, p = lane & 3u;
  const unsigned vb = 256u * (4u * h + q4) + 16u * (4u * q4 + ((2u * blk + (p >> 1)) ^ h)) + 8u * (p & 1u);
#pragma unroll
  for (int i = 0; i < 8; ++i) s.va[i] = opaque((vb ^ (64u * (i >> 1)) ^ (32u * (i & 1))) + base);
}
template <int N> DI void pair_a(Hd& s) {
  constexpr int ks = N >> 2, kb = ks >> 1, r = 8 * (ks & 1) + 2 * (N & 3);
  s.e[N][0] = __builtin_amdgcn_exp2f(__builtin_fmaf(s.S[kb][r], CEXP, s.mc));
  s.e[N][1] = __builtin_amdgcn_exp2f(__builtin_fmaf(s.S[kb][r + 1], CEXP, s.mc));
  PIN(s.e[N][0]); PIN(s.e[N][1]);
}
template <int N> DI void pair_b(Hd& s) {
  if constexpr (N == 0) { s.l[0] = s.e[N][0]; s.l[1] = s.e[N][1]; }
  else { s.l[0] += s.e[N][0]; s.l[1] += s.e[N][1]; }
  unsigned w = cvtpk(s.e[N][0], s.e[N][1]);
  PIN(s.l[0]); PIN(s.l[1]); PIN(w);
  s.P[N >> 2][N & 3] = w;
}
constexpr int pair_gap(int n) { return n < 32 ? 16 + (3 * n) / 2 : 64 + ((n - 32) * 57) / 32; }
constexpr int pair_at(int g) { for (int n = 0; n < 64; ++n) if (pair_gap(n) == g) return n; return -1; }
template <int I> DI void pack_item(const f32x16 (&o)[4], float il, unsigned ow) {
  constexpr int db = I >> 2, g = I & 3;
  const u32x2 w = {cvtpk(o[db][4 * g] * il, o[db][4 * g + 1] * il), cvtpk(o[db][4 * g + 2] * il, o[db][4 * g + 3] * il)};
  *(lds_u2p)(ow ^ (16u * I)) = w;
}
DI unsigned stage_base(const Ctx& c) { const unsigned lane = opaque(c.lane), l32 = lane & 31u, h = lane >> 5; return RQO + c.wid * 8192u + l32 * 256u + 16u * (l32 & 7u) + 8u * h; }

template <int H, int G> DI void gap(const Ctx& c, Hd& s, const bf16x8 (&q)[8], const Prev& pv, Stamps& st) {
  constexpr int R = 8 * H;
  if constexpr (G % 16 == 0) ST(1 + 10 * H + G / 16);
  if constexpr ((G + BOFF) % 16 == 0 && G + BOFF >= 16 && G + BOFF <= 112) wait_bar<younger(0, R + (G + BOFF) / 16, 1000 * H + G + 1)>();
  events_at<1000 * H + G + 1>(c, std::make_integer_sequence<int, NEV>{});
  if constexpr (G < 64) {
    if constexpr (G + PFK < 64) {
      if constexpr (((G + PFK) & 15) == 0) rebase<R + ((G + PFK) >> 4) - 1, R + ((G + PFK) >> 4)>(s.ka);
      k_read<H, G + PFK>(s);
    }
    if constexpr (G == 60) va_init(c, s, RING + (((R + 4) % 6) >= 3 ? 49152u : 0u));
    if constexpr (G + PFV >= 64) v_read<H, G + PFV - 64>(s);
    constexpr int kb = G >> 3, d0 = G & 7;
    if constexpr (d0 == 0) s.S[kb] = __builtin_amdgcn_mfma_f32_32x32x16_bf16(s.kf[G], q[d0], f32x16{}, 0, 0, 0);
    else                   s.S[kb] = __builtin_amdgcn_mfma_f32_32x32x16_bf16(s.kf[G], q[d0], s.S[kb], 0, 0, 0);
    PIN(s.S[kb]);
    if constexpr (G == 10) { s.mx[0] = fmax3(s.S[0][0], s.S[0][1], s.S[0][2]); s.mx[1] = fmax3(s.S[0][3], s.S[0][4], s.S[0][5]); PIN(s.mx[0]); PIN(s.mx[1]); }
    if constexpr (G == 11) { s.mx[0] = fmax3(s.mx[0], s.S[0][6], s.S[0][7]); s.mx[1] = fmax3(s.mx[1], s.S[0][8], s.S[0][9]); PIN(s.mx[0]); PIN(s.mx[1]); }
    if constexpr (G == 12) { s.mx[0] = fmax3(s.mx[0], s.S[0][10], s.S[0][11]); s.mx[1] = fmax3(s.mx[1], s.S[0][12], s.S[0][13]); PIN(s.mx[0]); PIN(s.mx[1]); }
    if constexpr (G == 13) { s.mx[0] = fmax3(s.mx[0], s.S[0][14], s.S[0][15]); s.mx[0] = fmax2(s.mx[0], s.mx[1]); PIN(s.mx[0]); }
    if constexpr (G == 14) { s.mc = -xhalf_max(s.mx[0]) * CEXP; PIN(s.mc); }
    if constexpr (H == 1 && G < 16) pack_item<G>(pv.o, pv.il, pv.ow);
  } else {
    constexpr int GP = G - 64, ks = GP >> 2, db = GP & 3;
    if constexpr (GP + PFV < 64) {
      if constexpr (((GP + PFV) & 15) == 0) rebase<R + 4 + ((GP + PFV) >> 4) - 1, R + 4 + ((GP + PFV) >> 4)>(s.va);
      v_read<H, GP + PFV>(s);
    }
    const s16x8 vf = __builtin_shufflevector(s.vlo[GP], s.vhi[GP], 0, 1, 2, 3, 4, 5, 6, 7);
    if constexpr (ks == 0) s.o[db] = __builtin_amdgcn_mfma_f32_32x32x16_bf16(__builtin_bit_cast(bf16x8, vf), __builtin_bit_cast(bf16x8, s.P[0]), f32x16{}, 0, 0, 0);
    else                   s.o[db] = __builtin_amdgcn_mfma_f32_32x32x16_bf16(__builtin_bit_cast(bf16x8, vf), __builtin_bit_cast(bf16x8, s.P[ks]), s.o[db], 0, 0, 0);
    PIN(s.o[db]);
  }
  if constexpr (pair_at(G - 1) >= 0) pair_b<pair_at(G - 1)>(s);
  if constexpr (pair_at(G) >= 0) pair_a<pair_at(G)>(s);
  GAP();
}
template <int H, int... Gs> DI void gaps(const Ctx& c, Hd& s, const bf16x8 (&q)[8], const Prev& pv, Stamps& st, std::integer_sequence<int, Gs...>) { (gap<H, Gs>(c, s, q, pv, st), ...); }

__device__ __forceinline__ void fallback(const Ctx& c, unsigned hoff, f32x16 (&o)[4], float& l) {
  const unsigned lane = c.lane, l32 = lane & 31u, h = lane >> 5;
  const char* qp = c.pq + hoff + (c.wid * 32u + l32) * 256u + h * 16u;
  const char* kp = c.pk + hoff + l32 * 256u + h * 16u;
  const short* vp = (const short*)(c.pv + hoff) + l32;
  bf16x8 qf[8];
#pragma unroll
  for (int d = 0; d < 8; ++d) qf[d] = *(const bf16x8*)(qp + 32 * d);
  float m = -__builtin_inff();
#pragma nounroll
  for (int kb = 0; kb < 8; ++kb) {
    f32x16 a = {};
#pragma unroll
    for (int d = 0; d < 8; ++d) a = __builtin_amdgcn_mfma_f32_32x32x16_bf16(*(const bf16x8*)(kp + kb * 8192 + 32 * d), qf[d], a, 0, 0, 0);
#pragma unroll
    for (int r = 0; r < 16; ++r) m = fmax2(m, a[r]);
  }
  m = xhalf_max(m);
  float ls = 0.f;
#pragma unroll
  for (int db = 0; db < 4; ++db) o[db] = f32x16{};
#pragma nounroll
  for (int kb = 0; kb < 8; ++kb) {
    f32x16 a = {};
#pragma unroll
    for (int d = 0; d < 8; ++d) a = __builtin_amdgcn_mfma_f32_32x32x16_bf16(*(const bf16x8*)(kp + kb * 8192 + 32 * d), qf[d], a, 0, 0, 0);
#pragma unroll
    for (int sx = 0; sx < 2; ++sx) {
      float e[8];
#pragma unroll
      for (int j = 0; j < 8; ++j) { e[j] = __builtin_amdgcn_exp2f((a[8 * sx + j] - m) * CEXP); ls += e[j]; }
      const u32x4 p = {cvtpk(e[0], e[1]), cvtpk(e[2], e[3]), cvtpk(e[4], e[5]), cvtpk(e[6], e[7])};
#pragma unroll
      for (int db = 0; db < 4; ++db) {
        s16x8 vf;
#pragma unroll
        for (int j = 0; j < 8; ++j) vf[j] = vp[(kb * 32 + 16 * sx + 8 * (j >> 2) + 4 * h + (j & 3)) * 128 + 32 * db];
        o[db] = __builtin_amdgcn_mfma_f32_32x32x16_bf16(__builtin_bit_cast(bf16x8, vf), __builtin_bit_cast(bf16x8, p), o[db], 0, 0, 0);
      }
    }
  }
  l = xhalf_sum(ls);
}

template <int H>
DI void head(const Ctx& c, bf16x8 (&q)[8], Prev& pv, Stamps& st) {
  constexpr int R = 8 * H;
  Hd s;
  { const unsigned ra = row_base(opaque(c.lane));
#pragma unroll
    for (int d0 = 0; d0 < 8; ++d0) s.ka[d0] = opaque((ra ^ (32u * d0)) + RING + ((R % 6) >= 3 ? 49152u : 0u)); }
  events_at<1000 * H>(c, std::make_integer_sequence<int, NEV>{});
  GAP();
  wait_bar<younger(0, R, 1000 * H + 1)>();
  GAP();
  k_read<H, 0>(s); k_read<H, 1>(s); k_read<H, 2>(s); k_read<H, 3>(s);
  GAP();
  gaps<H>(c, s, q, pv, st, std::make_integer_sequence<int, 128>{});
  ST(1 + 10 * H + 8);
  float l = xhalf_sum(s.l[0] + s.l[1]);
  constexpr float LIM_HI = (float)(1ull << LIMLOG), LIM_LO = 1.0f / LIM_HI;
  if (__builtin_expect(__any(!(l <= LIM_HI && l >= LIM_LO)), 0)) fallback(c, H ? c.hB : c.hA, s.o, l);
  const float il = __builtin_amdgcn_rcpf(l);
  if constexpr (H == 0) {
#pragma unroll
    for (int db = 0; db < 4; ++db) pv.o[db] = s.o[db];
    pv.il = il; pv.ow = stage_base(c);
    GAP();
    wait_vm<younger(1, 3, 1000)>();
    GAP();
    read_q(c, q);
  } else {
    const unsigned ow = stage_base(c);
    pack_item<0>(s.o, il, ow); pack_item<1>(s.o, il, ow); pack_item<2>(s.o, il, ow); pack_item<3>(s.o, il, ow);
    pack_item<4>(s.o, il, ow); pack_item<5>(s.o, il, ow); pack_item<6>(s.o, il, ow); pack_item<7>(s.o, il, ow);
    pack_item<8>(s.o, il, ow); pack_item<9>(s.o, il, ow); pack_item<10>(s.o, il, ow); pack_item<11>(s.o, il, ow);
    pack_item<12>(s.o, il, ow); pack_item<13>(s.o, il, ow); pack_item<14>(s.o, il, ow); pack_item<15>(s.o, il, ow);
    store_o<0, 8>(c, c.hB);
  }
  GAP();
}

__global__ __launch_bounds__(512, 2) void attn_fwd(const void* __restrict__ Q, const void* __restrict__ K, const void* __restrict__ V, void* __restrict__ O, unsigned* __restrict__ dbg) {
  Stamps st;
  unsigned long long rt0; asm volatile("s_memrealtime %0\n\ts_waitcnt lgkmcnt(0)" : "=s"(rt0) :: "memory");
  for (int i = 0; i < NST; ++i) st.t[i] = 0;
  ST(0);
  Ctx c;
  const unsigned tid = threadIdx.x, lane = tid & 63u;
  c.lane = lane;
  c.wid = __builtin_amdgcn_readfirstlane(tid >> 6);
  const int nbytes = NHEADS * (int)HEADB;
  c.sq = __builtin_amdgcn_make_buffer_rsrc((void*)Q, (short)0, nbytes, 0x00020000);
  c.sk = __builtin_amdgcn_make_buffer_rsrc((void*)K, (short)0, nbytes, 0x00020000);
  c.sv = __builtin_amdgcn_make_buffer_rsrc((void*)V, (short)0, nbytes, 0x00020000);
  c.so = __builtin_amdgcn_make_buffer_rsrc((void*)O, (short)0, nbytes, 0x00020000);
  c.pq = (const char*)Q; c.pk = (const char*)K; c.pv = (const char*)V;
  {
    const unsigned r = lane >> 4, p = lane & 15u;
    c.v0 = r * 256u + 16u * (p ^ (r << 2));
  }
  { const unsigned b = blockIdx.x;
    const unsigned h0 = HMAP == 0 ? b : (HMAP == 1 ? 2u * b : (b & 7u) * 64u + (b >> 3) * 2u);
    c.hA = h0 * HEADB; c.hB = (h0 + (HMAP == 0 ? (unsigned)NWG : 1u)) * HEADB; }
  asm volatile("s_nop 4");
  dma_q<0>(c, c.hA); dma_q<1>(c, c.hA); dma_q<2>(c, c.hA); dma_q<3>(c, c.hA);
  dma_ring<0>(c); dma_ring<1>(c);
  bf16x8 q[8];
  GAP();
  wait_vm<4>();
  GAP();
  read_q(c, q);
  Prev pv;
  head<0>(c, q, pv, st);
  head<1>(c, q, pv, st);
  ST(21);
  asm volatile("s_waitcnt vmcnt(0)" ::: "memory");
  ST(22);
  unsigned long long rt1; asm volatile("s_memrealtime %0\n\ts_waitcnt lgkmcnt(0)" : "=s"(rt1) :: "memory");
  st.t[38] = (unsigned)rt0; st.t[39] = (unsigned)rt1;
  { const unsigned target = 6000u + (SIDE == 0 ? 0u : st.t[SIDE] - (unsigned)rt0);
    for (;;) { unsigned long long now; asm volatile("s_memrealtime %0\n\ts_waitcnt lgkmcnt(0)" : "=s"(now) :: "memory"); if ((unsigned)now - (unsigned)rt0 >= target) break; __builtin_amdgcn_s_sleep(2); } }
}

extern "C" void kernel_launch(void* const* d_in, const int* in_sizes, int n_in, void* d_out, int out_size, void* d_ws, size_t ws_size, hipStream_t stream) {
  (void)in_sizes; (void)n_in; (void)out_size; (void)ws_size;
  (void)hipFuncSetAttribute((const void*)attn_fwd, hipFuncAttributeMaxDynamicSharedMemorySize, (int)LDS_BYTES);
  hipLaunchKernelGGL(attn_fwd, dim3(NWG), dim3(512), LDS_BYTES, stream, d_in[0], d_in[1], d_in[2], d_out, (unsigned*)d_ws);
}
```

```cpp
#include <hip/hip_runtime.h>
#include <hip/hip_bf16.h>
#include <utility>

typedef float    f32x16 __attribute__((ext_vector_type(16)));
typedef float    f32x2  __attribute__((ext_vector_type(2)));
typedef __bf16   bf16x8 __attribute__((ext_vector_type(8)));
typedef __bf16   bf16x2 __attribute__((ext_vector_type(2)));
typedef short    s16x4  __attribute__((ext_vector_type(4)));
typedef short    s16x8  __attribute__((ext_vector_type(8)));
typedef unsigned u32x4  __attribute__((ext_vector_type(4)));
typedef unsigned u32x2  __attribute__((ext_vector_type(2)));
typedef __attribute__((address_space(3))) const bf16x8* lds_b8p;
typedef __attribute__((address_space(3))) s16x4* lds_s4p;
typedef __attribute__((address_space(3))) u32x2* lds_u2p;
typedef __attribute__((address_space(3))) const u32x4* lds_u4p;

#define DI __device__ __forceinline__
#define GAP() __builtin_amdgcn_sched_barrier(0)
#define PIN(x) asm volatile("" : "+v"(x))
#define LDPOL " nt"
#define STAUX 16
#define BOFF 8
#define HMAP 2
#define LIMLOG 30

constexpr unsigned HEADB = 65536u;
constexpr unsigned RQO = 0u, RING = 65536u, LDS_BYTES = 163840u;
constexpr int NHEADS = 512, NWG = 256;
constexpr float CEXP = 0.08838834764831845f * 1.4426950408889634f;
constexpr int PFK = 4, PFV = 3;

DI unsigned cvtpk(float lo, float hi) { f32x2 v = {lo, hi}; bf16x2 b = __builtin_convertvector(v, bf16x2); return __builtin_bit_cast(unsigned, b); }
DI float fmax2(float a, float b) { return __builtin_elementwise_maximum(a, b); }
DI float fmax3(float a, float b, float c) { return __builtin_elementwise_maximum(__builtin_elementwise_maximum(a, b), c); }
DI unsigned opaque(unsigned x) { asm volatile("" : "+v"(x)); return x; }
DI float xhalf_max(float m) { auto rr = __builtin_amdgcn_permlane32_swap(__float_as_uint(m), __float_as_uint(m), false, false); return fmax2(__uint_as_float(rr[0]), __uint_as_float(rr[1])); }
DI float xhalf_sum(float m) { auto rr = __builtin_amdgcn_permlane32_swap(__float_as_uint(m), __float_as_uint(m), false, false); return __uint_as_float(rr[0]) + __uint_as_float(rr[1]); }

struct Stamps { int dummy; };
#define ST(i) ((void)0)

DI void dma2(unsigned m0v, unsigned v0, unsigned v1, __amdgpu_buffer_rsrc_t srd, unsigned soff) {
  asm volatile("s_mov_b32 m0, %0\n\ts_nop 0\n\t"
               "buffer_load_dwordx4 %1, %3, %4 offen" LDPOL " lds\n\t"
               "buffer_load_dwordx4 %2, %3, %4 offen offset:1024" LDPOL " lds"
               :: "s"(m0v), "v"(v0), "v"(v1), "s"(srd), "s"(soff) : "memory");
}
template <int SEL, int IMM> DI void dma1_if(unsigned sel, unsigned m0v, unsigned v, __amdgpu_buffer_rsrc_t srd, unsigned soff) {
  asm volatile("s_cmp_eq_u32 %0, %5\n\ts_cbranch_scc0 1f\n\ts_mov_b32 m0, %1\n\ts_nop 0\n\t"
               "buffer_load_dwordx4 %2, %3, %4 offen offset:%6" LDPOL " lds\n1:"
               :: "s"(sel), "s"(m0v), "v"(v), "s"(srd), "s"(soff), "i"(SEL), "i"(IMM) : "memory", "scc");
}
template <int N> DI void wait_bar() { asm volatile("s_waitcnt vmcnt(%0)\n\ts_barrier" :: "i"(N) : "memory"); }
template <int N> DI void wait_vm() { asm volatile("s_waitcnt vmcnt(%0)" :: "i"(N) : "memory"); }

struct Ctx {
  __amdgpu_buffer_rsrc_t sq, sk, sv, so;
  const char *pq, *pk, *pv;
  unsigned wid, lane;
  unsigned v0;
  unsigned vs[4], vkv[2];
  unsigned hA, hB;
};
constexpr unsigned slot_of(int i) { return RING + 16384u * (unsigned)(i % 6); }

struct Ev { int pos, len, kind, idx; };
#define EVTAB \
  {0, 0, 0, 2}, {0 + 1, 8, 0, 3}, {8 + 1, 16, 0, 4}, {24 + 1, 16, 0, 5}, {40 + 1, 16, 0, 6}, {56 + 1, 16, 0, 7}, {56 + 1, 16, 1, 0}, {72 + 1, 16, 1, 1}, {72 + 1, 16, 1, 2}, \
  {88 + 1, 16, 1, 3}, {88 + 1, 16, 0, 8}, {104 + 1, 16, 0, 9}, {104 + 1, 16, 0, 10}, {120 + 1, 8, 0, 11}, \
  {1000 + 0 + 1, 8, 0, 12}, {1000 + 8 + 1, 16, 0, 13}, {1000 + 24 + 1, 16, 0, 14}, {1000 + 40 + 1, 16, 0, 15}, \
  {1000 + 64 + 1, 0, 2, 0}, {1000 + 80 + 1, 0, 2, 1}, {1000 + 96 + 1, 0, 2, 2}, {1000 + 112 + 1, 0, 2, 3}
constexpr Ev EV[] = {EVTAB};
constexpr int NEV = sizeof(EV) / sizeof(EV[0]);
constexpr int younger(int kind, int idx, int pos) {
  int e0 = -1;
  for (int e = 0; e < NEV; ++e) if (EV[e].kind == kind && EV[e].idx == idx) e0 = e;
  int n = 0;
  if (e0 < 0) n = (kind == 0 && idx == 0) ? 2 : 0;
  for (int e = e0 + 1; e < NEV; ++e) if (EV[e].pos < pos) n += 2;
  return n;
}
constexpr int win_units(int e) { int n = 0; for (int x = 0; x < NEV; ++x) if (EV[x].pos == EV[e].pos) ++n; return n; }
constexpr int win_rank(int e) { int n = 0; for (int x = 0; x < e; ++x) if (EV[x].pos == EV[e].pos) ++n; return n; }

template <int I> DI void dma_ring(const Ctx& c) {
  const unsigned w2 = (32u * c.wid) & 48u;
  (void)w2;
  dma2(slot_of(I) + c.wid * 2048u, c.vkv[0], c.vkv[1], (I & 4) ? c.sv : c.sk, ((I & 8) ? c.hB : c.hA) + 16384u * (I & 3) + c.wid * 2048u);
}
template <int T> DI void dma_q(const Ctx& c, unsigned hoff) {
  const unsigned o = c.wid * 8192u + 2048u * T;
  dma2(RQO + o, c.vs[(2 * T) & 3], c.vs[(2 * T + 1) & 3], c.sq, hoff + o);
}
DI unsigned row_base(unsigned lane) { const unsigned l32 = lane & 31u, h = lane >> 5, f = ((l32 & 3u) << 2) | ((l32 >> 2) & 3u); return l32 * 256u + 16u * (f ^ h); }
DI void read_q(const Ctx& c, bf16x8 (&q)[8]) {
  const unsigned b = row_base(opaque(c.lane)) + RQO + c.wid * 8192u;
#pragma unroll
  for (int d0 = 0; d0 < 8; ++d0) q[d0] = *(lds_b8p)(b ^ (32u * d0));
}
template <int I0, int I1> DI void store_o(const Ctx& c, unsigned hoff) {
  const unsigned lane = opaque(c.lane), rr = lane >> 4, cc = lane & 15u;
  const unsigned b0 = RQO + c.wid * 8192u + rr * 256u + 16u * (cc ^ rr), b1 = b0 ^ 64u;
  u32x4 w[8];
#pragma unroll
  for (int i = I0; i < I1; ++i) w[i] = *(lds_u4p)(((i & 1) ? b1 : b0) + 1024u * i);
#pragma unroll
  for (int i = I0; i < I1; ++i) __builtin_amdgcn_raw_buffer_store_b128(w[i], c.so, lane * 16u + 1024u * i, hoff + c.wid * 8192u, STAUX);
}
template <int E> DI void run_event(const Ctx& c) {
  if constexpr (EV[E].kind == 0) dma_ring<EV[E].idx>(c);
  else if constexpr (EV[E].kind == 1) dma_q<EV[E].idx>(c, c.hB);
  else store_o<2 * EV[E].idx, 2 * EV[E].idx + 2>(c, c.hA);
}
template <int E, int I, int SEL, int SH> DI void piece_if(const Ctx& c) {
  constexpr int X = EV[E].idx;
  if constexpr (EV[E].kind == 0) dma1_if<SEL, 1024 * I>(c.wid >> SH, slot_of(X) + c.wid * 2048u, c.vkv[I], (X & 4) ? c.sv : c.sk, ((X & 8) ? c.hB : c.hA) + 16384u * (X & 3) + c.wid * 2048u);
  else dma1_if<SEL, 1024 * I>(c.wid >> SH, RQO + c.wid * 8192u + 2048u * X, c.vs[(2 * X + I) & 3], c.sq, c.hB + c.wid * 8192u + 2048u * X);
}
template <int POS, int E> DI void event_step(const Ctx& c) {
  if constexpr (EV[E].len == 0) { if constexpr (EV[E].pos == POS) run_event<E>(c); }
  else if constexpr (POS >= EV[E].pos && POS < EV[E].pos + EV[E].len) {
    constexpr int t = POS - EV[E].pos, n = win_units(E), per = 16 * n / EV[E].len;
    static_assert(per == 1 || per == 2 || per == 4, "window shape");
    constexpr int slot0 = t * per, k = slot0 / 8;
    if constexpr (k / 2 == win_rank(E)) piece_if<E, k & 1, (slot0 % 8) / per, (per == 1 ? 0 : (per == 2 ? 1 : 2))>(c);
  }
}
template <int POS, int... Es> DI void events_at(const Ctx& c, std::integer_sequence<int, Es...>) { (event_step<POS, Es>(c), ...); }

struct Hd {
  f32x16 S[8], o[4];
  bf16x8 kf[64];
  s16x4 vlo[64], vhi[64];
  u32x4 P[16];
  unsigned ka[8], va[8];
  float mx[2], l[2], mc;
  float e[64][2];
};
struct Prev { f32x16 o[4]; float il; unsigned ow; };

template <int H, int G> DI void k_read(Hd& s) {
  constexpr int I = 8 * H + (G >> 4);
  s.kf[G] = *(lds_b8p)(s.ka[G & 7] + 16384u * ((I % 6) % 3) + 8192u * ((G >> 3) & 1));
}
template <int H, int GP> DI void v_read(Hd& s) {
  constexpr int I = 8 * H + 4 + (GP >> 4), ks = (GP >> 2) & 3, db = GP & 3;
  s.vlo[GP] = __builtin_amdgcn_ds_read_tr16_b64_v4i16((lds_s4p)(s.va[2 * db] + 16384u * ((I % 6) % 3) + 4096u * ks));
  s.vhi[GP] = __builtin_amdgcn_ds_read_tr16_b64_v4i16((lds_s4p)(s.va[2 * db + 1] + 16384u * ((I % 6) % 3) + 4096u * ks + 2048u));
}
template <int IPREV, int INEXT> DI void rebase(unsigned (&a)[8]) {
  if constexpr ((IPREV % 6) / 3 != (INEXT % 6) / 3) {
#pragma unroll
    for (int i = 0; i < 8; ++i) a[i] = opaque((INEXT % 6) >= 3 ? a[i] + 49152u : a[i] - 49152u);
  }
}
DI void va_init(const Ctx& c, Hd& s, unsigned base) {
  const unsigned lane = opaque(c.lane), h = lane >> 5, blk = (lane >> 4) & 1u, q4 = (lane & 15u) >> 2, p = lane & 3u;
  const unsigned vb = 256u * (4u * h + q4) + 16u * (4u * q4 + ((2u * blk + (p >> 1)) ^ h)) + 8u * (p & 1u);
#pragma unroll
  for (int i = 0; i < 8; ++i) s.va[i] = opaque((vb ^ (64u * (i >> 1)) ^ (32u * (i & 1))) + base);
}
template <int N> DI void pair_a(Hd& s) {
  constexpr int ks = N >> 2, kb = ks >> 1, r = 8 * (ks & 1) + 2 * (N & 3);
  s.e[N][0] = __builtin_amdgcn_exp2f(__builtin_fmaf(s.S[kb][r], CEXP, s.mc));
  s.e[N][1] = __builtin_amdgcn_exp2f(__builtin_fmaf(s.S[kb][r + 1], CEXP, s.mc));
  PIN(s.e[N][0]); PIN(s.e[N][1]);
}
template <int N> DI void pair_b(Hd& s) {
  if constexpr (N == 0) { s.l[0] = s.e[N][0]; s.l[1] = s.e[N][1]; }
  else { s.l[0] += s.e[N][0]; s.l[1] += s.e[N][1]; }
  unsigned w = cvtpk(s.e[N][0], s.e[N][1]);
  PIN(s.l[0]); PIN(s.l[1]); PIN(w);
  s.P[N >> 2][N & 3] = w;
}
constexpr int pair_gap(int n) { return n < 32 ? 16 + (3 * n) / 2 : 64 + ((n - 32) * 57) / 32; }
constexpr int pair_at(int g) { for (int n = 0; n < 64; ++n) if (pair_gap(n) == g) return n; return -1; }
template <int I> DI void pack_item(const f32x16 (&o)[4], float il, unsigned ow) {
  constexpr int db = I >> 2, g = I & 3;
  const u32x2 w = {cvtpk(o[db][4 * g] * il, o[db][4 * g + 1] * il), cvtpk(o[db][4 * g + 2] * il, o[db][4 * g + 3] * il)};
  *(lds_u2p)(ow ^ (16u * I)) = w;
}
DI unsigned stage_base(const Ctx& c) { const unsigned lane = opaque(c.lane), l32 = lane & 31u, h = lane >> 5; return RQO + c.wid * 8192u + l32 * 256u + 16u * (l32 & 7u) + 8u * h; }

template <int H, int G> DI void gap(const Ctx& c, Hd& s, const bf16x8 (&q)[8], const Prev& pv, Stamps& st) {
  constexpr int R = 8 * H;
  if constexpr ((G + BOFF) % 16 == 0 && G + BOFF >= 16 && G + BOFF <= 112) wait_bar<younger(0, R + (G + BOFF) / 16, 1000 * H + G + 1)>();
  events_at<1000 * H + G + 1>(c, std::make_integer_sequence<int, NEV>{});
  if constexpr (G < 64) {
    if constexpr (G + PFK < 64) {
      if constexpr (((G + PFK) & 15) == 0) rebase<R + ((G + PFK) >> 4) - 1, R + ((G + PFK) >> 4)>(s.ka);
      k_read<H, G + PFK>(s);
    }
    if constexpr (G == 60) va_init(c, s, RING + (((R + 4) % 6) >= 3 ? 49152u : 0u));
    if constexpr (G + PFV >= 64) v_read<H, G + PFV - 64>(s);
    constexpr int kb = G >> 3, d0 = G & 7;
    if constexpr (d0 == 0) s.S[kb] = __builtin_amdgcn_mfma_f32_32x32x16_bf16(s.kf[G], q[d0], f32x16{}, 0, 0, 0);
    else                   s.S[kb] = __builtin_amdgcn_mfma_f32_32x32x16_bf16(s.kf[G], q[d0], s.S[kb], 0, 0, 0);
    PIN(s.S[kb]);
    if constexpr (G == 10) { s.mx[0] = fmax3(s.S[0][0], s.S[0][1], s.S[0][2]); s.mx[1] = fmax3(s.S[0][3], s.S[0][4], s.S[0][5]); PIN(s.mx[0]); PIN(s.mx[1]); }
    if constexpr (G == 11) { s.mx[0] = fmax3(s.mx[0], s.S[0][6], s.S[0][7]); s.mx[1] = fmax3(s.mx[1], s.S[0][8], s.S[0][9]); PIN(s.mx[0]); PIN(s.mx[1]); }
    if constexpr (G == 12) { s.mx[0] = fmax3(s.mx[0], s.S[0][10], s.S[0][11]); s.mx[1] = fmax3(s.mx[1], s.S[0][12], s.S[0][13]); PIN(s.mx[0]); PIN(s.mx[1]); }
    if constexpr (G == 13) { s.mx[0] = fmax3(s.mx[0], s.S[0][14], s.S[0][15]); s.mx[0] = fmax2(s.mx[0], s.mx[1]); PIN(s.mx[0]); }
    if constexpr (G == 14) { s.mc = -xhalf_max(s.mx[0]) * CEXP; PIN(s.mc); }
    if constexpr (H == 1 && G < 16) pack_item<G>(pv.o, pv.il, pv.ow);
  } else {
    constexpr int GP = G - 64, ks = GP >> 2, db = GP & 3;
    if constexpr (GP + PFV < 64) {
      if constexpr (((GP + PFV) & 15) == 0) rebase<R + 4 + ((GP + PFV) >> 4) - 1, R + 4 + ((GP + PFV) >> 4)>(s.va);
      v_read<H, GP + PFV>(s);
    }
    const s16x8 vf = __builtin_shufflevector(s.vlo[GP], s.vhi[GP], 0, 1, 2, 3, 4, 5, 6, 7);
    if constexpr (ks == 0) s.o[db] = __builtin_amdgcn_mfma_f32_32x32x16_bf16(__builtin_bit_cast(bf16x8, vf), __builtin_bit_cast(bf16x8, s.P[0]), f32x16{}, 0, 0, 0);
    else                   s.o[db] = __builtin_amdgcn_mfma_f32_32x32x16_bf16(__builtin_bit_cast(bf16x8, vf), __builtin_bit_cast(bf16x8, s.P[ks]), s.o[db], 0, 0, 0);
    PIN(s.o[db]);
  }
  if constexpr (pair_at(G - 1) >= 0) pair_b<pair_at(G - 1)>(s);
  if constexpr (pair_at(G) >= 0) pair_a<pair_at(G)>(s);
  GAP();
}
template <int H, int... Gs> DI void gaps(const Ctx& c, Hd& s, const bf16x8 (&q)[8], const Prev& pv, Stamps& st, std::integer_sequence<int, Gs...>) { (gap<H, Gs>(c, s, q, pv, st), ...); }

__device__ __forceinline__ void fallback(const Ctx& c, unsigned hoff, f32x16 (&o)[4], float& l) {
  const unsigned lane = c.lane, l32 = lane & 31u, h = lane >> 5;
  const char* qp = c.pq + hoff + (c.wid * 32u + l32) * 256u + h * 16u;
  const char* kp = c.pk + hoff + l32 * 256u + h * 16u;
  const short* vp = (const short*)(c.pv + hoff) + l32;
  bf16x8 qf[8];
#pragma unroll
  for (int d = 0; d < 8; ++d) qf[d] = *(const bf16x8*)(qp + 32 * d);
  float m = -__builtin_inff();
#pragma nounroll
  for (int kb = 0; kb < 8; ++kb) {
    f32x16 a = {};
#pragma unroll
    for (int d = 0; d < 8; ++d) a = __builtin_amdgcn_mfma_f32_32x32x16_bf16(*(const bf16x8*)(kp + kb * 8192 + 32 * d), qf[d], a, 0, 0, 0);
#pragma unroll
    for (int r = 0; r < 16; ++r) m = fmax2(m, a[r]);
  }
  m = xhalf_max(m);
  float ls = 0.f;
#pragma unroll
  for (int db = 0; db < 4; ++db) o[db] = f32x16{};
#pragma nounroll
  for (int kb = 0; kb < 8; ++kb) {
    f32x16 a = {};
#pragma unroll
    for (int d = 0; d < 8; ++d) a = __builtin_amdgcn_mfma_f32_32x32x16_bf16(*(const bf16x8*)(kp + kb * 8192 + 32 * d), qf[d], a, 0, 0, 0);
#pragma unroll
    for (int sx = 0; sx < 2; ++sx) {
      float e[8];
#pragma unroll
      for (int j = 0; j < 8; ++j) { e[j] = __builtin_amdgcn_exp2f((a[8 * sx + j] - m) * CEXP); ls += e[j]; }
      const u32x4 p = {cvtpk(e[0], e[1]), cvtpk(e[2], e[3]), cvtpk(e[4], e[5]), cvtpk(e[6], e[7])};
#pragma unroll
      for (int db = 0; db < 4; ++db) {
        s16x8 vf;
#pragma unroll
        for (int j = 0; j < 8; ++j) vf[j] = vp[(kb * 32 + 16 * sx + 8 * (j >> 2) + 4 * h + (j & 3)) * 128 + 32 * db];
        o[db] = __builtin_amdgcn_mfma_f32_32x32x16_bf16(__builtin_bit_cast(bf16x8, vf), __builtin_bit_cast(bf16x8, p), o[db], 0, 0, 0);
      }
    }
  }
  l = xhalf_sum(ls);
}

template <int H>
DI void head(const Ctx& c, bf16x8 (&q)[8], Prev& pv, Stamps& st) {
  constexpr int R = 8 * H;
  Hd s;
  { const unsigned ra = row_base(opaque(c.lane));
#pragma unroll
    for (int d0 = 0; d0 < 8; ++d0) s.ka[d0] = opaque((ra ^ (32u * d0)) + RING + ((R % 6) >= 3 ? 49152u : 0u)); }
  events_at<1000 * H>(c, std::make_integer_sequence<int, NEV>{});
  GAP();
  wait_bar<younger(0, R, 1000 * H + 1)>();
  GAP();
  k_read<H, 0>(s); k_read<H, 1>(s); k_read<H, 2>(s); k_read<H, 3>(s);
  GAP();
  gaps<H>(c, s, q, pv, st, std::make_integer_sequence<int, 128>{});
  ST(1 + 10 * H + 8);
  float l = xhalf_sum(s.l[0] + s.l[1]);
  constexpr float LIM_HI = (float)(1ull << LIMLOG), LIM_LO = 1.0f / LIM_HI;
  if (__builtin_expect(__any(!(l <= LIM_HI && l >= LIM_LO)), 0)) fallback(c, H ? c.hB : c.hA, s.o, l);
  const float il = __builtin_amdgcn_rcpf(l);
  if constexpr (H == 0) {
#pragma unroll
    for (int db = 0; db < 4; ++db) pv.o[db] = s.o[db];
    pv.il = il; pv.ow = stage_base(c);
    GAP();
    wait_vm<younger(1, 3, 1000)>();
    GAP();
    read_q(c, q);
  } else {
    const unsigned ow = stage_base(c);
    pack_item<0>(s.o, il, ow); pack_item<1>(s.o, il, ow); pack_item<2>(s.o, il, ow); pack_item<3>(s.o, il, ow);
    pack_item<4>(s.o, il, ow); pack_item<5>(s.o, il, ow); pack_item<6>(s.o, il, ow); pack_item<7>(s.o, il, ow);
    pack_item<8>(s.o, il, ow); pack_item<9>(s.o, il, ow); pack_item<10>(s.o, il, ow); pack_item<11>(s.o, il, ow);
    pack_item<12>(s.o, il, ow); pack_item<13>(s.o, il, ow); pack_item<14>(s.o, il, ow); pack_item<15>(s.o, il, ow);
    store_o<0, 8>(c, c.hB);
  }
  GAP();
}

__global__ __launch_bounds__(512, 2) void attn_fwd(const void* __restrict__ Q, const void* __restrict__ K, const void* __restrict__ V, void* __restrict__ O, unsigned* __restrict__ dbg) {
  Stamps st;
  ST(0);
  Ctx c;
  const unsigned tid = threadIdx.x, lane = tid & 63u;
  c.lane = lane;
  c.wid = __builtin_amdgcn_readfirstlane(tid >> 6);
  const int nbytes = NHEADS * (int)HEADB;
  c.sq = __builtin_amdgcn_make_buffer_rsrc((void*)Q, (short)0, nbytes, 0x00020000);
  c.sk = __builtin_amdgcn_make_buffer_rsrc((void*)K, (short)0, nbytes, 0x00020000);
  c.sv = __builtin_amdgcn_make_buffer_rsrc((void*)V, (short)0, nbytes, 0x00020000);
  c.so = __builtin_amdgcn_make_buffer_rsrc((void*)O, (short)0, nbytes, 0x00020000);
  c.pq = (const char*)Q; c.pk = (const char*)K; c.pv = (const char*)V;
  {
    const unsigned r = lane >> 4, p = lane & 15u;
    c.v0 = r * 256u + 16u * (p ^ (r << 2));
#pragma unroll
    for (int j = 0; j < 4; ++j) c.vs[j] = opaque(c.v0 ^ (16u * j));
    const unsigned w2 = (32u * c.wid) & 48u;
    c.vkv[0] = opaque(c.v0 ^ w2); c.vkv[1] = opaque(c.v0 ^ (w2 + 16u));
  }
  { const unsigned b = blockIdx.x;
    const unsigned h0 = HMAP == 0 ? b : (HMAP == 1 ? 2u * b : (b & 7u) * 64u + (b >> 3) * 2u);
    c.hA = h0 * HEADB; c.hB = (h0 + (HMAP == 0 ? (unsigned)NWG : 1u)) * HEADB; }
  asm volatile("s_nop 4");
  dma_q<0>(c, c.hA); dma_q<1>(c, c.hA); dma_q<2>(c, c.hA); dma_q<3>(c, c.hA);
  dma_ring<0>(c); dma_ring<1>(c);
  bf16x8 q[8];
  GAP();
  wait_vm<4>();
  GAP();
  read_q(c, q);
  Prev pv;
  head<0>(c, q, pv, st);
  head<1>(c, q, pv, st);
}

extern "C" void kernel_launch(void* const* d_in, const int* in_sizes, int n_in, void* d_out, int out_size, void* d_ws, size_t ws_size, hipStream_t stream) {
  (void)in_sizes; (void)n_in; (void)out_size; (void)ws_size;
  (void)hipFuncSetAttribute((const void*)attn_fwd, hipFuncAttributeMaxDynamicSharedMemorySize, (int)LDS_BYTES);
  hipLaunchKernelGGL(attn_fwd, dim3(NWG), dim3(512), LDS_BYTES, stream, d_in[0], d_in[1], d_in[2], d_out, (unsigned*)d_ws);
}
```

```cpp
#include <hip/hip_runtime.h>
#include <hip/hip_bf16.h>
#include <utility>
#define STAMPS
#define RSTAMP
#define SIDE 0

typedef float    f32x16 __attribute__((ext_vector_type(16)));
typedef float    f32x2  __attribute__((ext_vector_type(2)));
typedef __bf16   bf16x8 __attribute__((ext_vector_type(8)));
typedef __bf16   bf16x2 __attribute__((ext_vector_type(2)));
typedef short    s16x4  __attribute__((ext_vector_type(4)));
typedef short    s16x8  __attribute__((ext_vector_type(8)));
typedef unsigned u32x4  __attribute__((ext_vector_type(4)));
typedef unsigned u32x2  __attribute__((ext_vector_type(2)));
typedef __attribute__((address_space(3))) const bf16x8* lds_b8p;
typedef __attribute__((address_space(3))) s16x4* lds_s4p;
typedef __attribute__((address_space(3))) u32x2* lds_u2p;
typedef __attribute__((address_space(3))) const u32x4* lds_u4p;

#define DI __device__ __forceinline__
#define GAP() __builtin_amdgcn_sched_barrier(0)
#define PIN(x) asm volatile("" : "+v"(x))
#define LDPOL " nt"
#define STAUX 18
#define BOFF 8
#define HMAP 2
#define LIMLOG 30

constexpr unsigned HEADB = 65536u;
constexpr unsigned RQO = 0u, RING = 65536u, LDS_BYTES = 163840u;
constexpr int NHEADS = 512, NWG = 256;
constexpr float CEXP = 0.08838834764831845f * 1.4426950408889634f;
constexpr int PFK = 4, PFV = 3;

DI unsigned cvtpk(float lo, float hi) { f32x2 v = {lo, hi}; bf16x2 b = __builtin_convertvector(v, bf16x2); return __builtin_bit_cast(unsigned, b); }
DI float fmax2(float a, float b) { return __builtin_elementwise_maximum(a, b); }
DI float fmax3(float a, float b, float c) { return __builtin_elementwise_maximum(__builtin_elementwise_maximum(a, b), c); }
DI unsigned opaque(unsigned x) { asm volatile("" : "+v"(x)); return x; }
DI float xhalf_max(float m) { auto rr = __builtin_amdgcn_permlane32_swap(__float_as_uint(m), __float_as_uint(m), false, false); return fmax2(__uint_as_float(rr[0]), __uint_as_float(rr[1])); }
DI float xhalf_sum(float m) { auto rr = __builtin_amdgcn_permlane32_swap(__float_as_uint(m), __float_as_uint(m), false, false); return __uint_as_float(rr[0]) + __uint_as_float(rr[1]); }

#define NST 40
struct Stamps { unsigned t[NST]; };
DI unsigned stamp_now() { unsigned long long t; GAP();
  asm volatile("s_memrealtime %0\n\ts_waitcnt lgkmcnt(0)" : "=s"(t) :: "memory");
  GAP(); return (unsigned)t; }
#define ST(i) (st.t[i] = stamp_now())

DI void dma2(unsigned m0v, unsigned v0, unsigned v1, __amdgpu_buffer_rsrc_t srd, unsigned soff) {
  asm volatile("s_mov_b32 m0, %0\n\ts_nop 0\n\t"
               "buffer_load_dwordx4 %1, %3, %4 offen" LDPOL " lds\n\t"
               "buffer_load_dwordx4 %2, %3, %4 offen offset:1024" LDPOL " lds"
               :: "s"(m0v), "v"(v0), "v"(v1), "s"(srd), "s"(soff) : "memory");
}
template <int SEL, int IMM> DI void dma1_if(unsigned sel, unsigned m0v, unsigned v, __amdgpu_buffer_rsrc_t srd, unsigned soff) {
  asm volatile("s_cmp_eq_u32 %0, %5\n\ts_cbranch_scc0 1f\n\ts_mov_b32 m0, %1\n\ts_nop 0\n\t"
               "buffer_load_dwordx4 %2, %3, %4 offen offset:%6" LDPOL " lds\n1:"
               :: "s"(sel), "s"(m0v), "v"(v), "s"(srd), "s"(soff), "i"(SEL), "i"(IMM) : "memory", "scc");
}
template <int N> DI void wait_bar() { asm volatile("s_waitcnt vmcnt(%0)\n\ts_barrier" :: "i"(N) : "memory"); }
template <int N> DI void wait_vm() { asm volatile("s_waitcnt vmcnt(%0)" :: "i"(N) : "memory"); }

struct Ctx {
  __amdgpu_buffer_rsrc_t sq, sk, sv, so;
  const char *pq, *pk, *pv;
  unsigned wid, lane;
  unsigned v0;
  unsigned vs[4], vkv[2];
  unsigned hA, hB;
};
constexpr unsigned slot_of(int i) { return RING + 16384u * (unsigned)(i % 6); }

struct Ev { int pos, len, kind, idx; };
#define EVTAB \
  {0, 0, 0, 2}, {0 + 1, 8, 0, 3}, {8 + 1, 16, 0, 4}, {24 + 1, 16, 0, 5}, {40 + 1, 16, 0, 6}, {56 + 1, 16, 0, 7}, {56 + 1, 16, 1, 0}, {72 + 1, 16, 1, 1}, {72 + 1, 16, 1, 2}, \
  {88 + 1, 16, 1, 3}, {88 + 1, 16, 0, 8}, {104 + 1, 16, 0, 9}, {104 + 1, 16, 0, 10}, {120 + 1, 8, 0, 11}, \
  {1000 + 0 + 1, 8, 0, 12}, {1000 + 8 + 1, 16, 0, 13}, {1000 + 24 + 1, 16, 0, 14}, {1000 + 40 + 1, 16, 0, 15}, \
  {1000 + 64 + 1, 0, 2, 0}, {1000 + 80 + 1, 0, 2, 1}, {1000 + 96 + 1, 0, 2, 2}, {1000 + 112 + 1, 0, 2, 3}
constexpr Ev EV[] = {EVTAB};
constexpr int NEV = sizeof(EV) / sizeof(EV[0]);
constexpr int younger(int kind, int idx, int pos) {
  int e0 = -1;
  for (int e = 0; e < NEV; ++e) if (EV[e].kind == kind && EV[e].idx == idx) e0 = e;
  int n = 0;
  if (e0 < 0) n = (kind == 0 && idx == 0) ? 2 : 0;
  for (int e = e0 + 1; e < NEV; ++e) if (EV[e].pos < pos) n += 2;
  return n;
}
constexpr int win_units(int e) { int n = 0; for (int x = 0; x < NEV; ++x) if (EV[x].pos == EV[e].pos) ++n; return n; }
constexpr int win_rank(int e) { int n = 0; for (int x = 0; x < e; ++x) if (EV[x].pos == EV[e].pos) ++n; return n; }

template <int I> DI void dma_ring(const Ctx& c) {
  const unsigned w2 = (32u * c.wid) & 48u;
  (void)w2;
  dma2(slot_of(I) + c.wid * 2048u, c.vkv[0], c.vkv[1], (I & 4) ? c.sv : c.sk, ((I & 8) ? c.hB : c.hA) + 16384u * (I & 3) + c.wid * 2048u);
}
template <int T> DI void dma_q(const Ctx& c, unsigned hoff) {
  const unsigned o = c.wid * 8192u + 2048u * T;
  dma2(RQO + o, c.vs[(2 * T) & 3], c.vs[(2 * T + 1) & 3], c.sq, hoff + o);
}
DI unsigned row_base(unsigned lane) { const unsigned l32 = lane & 31u, h = lane >> 5, f = ((l32 & 3u) << 2) | ((l32 >> 2) & 3u); return l32 * 256u + 16u * (f ^ h); }
DI void read_q(const Ctx& c, bf16x8 (&q)[8]) {
  const unsigned b = row_base(opaque(c.lane)) + RQO + c.wid * 8192u;
#pragma unroll
  for (int d0 = 0; d0 < 8; ++d0) q[d0] = *(lds_b8p)(b ^ (32u * d0));
}
template <int I0, int I1> DI void store_o(const Ctx& c, unsigned hoff) {
  const unsigned lane = opaque(c.lane), rr = lane >> 4, cc = lane & 15u;
  const unsigned b0 = RQO + c.wid * 8192u + rr * 256u + 16u * (cc ^ rr), b1 = b0 ^ 64u;
  u32x4 w[8];
#pragma unroll
  for (int i = I0; i < I1; ++i) w[i] = *(lds_u4p)(((i & 1) ? b1 : b0) + 1024u * i);
#pragma unroll
  for (int i = I0; i < I1; ++i) __builtin_amdgcn_raw_buffer_store_b128(w[i], c.so, lane * 16u + 1024u * i, hoff + c.wid * 8192u, STAUX);
}
template <int E> DI void run_event(const Ctx& c) {
  if constexpr (EV[E].kind == 0) dma_ring<EV[E].idx>(c);
  else if constexpr (EV[E].kind == 1) dma_q<EV[E].idx>(c, c.hB);
  else store_o<2 * EV[E].idx, 2 * EV[E].idx + 2>(c, c.hA);
}
template <int E, int I, int SEL, int SH> DI void piece_if(const Ctx& c) {
  constexpr int X = EV[E].idx;
  if constexpr (EV[E].kind == 0) dma1_if<SEL, 1024 * I>(c.wid >> SH, slot_of(X) + c.wid * 2048u, c.vkv[I], (X & 4) ? c.sv : c.sk, ((X & 8) ? c.hB : c.hA) + 16384u * (X & 3) + c.wid * 2048u);
  else dma1_if<SEL, 1024 * I>(c.wid >> SH, RQO + c.wid * 8192u + 2048u * X, c.vs[(2 * X + I) & 3], c.sq, c.hB + c.wid * 8192u + 2048u * X);
}
template <int POS, int E> DI void event_step(const Ctx& c) {
  if constexpr (EV[E].len == 0) { if constexpr (EV[E].pos == POS) run_event<E>(c); }
  else if constexpr (POS >= EV[E].pos && POS < EV[E].pos + EV[E].len) {
    constexpr int t = POS - EV[E].pos, n = win_units(E), per = 16 * n / EV[E].len;
    static_assert(per == 1 || per == 2 || per == 4, "window shape");
    constexpr int slot0 = t * per, k = slot0 / 8;
    if constexpr (k / 2 == win_rank(E)) piece_if<E, k & 1, (slot0 % 8) / per, (per == 1 ? 0 : (per == 2 ? 1 : 2))>(c);
  }
}
template <int POS, int... Es> DI void events_at(const Ctx& c, std::integer_sequence<int, Es...>) { (event_step<POS, Es>(c), ...); }

struct Hd {
  f32x16 S[8], o[4];
  bf16x8 kf[64];
  s16x4 vlo[64], vhi[64];
  u32x4 P[16];
  unsigned ka[8], va[8];
  float mx[2], l[2], mc;
  float e[64][2];
};
struct Prev { f32x16 o[4]; float il; unsigned ow; };

template <int H, int G> DI void k_read(Hd& s) {
  constexpr int I = 8 * H + (G >> 4);
  s.kf[G] = *(lds_b8p)(s.ka[G & 7] + 16384u * ((I % 6) % 3) + 8192u * ((G >> 3) & 1));
}
template <int H, int GP> DI void v_read(Hd& s) {
  constexpr int I = 8 * H + 4 + (GP >> 4), ks = (GP >> 2) & 3, db = GP & 3;
  s.vlo[GP] = __builtin_amdgcn_ds_read_tr16_b64_v4i16((lds_s4p)(s.va[2 * db] + 16384u * ((I % 6) % 3) + 4096u * ks));
  s.vhi[GP] = __builtin_amdgcn_ds_read_tr16_b64_v4i16((lds_s4p)(s.va[2 * db + 1] + 16384u * ((I % 6) % 3) + 4096u * ks + 2048u));
}
template <int IPREV, int INEXT> DI void rebase(unsigned (&a)[8]) {
  if constexpr ((IPREV % 6) / 3 != (INEXT % 6) / 3) {
#pragma unroll
    for (int i = 0; i < 8; ++i) a[i] = opaque((INEXT % 6) >= 3 ? a[i] + 49152u : a[i] - 49152u);
  }
}
DI void va_init(const Ctx& c, Hd& s, unsigned base) {
  const unsigned lane = opaque(c.lane), h = lane >> 5, blk = (lane >> 4) & 1u, q4 = (lane & 15u) >> 2, p = lane & 3u;
  const unsigned vb = 256u * (4u * h + q4) + 16u * (4u * q4 + ((2u * blk + (p >> 1)) ^ h)) + 8u * (p & 1u);
#pragma unroll
  for (int i = 0; i < 8; ++i) s.va[i] = opaque((vb ^ (64u * (i >> 1)) ^ (32u * (i & 1))) + base);
}
template <int N> DI void pair_a(Hd& s) {
  constexpr int ks = N >> 2, kb = ks >> 1, r = 8 * (ks & 1) + 2 * (N & 3);
  s.e[N][0] = __builtin_amdgcn_exp2f(__builtin_fmaf(s.S[kb][r], CEXP, s.mc));
  s.e[N][1] = __builtin_amdgcn_exp2f(__builtin_fmaf(s.S[kb][r + 1], CEXP, s.mc));
  PIN(s.e[N][0]); PIN(s.e[N][1]);
}
template <int N> DI void pair_b(Hd& s) {
  if constexpr (N == 0) { s.l[0] = s.e[N][0]; s.l[1] = s.e[N][1]; }
  else { s.l[0] += s.e[N][0]; s.l[1] += s.e[N][1]; }
  unsigned w = cvtpk(s.e[N][0], s.e[N][1]);
  PIN(s.l[0]); PIN(s.l[1]); PIN(w);
  s.P[N >> 2][N & 3] = w;
}
constexpr int pair_gap(int n) { return n < 32 ? 16 + (3 * n) / 2 : 64 + ((n - 32) * 57) / 32; }
constexpr int pair_at(int g) { for (int n = 0; n < 64; ++n) if (pair_gap(n) == g) return n; return -1; }
template <int I> DI void pack_item(const f32x16 (&o)[4], float il, unsigned ow) {
  constexpr int db = I >> 2, g = I & 3;
  const u32x2 w = {cvtpk(o[db][4 * g] * il, o[db][4 * g + 1] * il), cvtpk(o[db][4 * g + 2] * il, o[db][4 * g + 3] * il)};
  *(lds_u2p)(ow ^ (16u * I)) = w;
}
DI unsigned stage_base(const Ctx& c) { const unsigned lane = opaque(c.lane), l32 = lane & 31u, h = lane >> 5; return RQO + c.wid * 8192u + l32 * 256u + 16u * (l32 & 7u) + 8u * h; }

template <int H, int G> DI void gap(const Ctx& c, Hd& s, const bf16x8 (&q)[8], const Prev& pv, Stamps& st) {
  constexpr int R = 8 * H;
  if constexpr (G % 16 == 0) ST(1 + 10 * H + G / 16);
  if constexpr ((G + BOFF) % 16 == 0 && G + BOFF >= 16 && G + BOFF <= 112) wait_bar<younger(0, R + (G + BOFF) / 16, 1000 * H + G + 1)>();
  events_at<1000 * H + G + 1>(c, std::make_integer_sequence<int, NEV>{});
  if constexpr (G < 64) {
    if constexpr (G + PFK < 64) {
      if constexpr (((G + PFK) & 15) == 0) rebase<R + ((G + PFK) >> 4) - 1, R + ((G + PFK) >> 4)>(s.ka);
      k_read<H, G + PFK>(s);
    }
    if constexpr (G == 60) va_init(c, s, RING + (((R + 4) % 6) >= 3 ? 49152u : 0u));
    if constexpr (G + PFV >= 64) v_read<H, G + PFV - 64>(s);
    constexpr int kb = G >> 3, d0 = G & 7;
    if constexpr (d0 == 0) s.S[kb] = __builtin_amdgcn_mfma_f32_32x32x16_bf16(s.kf[G], q[d0], f32x16{}, 0, 0, 0);
    else                   s.S[kb] = __builtin_amdgcn_mfma_f32_32x32x16_bf16(s.kf[G], q[d0], s.S[kb], 0, 0, 0);
    PIN(s.S[kb]);
    if constexpr (G == 10) { s.mx[0] = fmax3(s.S[0][0], s.S[0][1], s.S[0][2]); s.mx[1] = fmax3(s.S[0][3], s.S[0][4], s.S[0][5]); PIN(s.mx[0]); PIN(s.mx[1]); }
    if constexpr (G == 11) { s.mx[0] = fmax3(s.mx[0], s.S[0][6], s.S[0][7]); s.mx[1] = fmax3(s.mx[1], s.S[0][8], s.S[0][9]); PIN(s.mx[0]); PIN(s.mx[1]); }
    if constexpr (G == 12) { s.mx[0] = fmax3(s.mx[0], s.S[0][10], s.S[0][11]); s.mx[1] = fmax3(s.mx[1], s.S[0][12], s.S[0][13]); PIN(s.mx[0]); PIN(s.mx[1]); }
    if constexpr (G == 13) { s.mx[0] = fmax3(s.mx[0], s.S[0][14], s.S[0][15]); s.mx[0] = fmax2(s.mx[0], s.mx[1]); PIN(s.mx[0]); }
    if constexpr (G == 14) { s.mc = -xhalf_max(s.mx[0]) * CEXP; PIN(s.mc); }
    if constexpr (H == 1 && G < 16) pack_item<G>(pv.o, pv.il, pv.ow);
  } else {
    constexpr int GP = G - 64, ks = GP >> 2, db = GP & 3;
    if constexpr (GP + PFV < 64) {
      if constexpr (((GP + PFV) & 15) == 0) rebase<R + 4 + ((GP + PFV) >> 4) - 1, R + 4 + ((GP + PFV) >> 4)>(s.va);
      v_read<H, GP + PFV>(s);
    }
    const s16x8 vf = __builtin_shufflevector(s.vlo[GP], s.vhi[GP], 0, 1, 2, 3, 4, 5, 6, 7);
    if constexpr (ks == 0) s.o[db] = __builtin_amdgcn_mfma_f32_32x32x16_bf16(__builtin_bit_cast(bf16x8, vf), __builtin_bit_cast(bf16x8, s.P[0]), f32x16{}, 0, 0, 0);
    else                   s.o[db] = __builtin_amdgcn_mfma_f32_32x32x16_bf16(__builtin_bit_cast(bf16x8, vf), __builtin_bit_cast(bf16x8, s.P[ks]), s.o[db], 0, 0, 0);
    PIN(s.o[db]);
  }
  if constexpr (pair_at(G - 1) >= 0) pair_b<pair_at(G - 1)>(s);
  if constexpr (pair_at(G) >= 0) pair_a<pair_at(G)>(s);
  GAP();
}
template <int H, int... Gs> DI void gaps(const Ctx& c, Hd& s, const bf16x8 (&q)[8], const Prev& pv, Stamps& st, std::integer_sequence<int, Gs...>) { (gap<H, Gs>(c, s, q, pv, st), ...); }

__device__ __forceinline__ void fallback(const Ctx& c, unsigned hoff, f32x16 (&o)[4], float& l) {
  const unsigned lane = c.lane, l32 = lane & 31u, h = lane >> 5;
  const char* qp = c.pq + hoff + (c.wid * 32u + l32) * 256u + h * 16u;
  const char* kp = c.pk + hoff + l32 * 256u + h * 16u;
  const short* vp = (const short*)(c.pv + hoff) + l32;
  bf16x8 qf[8];
#pragma unroll
  for (int d = 0; d < 8; ++d) qf[d] = *(const bf16x8*)(qp + 32 * d);
  float m = -__builtin_inff();
#pragma nounroll
  for (int kb = 0; kb < 8; ++kb) {
    f32x16 a = {};
#pragma unroll
    for (int d = 0; d < 8; ++d) a = __builtin_amdgcn_mfma_f32_32x32x16_bf16(*(const bf16x8*)(kp + kb * 8192 + 32 * d), qf[d], a, 0, 0, 0);
#pragma unroll
    for (int r = 0; r < 16; ++r) m = fmax2(m, a[r]);
  }
  m = xhalf_max(m);
  float ls = 0.f;
#pragma unroll
  for (int db = 0; db < 4; ++db) o[db] = f32x16{};
#pragma nounroll
  for (int kb = 0; kb < 8; ++kb) {
    f32x16 a = {};
#pragma unroll
    for (int d = 0; d < 8; ++d) a = __builtin_amdgcn_mfma_f32_32x32x16_bf16(*(const bf16x8*)(kp + kb * 8192 + 32 * d), qf[d], a, 0, 0, 0);
#pragma unroll
    for (int sx = 0; sx < 2; ++sx) {
      float e[8];
#pragma unroll
      for (int j = 0; j < 8; ++j) { e[j] = __builtin_amdgcn_exp2f((a[8 * sx + j] - m) * CEXP); ls += e[j]; }
      const u32x4 p = {cvtpk(e[0], e[1]), cvtpk(e[2], e[3]), cvtpk(e[4], e[5]), cvtpk(e[6], e[7])};
#pragma unroll
      for (int db = 0; db < 4; ++db) {
        s16x8 vf;
#pragma unroll
        for (int j = 0; j < 8; ++j) vf[j] = vp[(kb * 32 + 16 * sx + 8 * (j >> 2) + 4 * h + (j & 3)) * 128 + 32 * db];
        o[db] = __builtin_amdgcn_mfma_f32_32x32x16_bf16(__builtin_bit_cast(bf16x8, vf), __builtin_bit_cast(bf16x8, p), o[db], 0, 0, 0);
      }
    }
  }
  l = xhalf_sum(ls);
}

template <int H>
DI void head(const Ctx& c, bf16x8 (&q)[8], Prev& pv, Stamps& st) {
  constexpr int R = 8 * H;
  Hd s;
  { const unsigned ra = row_base(opaque(c.lane));
#pragma unroll
    for (int d0 = 0; d0 < 8; ++d0) s.ka[d0] = opaque((ra ^ (32u * d0)) + RING + ((R % 6) >= 3 ? 49152u : 0u)); }
  events_at<1000 * H>(c, std::make_integer_sequence<int, NEV>{});
  GAP();
  wait_bar<younger(0, R, 1000 * H + 1)>();
  GAP();
  k_read<H, 0>(s); k_read<H, 1>(s); k_read<H, 2>(s); k_read<H, 3>(s);
  GAP();
  gaps<H>(c, s, q, pv, st, std::make_integer_sequence<int, 128>{});
  ST(1 + 10 * H + 8);
  float l = xhalf_sum(s.l[0] + s.l[1]);
  constexpr float LIM_HI = (float)(1ull << LIMLOG), LIM_LO = 1.0f / LIM_HI;
  if (__builtin_expect(__any(!(l <= LIM_HI && l >= LIM_LO)), 0)) fallback(c, H ? c.hB : c.hA, s.o, l);
  const float il = __builtin_amdgcn_rcpf(l);
  if constexpr (H == 0) {
#pragma unroll
    for (int db = 0; db < 4; ++db) pv.o[db] = s.o[db];
    pv.il = il; pv.ow = stage_base(c);
    GAP();
    wait_vm<younger(1, 3, 1000)>();
    GAP();
    read_q(c, q);
  } else {
    const unsigned ow = stage_base(c);
    pack_item<0>(s.o, il, ow); pack_item<1>(s.o, il, ow); pack_item<2>(s.o, il, ow); pack_item<3>(s.o, il, ow);
    pack_item<4>(s.o, il, ow); pack_item<5>(s.o, il, ow); pack_item<6>(s.o, il, ow); pack_item<7>(s.o, il, ow);
    pack_item<8>(s.o, il, ow); pack_item<9>(s.o, il, ow); pack_item<10>(s.o, il, ow); pack_item<11>(s.o, il, ow);
    pack_item<12>(s.o, il, ow); pack_item<13>(s.o, il, ow); pack_item<14>(s.o, il, ow); pack_item<15>(s.o, il, ow);
    store_o<0, 8>(c, c.hB);
  }
  GAP();
}

__global__ __launch_bounds__(512, 2) void attn_fwd(const void* __restrict__ Q, const void* __restrict__ K, const void* __restrict__ V, void* __restrict__ O, unsigned* __restrict__ dbg) {
  Stamps st;
  unsigned long long rt0; asm volatile("s_memrealtime %0\n\ts_waitcnt lgkmcnt(0)" : "=s"(rt0) :: "memory");
  for (int i = 0; i < NST; ++i) st.t[i] = 0;
  ST(0);
  Ctx c;
  const unsigned tid = threadIdx.x, lane = tid & 63u;
  c.lane = lane;
  c.wid = __builtin_amdgcn_readfirstlane(tid >> 6);
  const int nbytes = NHEADS * (int)HEADB;
  c.sq = __builtin_amdgcn_make_buffer_rsrc((void*)Q, (short)0, nbytes, 0x00020000);
  c.sk = __builtin_amdgcn_make_buffer_rsrc((void*)K, (short)0, nbytes, 0x00020000);
  c.sv = __builtin_amdgcn_make_buffer_rsrc((void*)V, (short)0, nbytes, 0x00020000);
  c.so = __builtin_amdgcn_make_buffer_rsrc((void*)O, (short)0, nbytes, 0x00020000);
  c.pq = (const char*)Q; c.pk = (const char*)K; c.pv = (const char*)V;
  {
    const unsigned r = lane >> 4, p = lane & 15u;
    c.v0 = r * 256u + 16u * (p ^ (r << 2));
#pragma unroll
    for (int j = 0; j < 4; ++j) c.vs[j] = opaque(c.v0 ^ (16u * j));
    const unsigned w2 = (32u * c.wid) & 48u;
    c.vkv[0] = opaque(c.v0 ^ w2); c.vkv[1] = opaque(c.v0 ^ (w2 + 16u));
  }
  { const unsigned b = blockIdx.x;
    const unsigned h0 = HMAP == 0 ? b : (HMAP == 1 ? 2u * b : (b & 7u) * 64u + (b >> 3) * 2u);
    c.hA = h0 * HEADB; c.hB = (h0 + (HMAP == 0 ? (unsigned)NWG : 1u)) * HEADB; }
  asm volatile("s_nop 4");
  dma_q<0>(c, c.hA); dma_q<1>(c, c.hA); dma_q<2>(c, c.hA); dma_q<3>(c, c.hA);
  dma_ring<0>(c); dma_ring<1>(c);
  bf16x8 q[8];
  GAP();
  wait_vm<4>();
  GAP();
  read_q(c, q);
  Prev pv;
  head<0>(c, q, pv, st);
  head<1>(c, q, pv, st);
  ST(21);
  asm volatile("s_waitcnt vmcnt(0)" ::: "memory");
  ST(22);
  unsigned long long rt1; asm volatile("s_memrealtime %0\n\ts_waitcnt lgkmcnt(0)" : "=s"(rt1) :: "memory");
  st.t[38] = (unsigned)rt0; st.t[39] = (unsigned)rt1;
  { const unsigned target = 6000u + (SIDE == 0 ? 0u : st.t[SIDE] - (unsigned)rt0);
    for (;;) { unsigned long long now; asm volatile("s_memrealtime %0\n\ts_waitcnt lgkmcnt(0)" : "=s"(now) :: "memory"); if ((unsigned)now - (unsigned)rt0 >= target) break; __builtin_amdgcn_s_sleep(2); } }
}

extern "C" void kernel_launch(void* const* d_in, const int* in_sizes, int n_in, void* d_out, int out_size, void* d_ws, size_t ws_size, hipStream_t stream) {
  (void)in_sizes; (void)n_in; (void)out_size; (void)ws_size;
  (void)hipFuncSetAttribute((const void*)attn_fwd, hipFuncAttributeMaxDynamicSharedMemorySize, (int)LDS_BYTES);
  hipLaunchKernelGGL(attn_fwd, dim3(NWG), dim3(512), LDS_BYTES, stream, d_in[0], d_in[1], d_in[2], d_out, (unsigned*)d_ws);
}
```

```cpp
#include <hip/hip_runtime.h>
#include <hip/hip_bf16.h>
#include <utility>

typedef float    f32x16 __attribute__((ext_vector_type(16)));
typedef float    f32x2  __attribute__((ext_vector_type(2)));
typedef __bf16   bf16x8 __attribute__((ext_vector_type(8)));
typedef __bf16   bf16x2 __attribute__((ext_vector_type(2)));
typedef short    s16x4  __attribute__((ext_vector_type(4)));
typedef short    s16x8  __attribute__((ext_vector_type(8)));
typedef unsigned u32x4  __attribute__((ext_vector_type(4)));
typedef unsigned u32x2  __attribute__((ext_vector_type(2)));
typedef __attribute__((address_space(3))) const bf16x8* lds_b8p;
typedef __attribute__((address_space(3))) s16x4* lds_s4p;
typedef __attribute__((address_space(3))) u32x2* lds_u2p;
typedef __attribute__((address_space(3))) const u32x4* lds_u4p;

#define DI __device__ __forceinline__
#define SPINBASE 6000u
#define GAP() __builtin_amdgcn_sched_barrier(0)
#define PIN(x) asm volatile("" : "+v"(x))
#define LDPOL " nt"
#define STAUX 16
#define BOFF 8

constexpr unsigned HEADB = 65536u;
constexpr unsigned RQO = 0u, RING = 65536u, LDS_BYTES = 163840u;
constexpr int NHEADS = 512, NWG = 256;
constexpr float CEXP = 0.08838834764831845f * 1.4426950408889634f;
constexpr int PFK = 4, PFV = 3;

DI unsigned cvtpk(float lo, float hi) { f32x2 v = {lo, hi}; bf16x2 b = __builtin_convertvector(v, bf16x2); return __builtin_bit_cast(unsigned, b); }
DI float fmax2(float a, float b) { return __builtin_elementwise_maximum(a, b); }
DI float fmax3(float a, float b, float c) { return __builtin_elementwise_maximum(__builtin_elementwise_maximum(a, b), c); }
DI unsigned opaque(unsigned x) { asm volatile("" : "+v"(x)); return x; }

struct Stamps { int dummy; };
#define ST(i) ((void)0)

DI void dma2(unsigned m0v, unsigned v0, unsigned v1, __amdgpu_buffer_rsrc_t srd, unsigned soff) {
  asm volatile("s_mov_b32 m0, %0\n\ts_nop 0\n\t"
               "buffer_load_dwordx4 %1, %3, %4 offen" LDPOL " lds\n\t"
               "buffer_load_dwordx4 %2, %3, %4 offen offset:1024" LDPOL " lds"
               :: "s"(m0v), "v"(v0), "v"(v1), "s"(srd), "s"(soff) : "memory");
}
template <int N> DI void wait_bar() { asm volatile("s_waitcnt vmcnt(%0)\n\ts_barrier" :: "i"(N) : "memory"); }
template <int N> DI void wait_vm() { asm volatile("s_waitcnt vmcnt(%0)" :: "i"(N) : "memory"); }

struct Ctx {
  __amdgpu_buffer_rsrc_t sq, sk, sv, so;
  unsigned wid, lane;
  unsigned v0;
  unsigned hA, hB;
};
constexpr unsigned slot_of(int i) { return RING + 16384u * (unsigned)(i % 6); }

template <int I> DI void dma_ring(const Ctx& c) {
  const unsigned w2 = (32u * c.wid) & 48u;
  dma2(slot_of(I) + c.wid * 2048u, c.v0 ^ w2, c.v0 ^ (w2 + 16u), (I & 4) ? c.sv : c.sk, ((I & 8) ? c.hB : c.hA) + 16384u * (I & 3) + c.wid * 2048u);
}
template <int T> DI void dma_q(const Ctx& c, unsigned hoff) {
  const unsigned o = c.wid * 8192u + 2048u * T;
  dma2(RQO + o, c.v0 ^ (16u * ((2 * T) & 3)), c.v0 ^ (16u * ((2 * T + 1) & 3)), c.sq, hoff + o);
}
DI unsigned row_base(unsigned lane) { const unsigned l32 = lane & 31u, h = lane >> 5, f = ((l32 & 3u) << 2) | ((l32 >> 2) & 3u); return l32 * 256u + 16u * (f ^ h); }
DI void read_q(const Ctx& c, bf16x8 (&q)[8]) {
  const unsigned b = row_base(opaque(c.lane)) + RQO + c.wid * 8192u;
#pragma unroll
  for (int d0 = 0; d0 < 8; ++d0) q[d0] = *(lds_b8p)(b ^ (32u * d0));
}
template <int I0, int I1> DI void store_o(const Ctx& c, unsigned hoff) {
  const unsigned lane = opaque(c.lane), rr = lane >> 4, cc = lane & 15u;
  const unsigned b0 = RQO + c.wid * 8192u + rr * 256u + 16u * (cc ^ rr), b1 = b0 ^ 64u;
  u32x4 w[8];
#pragma unroll
  for (int i = I0; i < I1; ++i) w[i] = *(lds_u4p)(((i & 1) ? b1 : b0) + 1024u * i);
#pragma unroll
  for (int i = I0; i < I1; ++i) __builtin_amdgcn_raw_buffer_store_b128(w[i], c.so, lane * 16u + 1024u * i, hoff + c.wid * 8192u, STAUX);
}

struct Hd {
  f32x16 S[8], o[4];
  bf16x8 kf[64];
  s16x4 vlo[64], vhi[64];
  u32x4 P[2];
  unsigned ka[8], va[8];
  float mx[2], l[2], mc;
};

template <int H, int G> DI void events(const Ctx& c) {
  constexpr int R = 8 * H;
  if constexpr ((G + BOFF) % 16 == 0 && G + BOFF >= 16 && G + BOFF <= 112) {
    constexpr int j = (G + BOFF) / 16;
    constexpr int pos[12] = {-1, 14, 28, 42, 56, 67, 77, 87, 97, 107, 117, 127};
    constexpr int ej = j - 2;
    int n = 0;
    for (int e = ej + 1; e < 12; ++e) if (pos[e] < G) n += (H == 1 && e >= 10) ? 0 : 2;
    constexpr int N = []() constexpr { int n = 0; for (int e = ej + 1; e < 12; ++e) if (pos[e] < G) n += (H == 1 && e >= 10) ? 0 : 2; return n; }();
    (void)n;
    wait_bar<N>();
  }
  if constexpr (G == 14) dma_ring<R + 3>(c);
  if constexpr (G == 28) dma_ring<R + 4>(c);
  if constexpr (G == 42) dma_ring<R + 5>(c);
  if constexpr (G == 56) dma_ring<R + 6>(c);
  if constexpr (G == 67) dma_ring<R + 7>(c);
  if constexpr (H == 0) {
    if constexpr (G == 77) dma_q<0>(c, c.hB);
    if constexpr (G == 87) dma_q<1>(c, c.hB);
    if constexpr (G == 97) dma_q<2>(c, c.hB);
    if constexpr (G == 107) dma_q<3>(c, c.hB);
    if constexpr (G == 117) dma_ring<8>(c);
    if constexpr (G == 127) dma_ring<9>(c);
  } else {
    if constexpr (G == 77) store_o<0, 2>(c, c.hA);
    if constexpr (G == 87) store_o<2, 4>(c, c.hA);
    if constexpr (G == 97) store_o<4, 6>(c, c.hA);
    if constexpr (G == 107) store_o<6, 8>(c, c.hA);
  }
}
template <int H, int G> DI void k_read(Hd& s) {
  constexpr int I = 8 * H + (G >> 4);
  s.kf[G] = *(lds_b8p)(s.ka[G & 7] + 16384u * ((I % 6) % 3) + 8192u * ((G >> 3) & 1));
}
template <int H, int GP> DI void v_read(Hd& s) {
  constexpr int I = 8 * H + 4 + (GP >> 4), ks = (GP >> 2) & 3, db = GP & 3;
  s.vlo[GP] = __builtin_amdgcn_ds_read_tr16_b64_v4i16((lds_s4p)(s.va[2 * db] + 16384u * ((I % 6) % 3) + 4096u * ks));
  s.vhi[GP] = __builtin_amdgcn_ds_read_tr16_b64_v4i16((lds_s4p)(s.va[2 * db + 1] + 16384u * ((I % 6) % 3) + 4096u * ks + 2048u));
}
template <int IPREV, int INEXT> DI void rebase(unsigned (&a)[8]) {
  if constexpr ((IPREV % 6) / 3 != (INEXT % 6) / 3) {
#pragma unroll
    for (int i = 0; i < 8; ++i) a[i] = opaque((INEXT % 6) >= 3 ? a[i] + 49152u : a[i] - 49152u);
  }
}
DI void sm_pair(Hd& s, int ks, int pr) {
  const int kb = ks >> 1, r = 8 * (ks & 1) + 2 * pr;
  const float e0 = __builtin_amdgcn_exp2f(__builtin_fmaf(s.S[kb][r], CEXP, s.mc));
  const float e1 = __builtin_amdgcn_exp2f(__builtin_fmaf(s.S[kb][r + 1], CEXP, s.mc));
  s.l[0] += e0; s.l[1] += e1;
  unsigned w = cvtpk(e0, e1);
  PIN(s.l[0]); PIN(s.l[1]); PIN(w);
  s.P[ks & 1][pr] = w;
}
DI void va_init(const Ctx& c, Hd& s, unsigned base) {
  const unsigned lane = opaque(c.lane), h = lane >> 5, blk = (lane >> 4) & 1u, q4 = (lane & 15u) >> 2, p = lane & 3u;
  const unsigned vb = 256u * (4u * h + q4) + 16u * (4u * q4 + ((2u * blk + (p >> 1)) ^ h)) + 8u * (p & 1u);
#pragma unroll
  for (int i = 0; i < 8; ++i) s.va[i] = opaque((vb ^ (64u * (i >> 1)) ^ (32u * (i & 1))) + base);
}

template <int H, int G> DI void gap(const Ctx& c, Hd& s, const bf16x8 (&q)[8], Stamps& st) {
  constexpr int R = 8 * H;
  events<H, G>(c);
  if constexpr (G < 64) {
    if constexpr (G + PFK < 64) {
      if constexpr (((G + PFK) & 15) == 0) rebase<R + ((G + PFK) >> 4) - 1, R + ((G + PFK) >> 4)>(s.ka);
      k_read<H, G + PFK>(s);
    }
    if constexpr (G == 60) va_init(c, s, RING + (((R + 4) % 6) >= 3 ? 49152u : 0u));
    if constexpr (G + PFV >= 64) v_read<H, G + PFV - 64>(s);
    constexpr int kb = G >> 3, d0 = G & 7;
    if constexpr (d0 == 0) s.S[kb] = __builtin_amdgcn_mfma_f32_32x32x16_bf16(s.kf[G], q[d0], f32x16{}, 0, 0, 0);
    else                   s.S[kb] = __builtin_amdgcn_mfma_f32_32x32x16_bf16(s.kf[G], q[d0], s.S[kb], 0, 0, 0);
    PIN(s.S[kb]);
    if constexpr (kb >= 1 && d0 >= 2 && d0 < 6) {
      constexpr int pb = kb - 1, r = 4 * (d0 - 2);
      if constexpr (pb == 0 && d0 == 2) { s.mx[0] = fmax2(s.S[pb][r], s.S[pb][r + 1]); s.mx[1] = fmax2(s.S[pb][r + 2], s.S[pb][r + 3]); }
      else { s.mx[0] = fmax3(s.mx[0], s.S[pb][r], s.S[pb][r + 1]); s.mx[1] = fmax3(s.mx[1], s.S[pb][r + 2], s.S[pb][r + 3]); }
      PIN(s.mx[0]); PIN(s.mx[1]);
    }
    if constexpr (G == 63) {
#pragma unroll
      for (int r = 0; r < 16; r += 4) { s.mx[0] = fmax3(s.mx[0], s.S[7][r], s.S[7][r + 1]); s.mx[1] = fmax3(s.mx[1], s.S[7][r + 2], s.S[7][r + 3]); }
      float m = fmax2(s.mx[0], s.mx[1]);
      auto rr = __builtin_amdgcn_permlane32_swap(__float_as_uint(m), __float_as_uint(m), false, false);
      m = fmax2(__uint_as_float(rr[0]), __uint_as_float(rr[1]));
      s.mc = -m * CEXP;
      s.l[0] = 0.f; s.l[1] = 0.f;
#pragma unroll
      for (int pr = 0; pr < 4; ++pr) sm_pair(s, 0, pr);
    }
  } else {
    constexpr int GP = G - 64, ks = GP >> 2, db = GP & 3;
    if constexpr (GP + PFV < 64) {
      if constexpr (((GP + PFV) & 15) == 0) rebase<R + 4 + ((GP + PFV) >> 4) - 1, R + 4 + ((GP + PFV) >> 4)>(s.va);
      v_read<H, GP + PFV>(s);
    }
    const s16x8 vf = __builtin_shufflevector(s.vlo[GP], s.vhi[GP], 0, 1, 2, 3, 4, 5, 6, 7);
    if constexpr (ks == 0) s.o[db] = __builtin_amdgcn_mfma_f32_32x32x16_bf16(__builtin_bit_cast(bf16x8, vf), __builtin_bit_cast(bf16x8, s.P[0]), f32x16{}, 0, 0, 0);
    else                   s.o[db] = __builtin_amdgcn_mfma_f32_32x32x16_bf16(__builtin_bit_cast(bf16x8, vf), __builtin_bit_cast(bf16x8, s.P[ks & 1]), s.o[db], 0, 0, 0);
    PIN(s.o[db]);
    if constexpr (ks + 1 < 16) sm_pair(s, ks + 1, db);
  }
  GAP();
}
template <int H, int... Gs> DI void gaps(const Ctx& c, Hd& s, const bf16x8 (&q)[8], Stamps& st, std::integer_sequence<int, Gs...>) { (gap<H, Gs>(c, s, q, st), ...); }

template <int H>
DI void head(const Ctx& c, bf16x8 (&q)[8], Stamps& st) {
  constexpr int R = 8 * H;
  Hd s;
  { const unsigned ra = row_base(opaque(c.lane));
#pragma unroll
    for (int d0 = 0; d0 < 8; ++d0) s.ka[d0] = opaque((ra ^ (32u * d0)) + RING + ((R % 6) >= 3 ? 49152u : 0u)); }
  dma_ring<R + 2>(c);
  GAP();
  wait_bar<4>();
  GAP();
  k_read<H, 0>(s); k_read<H, 1>(s); k_read<H, 2>(s); k_read<H, 3>(s);
  GAP();
  gaps<H>(c, s, q, st, std::make_integer_sequence<int, 128>{});
  ST(1 + 9 * H + 8 + (H ? 1 : 0));
  float ls = s.l[0] + s.l[1];
  { auto rr = __builtin_amdgcn_permlane32_swap(__float_as_uint(ls), __float_as_uint(ls), false, false);
    ls = __uint_as_float(rr[0]) + __uint_as_float(rr[1]); }
  const float il = __builtin_amdgcn_rcpf(ls);
  u32x2 w[16];
#pragma unroll
  for (int db = 0; db < 4; ++db)
#pragma unroll
    for (int g = 0; g < 4; ++g)
      w[4 * db + g] = u32x2{cvtpk(s.o[db][4 * g] * il, s.o[db][4 * g + 1] * il), cvtpk(s.o[db][4 * g + 2] * il, s.o[db][4 * g + 3] * il)};
  GAP();
  if constexpr (H == 0) {
    wait_vm<4>();
    GAP();
    read_q(c, q);
    GAP(); asm volatile("s_waitcnt lgkmcnt(0)" ::: "memory"); GAP();
  }
  {
    const unsigned lane = opaque(c.lane), l32 = lane & 31u, h = lane >> 5;
    const unsigned ow = RQO + c.wid * 8192u + l32 * 256u + 16u * (l32 & 7u) + 8u * h;
#pragma unroll
    for (int i = 0; i < 16; ++i) *(lds_u2p)(ow ^ (16u * i)) = w[i];
  }
  if constexpr (H == 1) store_o<0, 8>(c, c.hB);
  GAP();
}

__global__ __launch_bounds__(512, 2) void attn_fwd(const void* __restrict__ Q, const void* __restrict__ K, const void* __restrict__ V, void* __restrict__ O, unsigned* __restrict__ dbg) {
  Stamps st;
  ST(0);
  Ctx c;
  const unsigned tid = threadIdx.x, lane = tid & 63u;
  c.lane = lane;
  c.wid = __builtin_amdgcn_readfirstlane(tid >> 6);
  const int nbytes = NHEADS * (int)HEADB;
  c.sq = __builtin_amdgcn_make_buffer_rsrc((void*)Q, (short)0, nbytes, 0x00020000);
  c.sk = __builtin_amdgcn_make_buffer_rsrc((void*)K, (short)0, nbytes, 0x00020000);
  c.sv = __builtin_amdgcn_make_buffer_rsrc((void*)V, (short)0, nbytes, 0x00020000);
  c.so = __builtin_amdgcn_make_buffer_rsrc((void*)O, (short)0, nbytes, 0x00020000);
  {
    const unsigned r = lane >> 4, p = lane & 15u;
    c.v0 = r * 256u + 16u * (p ^ (r << 2));
  }
  c.hA = blockIdx.x * HEADB; c.hB = (blockIdx.x + NWG) * HEADB;
  asm volatile("s_nop 4");
  dma_q<0>(c, c.hA); dma_q<1>(c, c.hA); dma_q<2>(c, c.hA); dma_q<3>(c, c.hA);
  dma_ring<0>(c); dma_ring<1>(c);
  bf16x8 q[8];
  GAP();
  wait_vm<4>();
  GAP();
  ST(32);
  read_q(c, q);
  head<0>(c, q, st);
  head<1>(c, q, st);
}

extern "C" void kernel_launch(void* const* d_in, const int* in_sizes, int n_in, void* d_out, int out_size, void* d_ws, size_t ws_size, hipStream_t stream) {
  (void)in_sizes; (void)n_in; (void)out_size; (void)ws_size;
  (void)hipFuncSetAttribute((const void*)attn_fwd, hipFuncAttributeMaxDynamicSharedMemorySize, (int)LDS_BYTES);
  hipLaunchKernelGGL(attn_fwd, dim3(NWG), dim3(512), LDS_BYTES, stream, d_in[0], d_in[1], d_in[2], d_out, (unsigned*)d_ws);
}
```

```cpp
#include <hip/hip_runtime.h>
#include <hip/hip_bf16.h>
#include <utility>

typedef float    f32x16 __attribute__((ext_vector_type(16)));
typedef float    f32x2  __attribute__((ext_vector_type(2)));
typedef float    f32x4  __attribute__((ext_vector_type(4)));
typedef __bf16   bf16x8 __attribute__((ext_vector_type(8)));
typedef __bf16   bf16x2 __attribute__((ext_vector_type(2)));
typedef short    s16x4  __attribute__((ext_vector_type(4)));
typedef short    s16x8  __attribute__((ext_vector_type(8)));
typedef unsigned u32x4  __attribute__((ext_vector_type(4)));
typedef unsigned u32x2  __attribute__((ext_vector_type(2)));
typedef __attribute__((address_space(3))) const bf16x8* lds_b8p;
typedef __attribute__((address_space(3))) s16x4* lds_s4p;
typedef __attribute__((address_space(3))) u32x2* lds_u2p;
typedef __attribute__((address_space(3))) const u32x4* lds_u4p;

#define DI __device__ __forceinline__
#define GAP() __builtin_amdgcn_sched_barrier(0)
#define PIN(x) asm volatile("" : "+v"(x))
#define LDPOL " nt"
#define STAUX 18
#define BOFF 8
#define HMAP 2
#define MSUM 0
#define FINE 1
#define QK0 15
#define PVEND 58
#define NQK 32
#define PRIO 1
#define PSTEP 4
#define OOL 1
#define LIMLOG 30

constexpr unsigned HEADB = 65536u;
constexpr unsigned RQO = 0u, RING = 65536u, LDS_BYTES = 163840u;
constexpr int NHEADS = 512, NWG = 256;
constexpr float CEXP = 0.08838834764831845f * 1.4426950408889634f;
constexpr int PFK = 4, PFV = 3;

DI unsigned cvtpk(float lo, float hi) { f32x2 v = {lo, hi}; bf16x2 b = __builtin_convertvector(v, bf16x2); return __builtin_bit_cast(unsigned, b); }
DI float fmax2(float a, float b) { return __builtin_elementwise_maximum(a, b); }
DI float fmax3(float a, float b, float c) { return __builtin_elementwise_maximum(__builtin_elementwise_maximum(a, b), c); }
DI unsigned opaque(unsigned x) { asm volatile("" : "+v"(x)); return x; }
DI float xhalf_max(float m) { auto rr = __builtin_amdgcn_permlane32_swap(__float_as_uint(m), __float_as_uint(m), false, false); return fmax2(__uint_as_float(rr[0]), __uint_as_float(rr[1])); }
DI float xhalf_sum(float m) { auto rr = __builtin_amdgcn_permlane32_swap(__float_as_uint(m), __float_as_uint(m), false, false); return __uint_as_float(rr[0]) + __uint_as_float(rr[1]); }

struct Stamps { int dummy; };
#define ST(i) ((void)0)

DI void dma2(unsigned m0v, unsigned v0, unsigned v1, __amdgpu_buffer_rsrc_t srd, unsigned soff) {
  asm volatile("s_mov_b32 m0, %0\n\ts_nop 0\n\t"
               "buffer_load_dwordx4 %1, %3, %4 offen" LDPOL " lds\n\t"
               "buffer_load_dwordx4 %2, %3, %4 offen offset:1024" LDPOL " lds"
               :: "s"(m0v), "v"(v0), "v"(v1), "s"(srd), "s"(soff) : "memory");
}
template <int N> DI void wait_bar() { asm volatile("s_waitcnt vmcnt(%0)\n\ts_barrier" :: "i"(N) : "memory"); }
template <int N> DI void wait_vm() { asm volatile("s_waitcnt vmcnt(%0)" :: "i"(N) : "memory"); }

struct Ctx {
  __amdgpu_buffer_rsrc_t sq, sk, sv, so;
  const char *pq, *pk, *pv;
  unsigned wid, lane;
  unsigned v0;
  unsigned vs[4], vkv[2];
  unsigned hA, hB;
};
constexpr unsigned slot_of(int i) { return RING + 16384u * (unsigned)(i % 6); }

struct Ev { int pos, len, kind, idx, half; };
#define U2(p0, p1, k, i) {p0, 0, k, i, 0}, {p1, 0, k, i, 1}
#define W(p, l, k, i, h) {p, l, k, i, h}
#define EVTAB \
  U2(0, 0, 0, 2), W(1, 8, 0, 3, 0), W(9, 16, 0, 3, 1), W(9, 16, 0, 4, 0), W(25, 16, 0, 4, 1), W(25, 16, 0, 5, 0), W(25, 16, 0, 5, 1), \
  W(41, 16, 0, 6, 0), W(41, 16, 0, 6, 1), W(41, 16, 0, 7, 0), W(57, 16, 0, 7, 1), W(57, 16, 1, 0, 0), W(73, 16, 1, 0, 1), W(73, 16, 1, 1, 0), W(73, 16, 1, 1, 1), \
  W(89, 16, 1, 2, 0), W(89, 16, 1, 2, 1), W(105, 16, 1, 3, 0), W(105, 16, 1, 3, 1), W(105, 16, 0, 8, 0), W(121, 8, 0, 8, 1), U2(999, 999, 0, 9), \
  U2(1001, 1007, 0, 10), U2(1013, 1019, 0, 11), U2(1025, 1031, 0, 12), U2(1037, 1043, 0, 13), U2(1049, 1055, 0, 14), U2(1061, 1067, 0, 15), \
  {1073, 0, 2, 0, 0}, {1073, 0, 2, 1, 0}, {1085, 0, 2, 2, 0}, {1085, 0, 2, 3, 0}, {1097, 0, 2, 4, 0}, {1097, 0, 2, 5, 0}, {1109, 0, 2, 6, 0}, {1109, 0, 2, 7, 0}
constexpr Ev EV[] = {EVTAB};
constexpr int NEV = sizeof(EV) / sizeof(EV[0]);
constexpr int younger(int kind, int idx, int pos) {
  int e0 = -1;
  for (int e = 0; e < NEV; ++e) if (EV[e].kind == kind && EV[e].idx == idx) e0 = e;
  int n = 0;
  if (e0 < 0) n = (kind == 0 && idx == 0) ? 2 : 0;
  for (int e = e0 + 1; e < NEV; ++e) if (EV[e].pos < pos) ++n;
  return n;
}
constexpr bool ev_ok() {
  for (int e = 1; e < NEV; ++e) if (EV[e].pos < EV[e - 1].pos) return false;
  for (int e = 0; e < NEV; ++e) if (EV[e].len > 0) { const int g0 = (EV[e].pos - 1) % 1000, g1 = g0 + EV[e].len - 1; if ((g0 + BOFF) / 16 != (g1 + BOFF) / 16) return false; }
  return true;
}
static_assert(ev_ok(), "events in program order, windows inside one barrier interval");
constexpr int win_units(int e) { int n = 0; for (int x = 0; x < NEV; ++x) if (EV[x].pos == EV[e].pos && EV[x].len == EV[e].len) ++n; return n; }
constexpr int win_rank(int e) { int n = 0; for (int x = 0; x < e; ++x) if (EV[x].pos == EV[e].pos && EV[x].len == EV[e].len) ++n; return n; }

template <int I> DI void dma_ring(const Ctx& c) {
  const unsigned w2 = (32u * c.wid) & 48u;
  (void)w2;
  dma2(slot_of(I) + c.wid * 2048u, c.vkv[0], c.vkv[1], (I & 4) ? c.sv : c.sk, ((I & 8) ? c.hB : c.hA) + 16384u * (I & 3) + c.wid * 2048u);
}
template <int T> DI void dma_q(const Ctx& c, unsigned hoff) {
  const unsigned o = c.wid * 8192u + 2048u * T;
  dma2(RQO + o, c.vs[(2 * T) & 3], c.vs[(2 * T + 1) & 3], c.sq, hoff + o);
}
DI unsigned row_base(unsigned lane) { const unsigned l32 = lane & 31u, h = lane >> 5, f = ((l32 & 3u) << 2) | ((l32 >> 2) & 3u); return l32 * 256u + 16u * (f ^ h); }
DI void read_q(const Ctx& c, bf16x8 (&q)[8]) {
  const unsigned b = row_base(opaque(c.lane)) + RQO + c.wid * 8192u;
#pragma unroll
  for (int d0 = 0; d0 < 8; ++d0) q[d0] = *(lds_b8p)(b ^ (32u * d0));
}
template <int I0, int I1> DI void store_o(const Ctx& c, unsigned hoff) {
  const unsigned lane = opaque(c.lane), rr = lane >> 4, cc = lane & 15u;
  const unsigned b0 = RQO + c.wid * 8192u + rr * 256u + 16u * (cc ^ rr), b1 = b0 ^ 64u;
  u32x4 w[8];
#pragma unroll
  for (int i = I0; i < I1; ++i) w[i] = *(lds_u4p)(((i & 1) ? b1 : b0) + 1024u * i);
  const unsigned go = hoff + c.wid * 8192u + lane * 16u;
#pragma unroll
  for (int i = I0; i < I1; ++i) __builtin_amdgcn_raw_buffer_store_b128(w[i], c.so, go + 1024u * i, 0, STAUX);
}
template <int IMM> DI void dma1(unsigned m0v, unsigned v, __amdgpu_buffer_rsrc_t srd, unsigned soff) {
  asm volatile("s_mov_b32 m0, %0\n\ts_nop 0\n\tbuffer_load_dwordx4 %1, %2, %3 offen offset:%4" LDPOL " lds" :: "s"(m0v), "v"(v), "s"(srd), "s"(soff), "i"(IMM) : "memory");
}
template <int SEL, int IMM> DI void dma1_if(unsigned sel, unsigned m0v, unsigned v, __amdgpu_buffer_rsrc_t srd, unsigned soff) {
  asm volatile("s_cmp_eq_u32 %0, %5\n\ts_cbranch_scc0 1f\n\ts_mov_b32 m0, %1\n\ts_nop 0\n\t"
               "buffer_load_dwordx4 %2, %3, %4 offen offset:%6" LDPOL " lds\n1:"
               :: "s"(sel), "s"(m0v), "v"(v), "s"(srd), "s"(soff), "i"(SEL), "i"(IMM) : "memory", "scc");
}
template <int E, int WV> DI void run_event(const Ctx& c) {
  constexpr int X = EV[E].idx, I = EV[E].half;
  if constexpr (EV[E].kind == 0) {
    const unsigned m0v = slot_of(X) + c.wid * 2048u, so = ((X & 8) ? c.hB : c.hA) + 16384u * (X & 3) + c.wid * 2048u;
    if constexpr (WV < 0) dma1<1024 * I>(m0v, c.vkv[I], (X & 4) ? c.sv : c.sk, so); else dma1_if<WV, 1024 * I>(c.wid, m0v, c.vkv[I], (X & 4) ? c.sv : c.sk, so);
  } else if constexpr (EV[E].kind == 1) {
    const unsigned m0v = RQO + c.wid * 8192u + 2048u * X, so = c.hB + c.wid * 8192u + 2048u * X;
    if constexpr (WV < 0) dma1<1024 * I>(m0v, c.vs[(2 * X + I) & 3], c.sq, so); else dma1_if<WV, 1024 * I>(c.wid, m0v, c.vs[(2 * X + I) & 3], c.sq, so);
  } else store_o<X, X + 1>(c, c.hA);
}
template <int POS, int E, int... Ws> DI void window_step(const Ctx& c, std::integer_sequence<int, Ws...>) {
  constexpr int t = POS - EV[E].pos, k = win_units(E), r = win_rank(E), L = EV[E].len;
  ([&]() { if constexpr (((8 * r + Ws) * L) / (8 * k) == t) run_event<E, Ws>(c); }(), ...);
}
template <int POS, int E> DI void event_step(const Ctx& c) {
  if constexpr (EV[E].len == 0 || !FINE) { if constexpr (EV[E].len == 0 ? EV[E].pos == POS : POS == EV[E].pos + (win_rank(E) * EV[E].len) / win_units(E)) run_event<E, -1>(c); }
  else if constexpr (POS >= EV[E].pos && POS < EV[E].pos + EV[E].len) window_step<POS, E>(c, std::make_integer_sequence<int, 8>{});
}
template <int POS, int... Es> DI void events_at(const Ctx& c, std::integer_sequence<int, Es...>) { (event_step<POS, Es>(c), ...); }

struct Hd {
  f32x16 S[8], o[4];
  bf16x8 kf[64];
  s16x4 vlo[64], vhi[64];
  u32x4 P[16];
  unsigned ka[8], va[8];
  float mx[2], l[2], mc;
  float x[64][2], e[64][2];
  f32x4 lacc;
  u32x4 ones;
};
struct Prev { f32x16 o[4]; float il; unsigned ow; };

template <int H, int G> DI void k_read(Hd& s) {
  constexpr int I = 8 * H + (G >> 4);
  s.kf[G] = *(lds_b8p)(s.ka[G & 7] + 16384u * ((I % 6) % 3) + 8192u * ((G >> 3) & 1));
}
template <int H, int GP> DI void v_read(Hd& s) {
  constexpr int I = 8 * H + 4 + (GP >> 4), ks = (GP >> 2) & 3, db = GP & 3;
  s.vlo[GP] = __builtin_amdgcn_ds_read_tr16_b64_v4i16((lds_s4p)(s.va[2 * db] + 16384u * ((I % 6) % 3) + 4096u * ks));
  s.vhi[GP] = __builtin_amdgcn_ds_read_tr16_b64_v4i16((lds_s4p)(s.va[2 * db + 1] + 16384u * ((I % 6) % 3) + 4096u * ks + 2048u));
}
template <int IPREV, int INEXT> DI void rebase(unsigned (&a)[8]) {
  if constexpr ((IPREV % 6) / 3 != (INEXT % 6) / 3) {
#pragma unroll
    for (int i = 0; i < 8; ++i) a[i] = opaque((INEXT % 6) >= 3 ? a[i] + 49152u : a[i] - 49152u);
  }
}
DI void va_init(const Ctx& c, Hd& s, unsigned base) {
  const unsigned lane = opaque(c.lane), h = lane >> 5, blk = (lane >> 4) & 1u, q4 = (lane & 15u) >> 2, p = lane & 3u;
  const unsigned vb = 256u * (4u * h + q4) + 16u * (4u * q4 + ((2u * blk + (p >> 1)) ^ h)) + 8u * (p & 1u);
#pragma unroll
  for (int i = 0; i < 8; ++i) s.va[i] = opaque((vb ^ (64u * (i >> 1)) ^ (32u * (i & 1))) + base);
}
constexpr int op_n(int k) { const int j = k / 7, p = k % 7; return (p == 0 || p == 2) ? j : ((p == 1 || p == 3) ? j - 1 : j - 2); }
constexpr bool op_on(int k) { const int n = op_n(k), p = k % 7; return n >= 0 && n < 64 && !(MSUM && (p == 4 || p == 5)); }
constexpr int op_cost(int k) { const int p = k % 7; return !op_on(k) ? 0 : ((p == 1 || p == 3) ? 2 : ((MSUM && p == 6 && (op_n(k) & 3) == 3) ? 3 : 1)); }
constexpr int NOPS = 66 * 7;
struct OpTab { short first[130]; };
constexpr OpTab make_optab() {
  OpTab t{};
  int tot = 0;
  for (int i = 0; i < NOPS; ++i) tot += op_cost(i);
  const int c1 = tot * NQK / 64;
  int c = 0, g = 0;
  for (int k = 0; k < NOPS; ++k) {
    const int gk = c < c1 ? QK0 + c * (64 - QK0) / c1 : 64 + (c - c1) * (PVEND + 1) / (tot - c1);
    while (g <= gk) t.first[g++] = (short)k;
    c += op_cost(k);
  }
  while (g < 130) t.first[g++] = (short)NOPS;
  return t;
}
constexpr OpTab OPT = make_optab();
template <int K> DI void micro_op(Hd& s) {
  if constexpr (op_on(K)) {
    constexpr int n = op_n(K), p = K % 7, ks = n >> 2, kb = ks >> 1, r = 8 * (ks & 1) + 2 * (n & 3);
    if constexpr (p == 0 || p == 2) { s.x[n][p >> 1] = __builtin_fmaf(s.S[kb][r + (p >> 1)], CEXP, s.mc); PIN(s.x[n][p >> 1]); }
    else if constexpr (p == 1 || p == 3) { s.e[n][p >> 1] = __builtin_amdgcn_exp2f(s.x[n][p >> 1]); PIN(s.e[n][p >> 1]); }
    else if constexpr (p == 4 || p == 5) { if constexpr (n == 0) s.l[p - 4] = s.e[n][p - 4]; else s.l[p - 4] += s.e[n][p - 4]; PIN(s.l[p - 4]); }
    else {
      unsigned w = cvtpk(s.e[n][0], s.e[n][1]); PIN(w); s.P[ks][n & 3] = w;
      if constexpr (MSUM && (n & 3) == 3) {
        if constexpr (ks == 0) s.lacc = __builtin_amdgcn_mfma_f32_16x16x32_bf16(__builtin_bit_cast(bf16x8, s.ones), __builtin_bit_cast(bf16x8, s.P[ks]), f32x4{}, 0, 0, 0);
        else                   s.lacc = __builtin_amdgcn_mfma_f32_16x16x32_bf16(__builtin_bit_cast(bf16x8, s.ones), __builtin_bit_cast(bf16x8, s.P[ks]), s.lacc, 0, 0, 0);
        PIN(s.lacc);
      }
    }
  }
}
template <int K0, int... Ks> DI void micro_ops(Hd& s, std::integer_sequence<int, Ks...>) { (micro_op<K0 + Ks>(s), ...); }
template <int I, bool SCALAR> DI void pack_item(const f32x16 (&o)[4], float il, unsigned ow) {
  constexpr int db = I >> 2, g = I & 3;
  float a = o[db][4 * g] * il, b = o[db][4 * g + 1] * il, c = o[db][4 * g + 2] * il, d = o[db][4 * g + 3] * il;
  if constexpr (SCALAR) { PIN(a); PIN(b); PIN(c); PIN(d); }
  const u32x2 w = {cvtpk(a, b), cvtpk(c, d)};
  *(lds_u2p)(ow ^ (16u * I)) = w;
}
DI unsigned stage_base(const Ctx& c) { const unsigned lane = opaque(c.lane), l32 = lane & 31u, h = lane >> 5; return RQO + c.wid * 8192u + l32 * 256u + 16u * (l32 & 7u) + 8u * h; }

template <int H, int G> DI void gap(const Ctx& c, Hd& s, const bf16x8 (&q)[8], const Prev& pv, Stamps& st) {
  constexpr int R = 8 * H;
  if constexpr ((G + BOFF) % 16 == 0 && G + BOFF >= 16 && G + BOFF <= 112) wait_bar<younger(0, R + (G + BOFF) / 16, 1000 * H + G + 1)>();
  if constexpr (PRIO == 1 && ((G + BOFF) % 16) % PSTEP == 0) __builtin_amdgcn_s_setprio(3 - ((((G + BOFF) % 16) / PSTEP) & 3));
  events_at<1000 * H + G + 1>(c, std::make_integer_sequence<int, NEV>{});
  if constexpr (G < 64) {
    if constexpr (G + PFK < 64) {
      if constexpr (((G + PFK) & 15) == 0) rebase<R + ((G + PFK) >> 4) - 1, R + ((G + PFK) >> 4)>(s.ka);
      k_read<H, G + PFK>(s);
    }
    if constexpr (G == 60) va_init(c, s, RING + (((R + 4) % 6) >= 3 ? 49152u : 0u));
    if constexpr (G + PFV >= 64) v_read<H, G + PFV - 64>(s);
    constexpr int kb = G >> 3, d0 = G & 7;
    if constexpr (d0 == 0) s.S[kb] = __builtin_amdgcn_mfma_f32_32x32x16_bf16(s.kf[G], q[d0], f32x16{}, 0, 0, 0);
    else                   s.S[kb] = __builtin_amdgcn_mfma_f32_32x32x16_bf16(s.kf[G], q[d0], s.S[kb], 0, 0, 0);
    PIN(s.S[kb]);
    if constexpr (G == 10) { s.mx[0] = fmax3(s.S[0][0], s.S[0][1], s.S[0][2]); s.mx[1] = fmax3(s.S[0][3], s.S[0][4], s.S[0][5]); PIN(s.mx[0]); PIN(s.mx[1]); }
    if constexpr (G == 11) { s.mx[0] = fmax3(s.mx[0], s.S[0][6], s.S[0][7]); s.mx[1] = fmax3(s.mx[1], s.S[0][8], s.S[0][9]); PIN(s.mx[0]); PIN(s.mx[1]); }
    if constexpr (G == 12) { s.mx[0] = fmax3(s.mx[0], s.S[0][10], s.S[0][11]); s.mx[1] = fmax3(s.mx[1], s.S[0][12], s.S[0][13]); PIN(s.mx[0]); PIN(s.mx[1]); }
    if constexpr (G == 13) { s.mx[0] = fmax3(s.mx[0], s.S[0][14], s.S[0][15]); s.mx[0] = fmax2(s.mx[0], s.mx[1]); PIN(s.mx[0]); }
    if constexpr (G == 14) { s.mc = -xhalf_max(s.mx[0]) * CEXP; PIN(s.mc); }
    if constexpr (H == 1 && G < 16) pack_item<G, true>(pv.o, pv.il, pv.ow);
  } else {
    constexpr int GP = G - 64, ks = GP >> 2, db = GP & 3;
    if constexpr (GP + PFV < 64) {
      if constexpr (((GP + PFV) & 15) == 0) rebase<R + 4 + ((GP + PFV) >> 4) - 1, R + 4 + ((GP + PFV) >> 4)>(s.va);
      v_read<H, GP + PFV>(s);
    }
    const s16x8 vf = __builtin_shufflevector(s.vlo[GP], s.vhi[GP], 0, 1, 2, 3, 4, 5, 6, 7);
    if constexpr (ks == 0) s.o[db] = __builtin_amdgcn_mfma_f32_32x32x16_bf16(__builtin_bit_cast(bf16x8, vf), __builtin_bit_cast(bf16x8, s.P[0]), f32x16{}, 0, 0, 0);
    else                   s.o[db] = __builtin_amdgcn_mfma_f32_32x32x16_bf16(__builtin_bit_cast(bf16x8, vf), __builtin_bit_cast(bf16x8, s.P[ks]), s.o[db], 0, 0, 0);
    PIN(s.o[db]);
  }
  micro_ops<OPT.first[G]>(s, std::make_integer_sequence<int, OPT.first[G + 1] - OPT.first[G]>{});
  GAP();
}
template <int H, int... Gs> DI void gaps(const Ctx& c, Hd& s, const bf16x8 (&q)[8], const Prev& pv, Stamps& st, std::integer_sequence<int, Gs...>) { (gap<H, Gs>(c, s, q, pv, st), ...); }

__device__ __forceinline__ void fallback(const Ctx& c, unsigned hoff, f32x16 (&o)[4], float& l) {
  const unsigned lane = c.lane, l32 = lane & 31u, h = lane >> 5;
  const char* qp = c.pq + hoff + (c.wid * 32u + l32) * 256u + h * 16u;
  const char* kp = c.pk + hoff + l32 * 256u + h * 16u;
  const short* vp = (const short*)(c.pv + hoff) + l32;
  bf16x8 qf[8];
#pragma unroll
  for (int d = 0; d < 8; ++d) qf[d] = *(const bf16x8*)(qp + 32 * d);
  float m = -__builtin_inff();
#pragma nounroll
  for (int kb = 0; kb < 8; ++kb) {
    f32x16 a = {};
#pragma unroll
    for (int d = 0; d < 8; ++d) a = __builtin_amdgcn_mfma_f32_32x32x16_bf16(*(const bf16x8*)(kp + kb * 8192 + 32 * d), qf[d], a, 0, 0, 0);
#pragma unroll
    for (int r = 0; r < 16; ++r) m = fmax2(m, a[r]);
  }
  m = xhalf_max(m);
  float ls = 0.f;
#pragma unroll
  for (int db = 0; db < 4; ++db) o[db] = f32x16{};
#pragma nounroll
  for (int kb = 0; kb < 8; ++kb) {
    f32x16 a = {};
#pragma unroll
    for (int d = 0; d < 8; ++d) a = __builtin_amdgcn_mfma_f32_32x32x16_bf16(*(const bf16x8*)(kp + kb * 8192 + 32 * d), qf[d], a, 0, 0, 0);
#pragma unroll
    for (int sx = 0; sx < 2; ++sx) {
      float e[8];
#pragma unroll
      for (int j = 0; j < 8; ++j) { e[j] = __builtin_amdgcn_exp2f((a[8 * sx + j] - m) * CEXP); ls += e[j]; }
      const u32x4 p = {cvtpk(e[0], e[1]), cvtpk(e[2], e[3]), cvtpk(e[4], e[5]), cvtpk(e[6], e[7])};
#pragma unroll
      for (int db = 0; db < 4; ++db) {
        s16x8 vf;
#pragma unroll
        for (int j = 0; j < 8; ++j) vf[j] = vp[(kb * 32 + 16 * sx + 8 * (j >> 2) + 4 * h + (j & 3)) * 128 + 32 * db];
        o[db] = __builtin_amdgcn_mfma_f32_32x32x16_bf16(__builtin_bit_cast(bf16x8, vf), __builtin_bit_cast(bf16x8, p), o[db], 0, 0, 0);
      }
    }
  }
  l = xhalf_sum(ls);
}

template <int H>
DI void head(const Ctx& c, bf16x8 (&q)[8], Prev& pv, Stamps& st) {
  constexpr int R = 8 * H;
  Hd s;
  { const unsigned ra = row_base(opaque(c.lane));
#pragma unroll
    for (int d0 = 0; d0 < 8; ++d0) s.ka[d0] = opaque((ra ^ (32u * d0)) + RING + ((R % 6) >= 3 ? 49152u : 0u)); }
  if constexpr (MSUM) { const unsigned lane = opaque(c.lane); const unsigned one = (((lane >> 4) ^ (lane >> 2)) & 1u) ? 0u : 0x3F803F80u; s.ones = u32x4{one, one, one, one}; }
  events_at<1000 * H>(c, std::make_integer_sequence<int, NEV>{});
  GAP();
  wait_bar<younger(0, R, 1000 * H + 1)>();
  GAP();
  k_read<H, 0>(s); k_read<H, 1>(s); k_read<H, 2>(s); k_read<H, 3>(s);
  GAP();
  gaps<H>(c, s, q, pv, st, std::make_integer_sequence<int, 128>{});
  ST(1 + 10 * H + 8);
  float l = MSUM ? s.lacc[0] : xhalf_sum(s.l[0] + s.l[1]);
  constexpr float LIM_HI = (float)(1ull << LIMLOG), LIM_LO = 1.0f / LIM_HI;
  if (__builtin_expect(__any(!(l <= LIM_HI && l >= LIM_LO)), 0)) fallback(c, H ? c.hB : c.hA, s.o, l);
  const float il = __builtin_amdgcn_rcpf(l);
  if constexpr (H == 0) {
#pragma unroll
    for (int db = 0; db < 4; ++db) pv.o[db] = s.o[db];
    pv.il = il; pv.ow = stage_base(c);
    GAP();
    events_at<999>(c, std::make_integer_sequence<int, NEV>{});
    GAP();
    wait_vm<younger(1, 3, 1000)>();
    GAP();
    read_q(c, q);
  } else {
    const unsigned ow = stage_base(c);
    pack_item<0, false>(s.o, il, ow); pack_item<1, false>(s.o, il, ow); pack_item<2, false>(s.o, il, ow); pack_item<3, false>(s.o, il, ow);
    pack_item<4, false>(s.o, il, ow); pack_item<5, false>(s.o, il, ow); pack_item<6, false>(s.o, il, ow); pack_item<7, false>(s.o, il, ow);
    pack_item<8, false>(s.o, il, ow); pack_item<9, false>(s.o, il, ow); pack_item<10, false>(s.o, il, ow); pack_item<11, false>(s.o, il, ow);
    pack_item<12, false>(s.o, il, ow); pack_item<13, false>(s.o, il, ow); pack_item<14, false>(s.o, il, ow); pack_item<15, false>(s.o, il, ow);
    store_o<0, 8>(c, c.hB);
  }
  GAP();
}

__global__ __launch_bounds__(512, 2) void attn_fwd(const void* __restrict__ Q, const void* __restrict__ K, const void* __restrict__ V, void* __restrict__ O, unsigned* __restrict__ dbg) {
  Stamps st;
  ST(0);
  Ctx c;
  const unsigned tid = threadIdx.x, lane = tid & 63u;
  c.lane = lane;
  c.wid = __builtin_amdgcn_readfirstlane(tid >> 6);
  const int nbytes = NHEADS * (int)HEADB;
  c.sq = __builtin_amdgcn_make_buffer_rsrc((void*)Q, (short)0, nbytes, 0x00020000);
  c.sk = __builtin_amdgcn_make_buffer_rsrc((void*)K, (short)0, nbytes, 0x00020000);
  c.sv = __builtin_amdgcn_make_buffer_rsrc((void*)V, (short)0, nbytes, 0x00020000);
  c.so = __builtin_amdgcn_make_buffer_rsrc((void*)O, (short)0, nbytes, 0x00020000);
  c.pq = (const char*)Q; c.pk = (const char*)K; c.pv = (const char*)V;
  {
    const unsigned r = lane >> 4, p = lane & 15u;
    c.v0 = r * 256u + 16u * (p ^ (r << 2));
#pragma unroll
    for (int j = 0; j < 4; ++j) c.vs[j] = opaque(c.v0 ^ (16u * j));
    const unsigned w2 = (32u * c.wid) & 48u;
    c.vkv[0] = opaque(c.v0 ^ w2); c.vkv[1] = opaque(c.v0 ^ (w2 + 16u));
  }
  { const unsigned b = blockIdx.x;
    const unsigned h0 = HMAP == 0 ? b : (HMAP == 1 ? 2u * b : (b & 7u) * 64u + (b >> 3) * 2u);
    c.hA = h0 * HEADB; c.hB = (h0 + (HMAP == 0 ? (unsigned)NWG : 1u)) * HEADB; }
  asm volatile("s_nop 4");
  dma_q<0>(c, c.hA); dma_q<1>(c, c.hA); dma_q<2>(c, c.hA); dma_q<3>(c, c.hA);
  dma_ring<0>(c); dma_ring<1>(c);
  bf16x8 q[8];
  GAP();
  wait_vm<4>();
  GAP();
  read_q(c, q);
  Prev pv;
  head<0>(c, q, pv, st);
  head<1>(c, q, pv, st);
}

extern "C" void kernel_launch(void* const* d_in, const int* in_sizes, int n_in, void* d_out, int out_size, void* d_ws, size_t ws_size, hipStream_t stream) {
  (void)in_sizes; (void)n_in; (void)out_size; (void)ws_size;
  (void)hipFuncSetAttribute((const void*)attn_fwd, hipFuncAttributeMaxDynamicSharedMemorySize, (int)LDS_BYTES);
  hipLaunchKernelGGL(attn_fwd, dim3(NWG), dim3(512), LDS_BYTES, stream, d_in[0], d_in[1], d_in[2], d_out, (unsigned*)d_ws);
}
```

```cpp
#include <hip/hip_runtime.h>
#include <hip/hip_bf16.h>
#include <utility>
#define STAMPS
#define RSTAMP
#define ONLYEND
#define SIDE 0

typedef float    f32x16 __attribute__((ext_vector_type(16)));
typedef float    f32x2  __attribute__((ext_vector_type(2)));
typedef float    f32x4  __attribute__((ext_vector_type(4)));
typedef __bf16   bf16x8 __attribute__((ext_vector_type(8)));
typedef __bf16   bf16x2 __attribute__((ext_vector_type(2)));
typedef short    s16x4  __attribute__((ext_vector_type(4)));
typedef short    s16x8  __attribute__((ext_vector_type(8)));
typedef unsigned u32x4  __attribute__((ext_vector_type(4)));
typedef unsigned u32x2  __attribute__((ext_vector_type(2)));
typedef __attribute__((address_space(3))) const bf16x8* lds_b8p;
typedef __attribute__((address_space(3))) s16x4* lds_s4p;
typedef __attribute__((address_space(3))) u32x2* lds_u2p;
typedef __attribute__((address_space(3))) const u32x4* lds_u4p;

#define DI __device__ __forceinline__
#define GAP() __builtin_amdgcn_sched_barrier(0)
#define PIN(x) asm volatile("" : "+v"(x))
#define LDPOL " nt"
#define STAUX 18
#define BOFF 8
#define HMAP 2
#define MSUM 0
#define FINE 1
#define QK0 15
#define PVEND 58
#define NQK 32
#define PRIO 1
#define PSTEP 4
#define OOL 1
#define LIMLOG 30

constexpr unsigned HEADB = 65536u;
constexpr unsigned RQO = 0u, RING = 65536u, LDS_BYTES = 163840u;
constexpr int NHEADS = 512, NWG = 256;
constexpr float CEXP = 0.08838834764831845f * 1.4426950408889634f;
constexpr int PFK = 4, PFV = 3;

DI unsigned cvtpk(float lo, float hi) { f32x2 v = {lo, hi}; bf16x2 b = __builtin_convertvector(v, bf16x2); return __builtin_bit_cast(unsigned, b); }
DI float fmax2(float a, float b) { return __builtin_elementwise_maximum(a, b); }
DI float fmax3(float a, float b, float c) { return __builtin_elementwise_maximum(__builtin_elementwise_maximum(a, b), c); }
DI unsigned opaque(unsigned x) { asm volatile("" : "+v"(x)); return x; }
DI float xhalf_max(float m) { auto rr = __builtin_amdgcn_permlane32_swap(__float_as_uint(m), __float_as_uint(m), false, false); return fmax2(__uint_as_float(rr[0]), __uint_as_float(rr[1])); }
DI float xhalf_sum(float m) { auto rr = __builtin_amdgcn_permlane32_swap(__float_as_uint(m), __float_as_uint(m), false, false); return __uint_as_float(rr[0]) + __uint_as_float(rr[1]); }

#define NST 40
struct Stamps { unsigned t[NST]; };
DI unsigned stamp_now() { unsigned long long t; GAP();
  asm volatile("s_memrealtime %0\n\ts_waitcnt lgkmcnt(0)" : "=s"(t) :: "memory");
  GAP(); return (unsigned)t; }
#define ST(i) do { if ((i) == 0 || (i) == 1 || (i) == 9 || (i) == 11 || (i) == 19 || (i) == 22) st.t[i] = stamp_now(); } while (0)

DI void dma2(unsigned m0v, unsigned v0, unsigned v1, __amdgpu_buffer_rsrc_t srd, unsigned soff) {
  asm volatile("s_mov_b32 m0, %0\n\ts_nop 0\n\t"
               "buffer_load_dwordx4 %1, %3, %4 offen" LDPOL " lds\n\t"
               "buffer_load_dwordx4 %2, %3, %4 offen offset:1024" LDPOL " lds"
               :: "s"(m0v), "v"(v0), "v"(v1), "s"(srd), "s"(soff) : "memory");
}
template <int N> DI void wait_bar() { asm volatile("s_waitcnt vmcnt(%0)\n\ts_barrier" :: "i"(N) : "memory"); }
template <int N> DI void wait_vm() { asm volatile("s_waitcnt vmcnt(%0)" :: "i"(N) : "memory"); }

struct Ctx {
  __amdgpu_buffer_rsrc_t sq, sk, sv, so;
  const char *pq, *pk, *pv;
  unsigned wid, lane;
  unsigned v0;
  unsigned vs[4], vkv[2];
  unsigned hA, hB;
};
constexpr unsigned slot_of(int i) { return RING + 16384u * (unsigned)(i % 6); }

struct Ev { int pos, len, kind, idx, half; };
#define U2(p0, p1, k, i) {p0, 0, k, i, 0}, {p1, 0, k, i, 1}
#define W(p, l, k, i, h) {p, l, k, i, h}
#define EVTAB \
  U2(0, 0, 0, 2), W(1, 8, 0, 3, 0), W(9, 16, 0, 3, 1), W(9, 16, 0, 4, 0), W(25, 16, 0, 4, 1), W(25, 16, 0, 5, 0), W(25, 16, 0, 5, 1), \
  W(41, 16, 0, 6, 0), W(41, 16, 0, 6, 1), W(41, 16, 0, 7, 0), W(57, 16, 0, 7, 1), W(57, 16, 1, 0, 0), W(73, 16, 1, 0, 1), W(73, 16, 1, 1, 0), W(73, 16, 1, 1, 1), \
  W(89, 16, 1, 2, 0), W(89, 16, 1, 2, 1), W(105, 16, 1, 3, 0), W(105, 16, 1, 3, 1), W(105, 16, 0, 8, 0), W(121, 8, 0, 8, 1), U2(999, 999, 0, 9), \
  U2(1001, 1007, 0, 10), U2(1013, 1019, 0, 11), U2(1025, 1031, 0, 12), U2(1037, 1043, 0, 13), U2(1049, 1055, 0, 14), U2(1061, 1067, 0, 15), \
  {1073, 0, 2, 0, 0}, {1073, 0, 2, 1, 0}, {1085, 0, 2, 2, 0}, {1085, 0, 2, 3, 0}, {1097, 0, 2, 4, 0}, {1097, 0, 2, 5, 0}, {1109, 0, 2, 6, 0}, {1109, 0, 2, 7, 0}
constexpr Ev EV[] = {EVTAB};
constexpr int NEV = sizeof(EV) / sizeof(EV[0]);
constexpr int younger(int kind, int idx, int pos) {
  int e0 = -1;
  for (int e = 0; e < NEV; ++e) if (EV[e].kind == kind && EV[e].idx == idx) e0 = e;
  int n = 0;
  if (e0 < 0) n = (kind == 0 && idx == 0) ? 2 : 0;
  for (int e = e0 + 1; e < NEV; ++e) if (EV[e].pos < pos) ++n;
  return n;
}
constexpr bool ev_ok() {
  for (int e = 1; e < NEV; ++e) if (EV[e].pos < EV[e - 1].pos) return false;
  for (int e = 0; e < NEV; ++e) if (EV[e].len > 0) { const int g0 = (EV[e].pos - 1) % 1000, g1 = g0 + EV[e].len - 1; if ((g0 + BOFF) / 16 != (g1 + BOFF) / 16) return false; }
  return true;
}
static_assert(ev_ok(), "events in program order, windows inside one barrier interval");
constexpr int win_units(int e) { int n = 0; for (int x = 0; x < NEV; ++x) if (EV[x].pos == EV[e].pos && EV[x].len == EV[e].len) ++n; return n; }
constexpr int win_rank(int e) { int n = 0; for (int x = 0; x < e; ++x) if (EV[x].pos == EV[e].pos && EV[x].len == EV[e].len) ++n; return n; }

template <int I> DI void dma_ring(const Ctx& c) {
  const unsigned w2 = (32u * c.wid) & 48u;
  (void)w2;
  dma2(slot_of(I) + c.wid * 2048u, c.vkv[0], c.vkv[1], (I & 4) ? c.sv : c.sk, ((I & 8) ? c.hB : c.hA) + 16384u * (I & 3) + c.wid * 2048u);
}
template <int T> DI void dma_q(const Ctx& c, unsigned hoff) {
  const unsigned o = c.wid * 8192u + 2048u * T;
  dma2(RQO + o, c.vs[(2 * T) & 3], c.vs[(2 * T + 1) & 3], c.sq, hoff + o);
}
DI unsigned row_base(unsigned lane) { const unsigned l32 = lane & 31u, h = lane >> 5, f = ((l32 & 3u) << 2) | ((l32 >> 2) & 3u); return l32 * 256u + 16u * (f ^ h); }
DI void read_q(const Ctx& c, bf16x8 (&q)[8]) {
  const unsigned b = row_base(opaque(c.lane)) + RQO + c.wid * 8192u;
#pragma unroll
  for (int d0 = 0; d0 < 8; ++d0) q[d0] = *(lds_b8p)(b ^ (32u * d0));
}
template <int I0, int I1> DI void store_o(const Ctx& c, unsigned hoff) {
  const unsigned lane = opaque(c.lane), rr = lane >> 4, cc = lane & 15u;
  const unsigned b0 = RQO + c.wid * 8192u + rr * 256u + 16u * (cc ^ rr), b1 = b0 ^ 64u;
  u32x4 w[8];
#pragma unroll
  for (int i = I0; i < I1; ++i) w[i] = *(lds_u4p)(((i & 1) ? b1 : b0) + 1024u * i);
  const unsigned go = hoff + c.wid * 8192u + lane * 16u;
#pragma unroll
  for (int i = I0; i < I1; ++i) __builtin_amdgcn_raw_buffer_store_b128(w[i], c.so, go + 1024u * i, 0, STAUX);
}
template <int IMM> DI void dma1(unsigned m0v, unsigned v, __amdgpu_buffer_rsrc_t srd, unsigned soff) {
  asm volatile("s_mov_b32 m0, %0\n\ts_nop 0\n\tbuffer_load_dwordx4 %1, %2, %3 offen offset:%4" LDPOL " lds" :: "s"(m0v), "v"(v), "s"(srd), "s"(soff), "i"(IMM) : "memory");
}
template <int SEL, int IMM> DI void dma1_if(unsigned sel, unsigned m0v, unsigned v, __amdgpu_buffer_rsrc_t srd, unsigned soff) {
  asm volatile("s_cmp_eq_u32 %0, %5\n\ts_cbranch_scc0 1f\n\ts_mov_b32 m0, %1\n\ts_nop 0\n\t"
               "buffer_load_dwordx4 %2, %3, %4 offen offset:%6" LDPOL " lds\n1:"
               :: "s"(sel), "s"(m0v), "v"(v), "s"(srd), "s"(soff), "i"(SEL), "i"(IMM) : "memory", "scc");
}
template <int E, int WV> DI void run_event(const Ctx& c) {
  constexpr int X = EV[E].idx, I = EV[E].half;
  if constexpr (EV[E].kind == 0) {
    const unsigned m0v = slot_of(X) + c.wid * 2048u, so = ((X & 8) ? c.hB : c.hA) + 16384u * (X & 3) + c.wid * 2048u;
    if constexpr (WV < 0) dma1<1024 * I>(m0v, c.vkv[I], (X & 4) ? c.sv : c.sk, so); else dma1_if<WV, 1024 * I>(c.wid, m0v, c.vkv[I], (X & 4) ? c.sv : c.sk, so);
  } else if constexpr (EV[E].kind == 1) {
    const unsigned m0v = RQO + c.wid * 8192u + 2048u * X, so = c.hB + c.wid * 8192u + 2048u * X;
    if constexpr (WV < 0) dma1<1024 * I>(m0v, c.vs[(2 * X + I) & 3], c.sq, so); else dma1_if<WV, 1024 * I>(c.wid, m0v, c.vs[(2 * X + I) & 3], c.sq, so);
  } else store_o<X, X + 1>(c, c.hA);
}
template <int POS, int E, int... Ws> DI void window_step(const Ctx& c, std::integer_sequence<int, Ws...>) {
  constexpr int t = POS - EV[E].pos, k = win_units(E), r = win_rank(E), L = EV[E].len;
  ([&]() { if constexpr (((8 * r + Ws) * L) / (8 * k) == t) run_event<E, Ws>(c); }(), ...);
}
template <int POS, int E> DI void event_step(const Ctx& c) {
  if constexpr (EV[E].len == 0 || !FINE) { if constexpr (EV[E].len == 0 ? EV[E].pos == POS : POS == EV[E].pos + (win_rank(E) * EV[E].len) / win_units(E)) run_event<E, -1>(c); }
  else if constexpr (POS >= EV[E].pos && POS < EV[E].pos + EV[E].len) window_step<POS, E>(c, std::make_integer_sequence<int, 8>{});
}
template <int POS, int... Es> DI void events_at(const Ctx& c, std::integer_sequence<int, Es...>) { (event_step<POS, Es>(c), ...); }

struct Hd {
  f32x16 S[8], o[4];
  bf16x8 kf[64];
  s16x4 vlo[64], vhi[64];
  u32x4 P[16];
  unsigned ka[8], va[8];
  float mx[2], l[2], mc;
  float x[64][2], e[64][2];
  f32x4 lacc;
  u32x4 ones;
};
struct Prev { f32x16 o[4]; float il; unsigned ow; };

template <int H, int G> DI void k_read(Hd& s) {
  constexpr int I = 8 * H + (G >> 4);
  s.kf[G] = *(lds_b8p)(s.ka[G & 7] + 16384u * ((I % 6) % 3) + 8192u * ((G >> 3) & 1));
}
template <int H, int GP> DI void v_read(Hd& s) {
  constexpr int I = 8 * H + 4 + (GP >> 4), ks = (GP >> 2) & 3, db = GP & 3;
  s.vlo[GP] = __builtin_amdgcn_ds_read_tr16_b64_v4i16((lds_s4p)(s.va[2 * db] + 16384u * ((I % 6) % 3) + 4096u * ks));
  s.vhi[GP] = __builtin_amdgcn_ds_read_tr16_b64_v4i16((lds_s4p)(s.va[2 * db + 1] + 16384u * ((I % 6) % 3) + 4096u * ks + 2048u));
}
template <int IPREV, int INEXT> DI void rebase(unsigned (&a)[8]) {
  if constexpr ((IPREV % 6) / 3 != (INEXT % 6) / 3) {
#pragma unroll
    for (int i = 0; i < 8; ++i) a[i] = opaque((INEXT % 6) >= 3 ? a[i] + 49152u : a[i] - 49152u);
  }
}
DI void va_init(const Ctx& c, Hd& s, unsigned base) {
  const unsigned lane = opaque(c.lane), h = lane >> 5, blk = (lane >> 4) & 1u, q4 = (lane & 15u) >> 2, p = lane & 3u;
  const unsigned vb = 256u * (4u * h + q4) + 16u * (4u * q4 + ((2u * blk + (p >> 1)) ^ h)) + 8u * (p & 1u);
#pragma unroll
  for (int i = 0; i < 8; ++i) s.va[i] = opaque((vb ^ (64u * (i >> 1)) ^ (32u * (i & 1))) + base);
}
constexpr int op_n(int k) { const int j = k / 7, p = k % 7; return (p == 0 || p == 2) ? j : ((p == 1 || p == 3) ? j - 1 : j - 2); }
constexpr bool op_on(int k) { const int n = op_n(k), p = k % 7; return n >= 0 && n < 64 && !(MSUM && (p == 4 || p == 5)); }
constexpr int op_cost(int k) { const int p = k % 7; return !op_on(k) ? 0 : ((p == 1 || p == 3) ? 2 : ((MSUM && p == 6 && (op_n(k) & 3) == 3) ? 3 : 1)); }
constexpr int NOPS = 66 * 7;
struct OpTab { short first[130]; };
constexpr OpTab make_optab() {
  OpTab t{};
  int tot = 0;
  for (int i = 0; i < NOPS; ++i) tot += op_cost(i);
  const int c1 = tot * NQK / 64;
  int c = 0, g = 0;
  for (int k = 0; k < NOPS; ++k) {
    const int gk = c < c1 ? QK0 + c * (64 - QK0) / c1 : 64 + (c - c1) * (PVEND + 1) / (tot - c1);
    while (g <= gk) t.first[g++] = (short)k;
    c += op_cost(k);
  }
  while (g < 130) t.first[g++] = (short)NOPS;
  return t;
}
constexpr OpTab OPT = make_optab();
template <int K> DI void micro_op(Hd& s) {
  if constexpr (op_on(K)) {
    constexpr int n = op_n(K), p = K % 7, ks = n >> 2, kb = ks >> 1, r = 8 * (ks & 1) + 2 * (n & 3);
    if constexpr (p == 0 || p == 2) { s.x[n][p >> 1] = __builtin_fmaf(s.S[kb][r + (p >> 1)], CEXP, s.mc); PIN(s.x[n][p >> 1]); }
    else if constexpr (p == 1 || p == 3) { s.e[n][p >> 1] = __builtin_amdgcn_exp2f(s.x[n][p >> 1]); PIN(s.e[n][p >> 1]); }
    else if constexpr (p == 4 || p == 5) { if constexpr (n == 0) s.l[p - 4] = s.e[n][p - 4]; else s.l[p - 4] += s.e[n][p - 4]; PIN(s.l[p - 4]); }
    else {
      unsigned w = cvtpk(s.e[n][0], s.e[n][1]); PIN(w); s.P[ks][n & 3] = w;
      if constexpr (MSUM && (n & 3) == 3) {
        if constexpr (ks == 0) s.lacc = __builtin_amdgcn_mfma_f32_16x16x32_bf16(__builtin_bit_cast(bf16x8, s.ones), __builtin_bit_cast(bf16x8, s.P[ks]), f32x4{}, 0, 0, 0);
        else                   s.lacc = __builtin_amdgcn_mfma_f32_16x16x32_bf16(__builtin_bit_cast(bf16x8, s.ones), __builtin_bit_cast(bf16x8, s.P[ks]), s.lacc, 0, 0, 0);
        PIN(s.lacc);
      }
    }
  }
}
template <int K0, int... Ks> DI void micro_ops(Hd& s, std::integer_sequence<int, Ks...>) { (micro_op<K0 + Ks>(s), ...); }
template <int I, bool SCALAR> DI void pack_item(const f32x16 (&o)[4], float il, unsigned ow) {
  constexpr int db = I >> 2, g = I & 3;
  float a = o[db][4 * g] * il, b = o[db][4 * g + 1] * il, c = o[db][4 * g + 2] * il, d = o[db][4 * g + 3] * il;
  if constexpr (SCALAR) { PIN(a); PIN(b); PIN(c); PIN(d); }
  const u32x2 w = {cvtpk(a, b), cvtpk(c, d)};
  *(lds_u2p)(ow ^ (16u * I)) = w;
}
DI unsigned stage_base(const Ctx& c) { const unsigned lane = opaque(c.lane), l32 = lane & 31u, h = lane >> 5; return RQO + c.wid * 8192u + l32 * 256u + 16u * (l32 & 7u) + 8u * h; }

template <int H, int G> DI void gap(const Ctx& c, Hd& s, const bf16x8 (&q)[8], const Prev& pv, Stamps& st) {
  constexpr int R = 8 * H;
  if constexpr (G % 16 == 0) ST(1 + 10 * H + G / 16);
  if constexpr ((G + BOFF) % 16 == 0 && G + BOFF >= 16 && G + BOFF <= 112) wait_bar<younger(0, R + (G + BOFF) / 16, 1000 * H + G + 1)>();
  if constexpr (PRIO == 1 && ((G + BOFF) % 16) % PSTEP == 0) __builtin_amdgcn_s_setprio(3 - ((((G + BOFF) % 16) / PSTEP) & 3));
  events_at<1000 * H + G + 1>(c, std::make_integer_sequence<int, NEV>{});
  if constexpr (G < 64) {
    if constexpr (G + PFK < 64) {
      if constexpr (((G + PFK) & 15) == 0) rebase<R + ((G + PFK) >> 4) - 1, R + ((G + PFK) >> 4)>(s.ka);
      k_read<H, G + PFK>(s);
    }
    if constexpr (G == 60) va_init(c, s, RING + (((R + 4) % 6) >= 3 ? 49152u : 0u));
    if constexpr (G + PFV >= 64) v_read<H, G + PFV - 64>(s);
    constexpr int kb = G >> 3, d0 = G & 7;
    if constexpr (d0 == 0) s.S[kb] = __builtin_amdgcn_mfma_f32_32x32x16_bf16(s.kf[G], q[d0], f32x16{}, 0, 0, 0);
    else                   s.S[kb] = __builtin_amdgcn_mfma_f32_32x32x16_bf16(s.kf[G], q[d0], s.S[kb], 0, 0, 0);
    PIN(s.S[kb]);
    if constexpr (G == 10) { s.mx[0] = fmax3(s.S[0][0], s.S[0][1], s.S[0][2]); s.mx[1] = fmax3(s.S[0][3], s.S[0][4], s.S[0][5]); PIN(s.mx[0]); PIN(s.mx[1]); }
    if constexpr (G == 11) { s.mx[0] = fmax3(s.mx[0], s.S[0][6], s.S[0][7]); s.mx[1] = fmax3(s.mx[1], s.S[0][8], s.S[0][9]); PIN(s.mx[0]); PIN(s.mx[1]); }
    if constexpr (G == 12) { s.mx[0] = fmax3(s.mx[0], s.S[0][10], s.S[0][11]); s.mx[1] = fmax3(s.mx[1], s.S[0][12], s.S[0][13]); PIN(s.mx[0]); PIN(s.mx[1]); }
    if constexpr (G == 13) { s.mx[0] = fmax3(s.mx[0], s.S[0][14], s.S[0][15]); s.mx[0] = fmax2(s.mx[0], s.mx[1]); PIN(s.mx[0]); }
    if constexpr (G == 14) { s.mc = -xhalf_max(s.mx[0]) * CEXP; PIN(s.mc); }
    if constexpr (H == 1 && G < 16) pack_item<G, true>(pv.o, pv.il, pv.ow);
  } else {
    constexpr int GP = G - 64, ks = GP >> 2, db = GP & 3;
    if constexpr (GP + PFV < 64) {
      if constexpr (((GP + PFV) & 15) == 0) rebase<R + 4 + ((GP + PFV) >> 4) - 1, R + 4 + ((GP + PFV) >> 4)>(s.va);
      v_read<H, GP + PFV>(s);
    }
    const s16x8 vf = __builtin_shufflevector(s.vlo[GP], s.vhi[GP], 0, 1, 2, 3, 4, 5, 6, 7);
    if constexpr (ks == 0) s.o[db] = __builtin_amdgcn_mfma_f32_32x32x16_bf16(__builtin_bit_cast(bf16x8, vf), __builtin_bit_cast(bf16x8, s.P[0]), f32x16{}, 0, 0, 0);
    else                   s.o[db] = __builtin_amdgcn_mfma_f32_32x32x16_bf16(__builtin_bit_cast(bf16x8, vf), __builtin_bit_cast(bf16x8, s.P[ks]), s.o[db], 0, 0, 0);
    PIN(s.o[db]);
  }
  micro_ops<OPT.first[G]>(s, std::make_integer_sequence<int, OPT.first[G + 1] - OPT.first[G]>{});
  GAP();
}
template <int H, int... Gs> DI void gaps(const Ctx& c, Hd& s, const bf16x8 (&q)[8], const Prev& pv, Stamps& st, std::integer_sequence<int, Gs...>) { (gap<H, Gs>(c, s, q, pv, st), ...); }

__device__ __forceinline__ void fallback(const Ctx& c, unsigned hoff, f32x16 (&o)[4], float& l) {
  const unsigned lane = c.lane, l32 = lane & 31u, h = lane >> 5;
  const char* qp = c.pq + hoff + (c.wid * 32u + l32) * 256u + h * 16u;
  const char* kp = c.pk + hoff + l32 * 256u + h * 16u;
  const short* vp = (const short*)(c.pv + hoff) + l32;
  bf16x8 qf[8];
#pragma unroll
  for (int d = 0; d < 8; ++d) qf[d] = *(const bf16x8*)(qp + 32 * d);
  float m = -__builtin_inff();
#pragma nounroll
  for (int kb = 0; kb < 8; ++kb) {
    f32x16 a = {};
#pragma unroll
    for (int d = 0; d < 8; ++d) a = __builtin_amdgcn_mfma_f32_32x32x16_bf16(*(const bf16x8*)(kp + kb * 8192 + 32 * d), qf[d], a, 0, 0, 0);
#pragma unroll
    for (int r = 0; r < 16; ++r) m = fmax2(m, a[r]);
  }
  m = xhalf_max(m);
  float ls = 0.f;
#pragma unroll
  for (int db = 0; db < 4; ++db) o[db] = f32x16{};
#pragma nounroll
  for (int kb = 0; kb < 8; ++kb) {
    f32x16 a = {};
#pragma unroll
    for (int d = 0; d < 8; ++d) a = __builtin_amdgcn_mfma_f32_32x32x16_bf16(*(const bf16x8*)(kp + kb * 8192 + 32 * d), qf[d], a, 0, 0, 0);
#pragma unroll
    for (int sx = 0; sx < 2; ++sx) {
      float e[8];
#pragma unroll
      for (int j = 0; j < 8; ++j) { e[j] = __builtin_amdgcn_exp2f((a[8 * sx + j] - m) * CEXP); ls += e[j]; }
      const u32x4 p = {cvtpk(e[0], e[1]), cvtpk(e[2], e[3]), cvtpk(e[4], e[5]), cvtpk(e[6], e[7])};
#pragma unroll
      for (int db = 0; db < 4; ++db) {
        s16x8 vf;
#pragma unroll
        for (int j = 0; j < 8; ++j) vf[j] = vp[(kb * 32 + 16 * sx + 8 * (j >> 2) + 4 * h + (j & 3)) * 128 + 32 * db];
        o[db] = __builtin_amdgcn_mfma_f32_32x32x16_bf16(__builtin_bit_cast(bf16x8, vf), __builtin_bit_cast(bf16x8, p), o[db], 0, 0, 0);
      }
    }
  }
  l = xhalf_sum(ls);
}

template <int H>
DI void head(const Ctx& c, bf16x8 (&q)[8], Prev& pv, Stamps& st) {
  constexpr int R = 8 * H;
  Hd s;
  { const unsigned ra = row_base(opaque(c.lane));
#pragma unroll
    for (int d0 = 0; d0 < 8; ++d0) s.ka[d0] = opaque((ra ^ (32u * d0)) + RING + ((R % 6) >= 3 ? 49152u : 0u)); }
  if constexpr (MSUM) { const unsigned lane = opaque(c.lane); const unsigned one = (((lane >> 4) ^ (lane >> 2)) & 1u) ? 0u : 0x3F803F80u; s.ones = u32x4{one, one, one, one}; }
  events_at<1000 * H>(c, std::make_integer_sequence<int, NEV>{});
  GAP();
  wait_bar<younger(0, R, 1000 * H + 1)>();
  GAP();
  k_read<H, 0>(s); k_read<H, 1>(s); k_read<H, 2>(s); k_read<H, 3>(s);
  GAP();
  gaps<H>(c, s, q, pv, st, std::make_integer_sequence<int, 128>{});
  ST(1 + 10 * H + 8);
  float l = MSUM ? s.lacc[0] : xhalf_sum(s.l[0] + s.l[1]);
  constexpr float LIM_HI = (float)(1ull << LIMLOG), LIM_LO = 1.0f / LIM_HI;
  if (__builtin_expect(__any(!(l <= LIM_HI && l >= LIM_LO)), 0)) fallback(c, H ? c.hB : c.hA, s.o, l);
  const float il = __builtin_amdgcn_rcpf(l);
  if constexpr (H == 0) {
#pragma unroll
    for (int db = 0; db < 4; ++db) pv.o[db] = s.o[db];
    pv.il = il; pv.ow = stage_base(c);
    GAP();
    events_at<999>(c, std::make_integer_sequence<int, NEV>{});
    GAP();
    wait_vm<younger(1, 3, 1000)>();
    GAP();
    read_q(c, q);
  } else {
    const unsigned ow = stage_base(c);
    pack_item<0, false>(s.o, il, ow); pack_item<1, false>(s.o, il, ow); pack_item<2, false>(s.o, il, ow); pack_item<3, false>(s.o, il, ow);
    pack_item<4, false>(s.o, il, ow); pack_item<5, false>(s.o, il, ow); pack_item<6, false>(s.o, il, ow); pack_item<7, false>(s.o, il, ow);
    pack_item<8, false>(s.o, il, ow); pack_item<9, false>(s.o, il, ow); pack_item<10, false>(s.o, il, ow); pack_item<11, false>(s.o, il, ow);
    pack_item<12, false>(s.o, il, ow); pack_item<13, false>(s.o, il, ow); pack_item<14, false>(s.o, il, ow); pack_item<15, false>(s.o, il, ow);
    store_o<0, 8>(c, c.hB);
  }
  GAP();
}

__global__ __launch_bounds__(512, 2) void attn_fwd(const void* __restrict__ Q, const void* __restrict__ K, const void* __restrict__ V, void* __restrict__ O, unsigned* __restrict__ dbg) {
  Stamps st;
  unsigned long long rt0; asm volatile("s_memrealtime %0\n\ts_waitcnt lgkmcnt(0)" : "=s"(rt0) :: "memory");
  for (int i = 0; i < NST; ++i) st.t[i] = 0;
  ST(0);
  Ctx c;
  const unsigned tid = threadIdx.x, lane = tid & 63u;
  c.lane = lane;
  c.wid = __builtin_amdgcn_readfirstlane(tid >> 6);
  const int nbytes = NHEADS * (int)HEADB;
  c.sq = __builtin_amdgcn_make_buffer_rsrc((void*)Q, (short)0, nbytes, 0x00020000);
  c.sk = __builtin_amdgcn_make_buffer_rsrc((void*)K, (short)0, nbytes, 0x00020000);
  c.sv = __builtin_amdgcn_make_buffer_rsrc((void*)V, (short)0, nbytes, 0x00020000);
  c.so = __builtin_amdgcn_make_buffer_rsrc((void*)O, (short)0, nbytes, 0x00020000);
  c.pq = (const char*)Q; c.pk = (const char*)K; c.pv = (const char*)V;
  {
    const unsigned r = lane >> 4, p = lane & 15u;
    c.v0 = r * 256u + 16u * (p ^ (r << 2));
#pragma unroll
    for (int j = 0; j < 4; ++j) c.vs[j] = opaque(c.v0 ^ (16u * j));
    const unsigned w2 = (32u * c.wid) & 48u;
    c.vkv[0] = opaque(c.v0 ^ w2); c.vkv[1] = opaque(c.v0 ^ (w2 + 16u));
  }
  { const unsigned b = blockIdx.x;
    const unsigned h0 = HMAP == 0 ? b : (HMAP == 1 ? 2u * b : (b & 7u) * 64u + (b >> 3) * 2u);
    c.hA = h0 * HEADB; c.hB = (h0 + (HMAP == 0 ? (unsigned)NWG : 1u)) * HEADB; }
  asm volatile("s_nop 4");
  dma_q<0>(c, c.hA); dma_q<1>(c, c.hA); dma_q<2>(c, c.hA); dma_q<3>(c, c.hA);
  dma_ring<0>(c); dma_ring<1>(c);
  bf16x8 q[8];
  GAP();
  wait_vm<4>();
  GAP();
  read_q(c, q);
  Prev pv;
  head<0>(c, q, pv, st);
  head<1>(c, q, pv, st);
  ST(21);
  asm volatile("s_waitcnt vmcnt(0)" ::: "memory");
  ST(22);
  unsigned long long rt1; asm volatile("s_memrealtime %0\n\ts_waitcnt lgkmcnt(0)" : "=s"(rt1) :: "memory");
  st.t[38] = (unsigned)rt0; st.t[39] = (unsigned)rt1;
  { const unsigned target = 6000u + (SIDE == 0 ? 0u : st.t[SIDE] - (unsigned)rt0);
    for (;;) { unsigned long long now; asm volatile("s_memrealtime %0\n\ts_waitcnt lgkmcnt(0)" : "=s"(now) :: "memory"); if ((unsigned)now - (unsigned)rt0 >= target) break; __builtin_amdgcn_s_sleep(2); } }
}

extern "C" void kernel_launch(void* const* d_in, const int* in_sizes, int n_in, void* d_out, int out_size, void* d_ws, size_t ws_size, hipStream_t stream) {
  (void)in_sizes; (void)n_in; (void)out_size; (void)ws_size;
  (void)hipFuncSetAttribute((const void*)attn_fwd, hipFuncAttributeMaxDynamicSharedMemorySize, (int)LDS_BYTES);
  hipLaunchKernelGGL(attn_fwd, dim3(NWG), dim3(512), LDS_BYTES, stream, d_in[0], d_in[1], d_in[2], d_out, (unsigned*)d_ws);
}
```

```cpp
#include <hip/hip_runtime.h>
#include <hip/hip_bf16.h>
#include <utility>

typedef float    f32x16 __attribute__((ext_vector_type(16)));
typedef float    f32x2  __attribute__((ext_vector_type(2)));
typedef __bf16   bf16x8 __attribute__((ext_vector_type(8)));
typedef __bf16   bf16x2 __attribute__((ext_vector_type(2)));
typedef short    s16x4  __attribute__((ext_vector_type(4)));
typedef short    s16x8  __attribute__((ext_vector_type(8)));
typedef unsigned u32x4  __attribute__((ext_vector_type(4)));
typedef unsigned u32x2  __attribute__((ext_vector_type(2)));
typedef __attribute__((address_space(3))) const bf16x8* lds_b8p;
typedef __attribute__((address_space(3))) s16x4* lds_s4p;
typedef __attribute__((address_space(3))) u32x2* lds_u2p;
typedef __attribute__((address_space(3))) const u32x4* lds_u4p;

#define DI __device__ __forceinline__
#define GAP() __builtin_amdgcn_sched_barrier(0)
#define PIN(x) asm volatile("" : "+v"(x))

constexpr unsigned HEADB = 65536u;
constexpr unsigned RQO = 0u, RING = 65536u, LDS_BYTES = 163840u;
constexpr int NHEADS = 512, NWG = 256;
constexpr float CEXP = 0.08838834764831845f * 1.4426950408889634f;
constexpr float LIM_HI = 1073741824.0f, LIM_LO = 1.0f / LIM_HI;
constexpr int STORE_AUX = 18;
constexpr int BOFF = 8;
constexpr int PFK = 4, PFV = 3;
constexpr int QK0 = 15;
constexpr int NQK0 = 32, PVEND0 = 58;
constexpr int NQK1 = 44, PVEND1 = 27;
constexpr int RAMP = 12;

DI unsigned cvtpk(float lo, float hi) { f32x2 v = {lo, hi}; bf16x2 b = __builtin_convertvector(v, bf16x2); return __builtin_bit_cast(unsigned, b); }
DI float fmax2(float a, float b) { return __builtin_elementwise_maximum(a, b); }
DI float fmax3(float a, float b, float c) { return __builtin_elementwise_maximum(__builtin_elementwise_maximum(a, b), c); }
DI unsigned opaque(unsigned x) { asm volatile("" : "+v"(x)); return x; }
DI float xhalf_max(float m) { auto rr = __builtin_amdgcn_permlane32_swap(__float_as_uint(m), __float_as_uint(m), false, false); return fmax2(__uint_as_float(rr[0]), __uint_as_float(rr[1])); }
DI float xhalf_sum(float m) { auto rr = __builtin_amdgcn_permlane32_swap(__float_as_uint(m), __float_as_uint(m), false, false); return __uint_as_float(rr[0]) + __uint_as_float(rr[1]); }

template <int IMM> DI void dma1(unsigned m0v, unsigned v, __amdgpu_buffer_rsrc_t srd, unsigned soff) {
  asm volatile("s_mov_b32 m0, %0\n\ts_nop 0\n\tbuffer_load_dwordx4 %1, %2, %3 offen offset:%4 nt lds" :: "s"(m0v), "v"(v), "s"(srd), "s"(soff), "i"(IMM) : "memory");
}
template <int SEL, int IMM> DI void dma1_if(unsigned wid, unsigned m0v, unsigned v, __amdgpu_buffer_rsrc_t srd, unsigned soff) {
  asm volatile("s_cmp_eq_u32 %0, %5\n\ts_cbranch_scc0 1f\n\ts_mov_b32 m0, %1\n\ts_nop 0\n\t"
               "buffer_load_dwordx4 %2, %3, %4 offen offset:%6 nt lds\n1:"
               :: "s"(wid), "s"(m0v), "v"(v), "s"(srd), "s"(soff), "i"(SEL), "i"(IMM) : "memory", "scc");
}
template <int N> DI void wait_bar() { asm volatile("s_waitcnt vmcnt(%0)\n\ts_barrier" :: "i"(N) : "memory"); }
template <int N> DI void wait_vm() { asm volatile("s_waitcnt vmcnt(%0)" :: "i"(N) : "memory"); }

struct Ctx {
  __amdgpu_buffer_rsrc_t sq, sk, sv, so;
  const char *pq, *pk, *pv;
  unsigned wid, lane;
  unsigned vs[4], vkv[2];
  unsigned hA, hB;
};
constexpr unsigned slot_of(int i) { return RING + 16384u * (unsigned)(i % 6); }

struct Ev { int pos, len, kind, idx, half; };
#define U2(p0, p1, k, i) {p0, 0, k, i, 0}, {p1, 0, k, i, 1}
#define W(p, l, k, i, h) {p, l, k, i, h}
#define S1(p, i) {p, 0, 2, i, 0}
constexpr Ev EV[] = {
  U2(0, 0, 0, 2), W(1, 8, 0, 3, 0), W(9, 16, 0, 3, 1), W(9, 16, 0, 4, 0), W(25, 16, 0, 4, 1), W(25, 16, 0, 5, 0), W(25, 16, 0, 5, 1),
  W(41, 16, 0, 6, 0), W(41, 16, 0, 6, 1), W(41, 16, 0, 7, 0), W(57, 16, 0, 7, 1), W(57, 16, 1, 0, 0), W(73, 16, 1, 0, 1), W(73, 16, 1, 1, 0), W(73, 16, 1, 1, 1),
  W(89, 16, 1, 2, 0), W(89, 16, 1, 2, 1), W(105, 16, 1, 3, 0), W(105, 16, 1, 3, 1), W(105, 16, 0, 8, 0), W(121, 8, 0, 8, 1), U2(999, 999, 0, 9),
  W(1001, 8, 0, 10, 0), W(1001, 8, 0, 10, 1), W(1009, 16, 0, 11, 0), W(1009, 16, 0, 11, 1), W(1009, 16, 0, 12, 0),
  W(1025, 16, 0, 12, 1), W(1025, 16, 0, 13, 0), W(1025, 16, 0, 13, 1), W(1025, 16, 0, 14, 0), W(1041, 16, 0, 14, 1), W(1041, 16, 0, 15, 0), W(1041, 16, 0, 15, 1),
  S1(1066, 0), S1(1070, 1), S1(1074, 2), S1(1078, 3), S1(1082, 4), S1(1086, 5), S1(1090, 6), S1(1094, 7)};
constexpr int NEV = sizeof(EV) / sizeof(EV[0]);
constexpr int younger(int kind, int idx, int pos) {
  int e0 = -1;
  for (int e = 0; e < NEV; ++e) if (EV[e].kind == kind && EV[e].idx == idx) e0 = e;
  int n = 0;
  if (e0 < 0) n = (kind == 0 && idx == 0) ? 2 : 0;
  for (int e = e0 + 1; e < NEV; ++e) if (EV[e].pos < pos) ++n;
  return n;
}
constexpr bool ev_ok() {
  for (int e = 1; e < NEV; ++e) if (EV[e].pos < EV[e - 1].pos) return false;
  for (int e = 0; e < NEV; ++e) if (EV[e].len > 0) { const int g0 = (EV[e].pos - 1) % 1000, g1 = g0 + EV[e].len - 1; if ((g0 + BOFF) / 16 != (g1 + BOFF) / 16) return false; }
  return true;
}
static_assert(ev_ok(), "events in program order, windows inside one barrier interval");
constexpr int win_units(int e) { int n = 0; for (int x = 0; x < NEV; ++x) if (EV[x].pos == EV[e].pos && EV[x].len == EV[e].len) ++n; return n; }
constexpr int win_rank(int e) { int n = 0; for (int x = 0; x < e; ++x) if (EV[x].pos == EV[e].pos && EV[x].len == EV[e].len) ++n; return n; }

DI unsigned row_base(unsigned lane) { const unsigned l32 = lane & 31u, h = lane >> 5, f = ((l32 & 3u) << 2) | ((l32 >> 2) & 3u); return l32 * 256u + 16u * (f ^ h); }
DI void read_q(const Ctx& c, bf16x8 (&q)[8]) {
  const unsigned b = row_base(opaque(c.lane)) + RQO + c.wid * 8192u;
#pragma unroll
  for (int d0 = 0; d0 < 8; ++d0) q[d0] = *(lds_b8p)(b ^ (32u * d0));
}
template <int I> DI void store_rows(const Ctx& c, unsigned hoff) {
  const unsigned lane = opaque(c.lane), rr = lane >> 4, cc = lane & 15u;
  const u32x4 w = *(lds_u4p)(((RQO + c.wid * 8192u + rr * 256u + 16u * (cc ^ rr)) ^ (64u * (I & 1))) + 1024u * I);
  __builtin_amdgcn_raw_buffer_store_b128(w, c.so, hoff + c.wid * 8192u + lane * 16u + 1024u * I, 0, STORE_AUX);
}
template <int HF, int I> DI void store_half(const Ctx& c, unsigned hoff) {
  const unsigned lane = opaque(c.lane), rr = lane >> 3, cc = lane & 7u;
  const u32x4 w = *(lds_u4p)(RQO + c.wid * 8192u + rr * 256u + 16u * (cc ^ rr) + 128u * HF + 2048u * I);
  __builtin_amdgcn_raw_buffer_store_b128(w, c.so, hoff + c.wid * 8192u + rr * 256u + cc * 16u + 128u * HF + 2048u * I, 0, STORE_AUX);
}
template <int KIND, int X, int I, int WV> DI void load_piece(const Ctx& c) {
  if constexpr (KIND == 0) {
    const unsigned m0v = slot_of(X) + c.wid * 2048u, so = ((X & 8) ? c.hB : c.hA) + 16384u * (X & 3) + c.wid * 2048u;
    if constexpr (WV < 0) dma1<1024 * I>(m0v, c.vkv[I], (X & 4) ? c.sv : c.sk, so); else dma1_if<WV, 1024 * I>(c.wid, m0v, c.vkv[I], (X & 4) ? c.sv : c.sk, so);
  } else {
    const unsigned m0v = RQO + c.wid * 8192u + 2048u * X, so = (KIND == 1 ? c.hB : c.hA) + c.wid * 8192u + 2048u * X;
    if constexpr (WV < 0) dma1<1024 * I>(m0v, c.vs[(2 * X + I) & 3], c.sq, so); else dma1_if<WV, 1024 * I>(c.wid, m0v, c.vs[(2 * X + I) & 3], c.sq, so);
  }
}
template <int E, int WV> DI void run_event(const Ctx& c) {
  if constexpr (EV[E].kind == 2) store_rows<EV[E].idx>(c, c.hA); else load_piece<EV[E].kind, EV[E].idx, EV[E].half, WV>(c);
}
template <int POS, int E, int... Ws> DI void window_step(const Ctx& c, std::integer_sequence<int, Ws...>) {
  constexpr int t = POS - EV[E].pos, k = win_units(E), r = win_rank(E), L = EV[E].len;
  ([&]() { if constexpr (((8 * r + Ws) * L) / (8 * k) == t) run_event<E, Ws>(c); }(), ...);
}
template <int POS, int E> DI void event_step(const Ctx& c) {
  if constexpr (EV[E].len == 0) { if constexpr (EV[E].pos == POS) run_event<E, -1>(c); }
  else if constexpr (POS >= EV[E].pos && POS < EV[E].pos + EV[E].len) window_step<POS, E>(c, std::make_integer_sequence<int, 8>{});
}
template <int POS, int... Es> DI void events_at_(const Ctx& c, std::integer_sequence<int, Es...>) { (event_step<POS, Es>(c), ...); }
template <int POS> DI void events_at(const Ctx& c) { events_at_<POS>(c, std::make_integer_sequence<int, NEV>{}); }

struct Hd {
  f32x16 S[8], o[4];
  bf16x8 kf[64];
  s16x4 vlo[64], vhi[64];
  u32x4 P[16];
  unsigned ka[8], va[8];
  float mx[2], l[2], mc;
  float x[64][2], e[64][2];
  float lt, il; unsigned ow;
};
struct Prev { f32x16 o[4]; float il; unsigned ow; };

template <int H, int G> DI void k_read(Hd& s) {
  constexpr int I = 8 * H + (G >> 4);
  s.kf[G] = *(lds_b8p)(s.ka[G & 7] + 16384u * ((I % 6) % 3) + 8192u * ((G >> 3) & 1));
}
template <int H> constexpr int pv_ks(int gp) { return H ? (gp & 31) >> 1 : gp >> 2; }
template <int H> constexpr int pv_db(int gp) { return H ? 2 * (gp >> 5) + (gp & 1) : gp & 3; }
template <int H> constexpr int pv_tile(int gp) { return 8 * H + 4 + (pv_ks<H>(gp) >> 2); }
template <int H, int GP> DI void v_read(Hd& s) {
  constexpr int I = pv_tile<H>(GP), ks = pv_ks<H>(GP) & 3, db = pv_db<H>(GP);
  s.vlo[GP] = __builtin_amdgcn_ds_read_tr16_b64_v4i16((lds_s4p)(s.va[2 * db] + 16384u * ((I % 6) % 3) + 4096u * ks));
  s.vhi[GP] = __builtin_amdgcn_ds_read_tr16_b64_v4i16((lds_s4p)(s.va[2 * db + 1] + 16384u * ((I % 6) % 3) + 4096u * ks + 2048u));
}
template <int IPREV, int INEXT> DI void rebase(unsigned (&a)[8]) {
  if constexpr ((IPREV % 6) / 3 != (INEXT % 6) / 3) {
#pragma unroll
    for (int i = 0; i < 8; ++i) a[i] = opaque((INEXT % 6) >= 3 ? a[i] + 49152u : a[i] - 49152u);
  }
}
DI void va_init(const Ctx& c, Hd& s, unsigned base) {
  const unsigned lane = opaque(c.lane), h = lane >> 5, blk = (lane >> 4) & 1u, q4 = (lane & 15u) >> 2, p = lane & 3u;
  const unsigned vb = 256u * (4u * h + q4) + 16u * (4u * q4 + ((2u * blk + (p >> 1)) ^ h)) + 8u * (p & 1u);
#pragma unroll
  for (int i = 0; i < 8; ++i) s.va[i] = opaque((vb ^ (64u * (i >> 1)) ^ (32u * (i & 1))) + base);
}

constexpr int op_n(int k) { const int j = k / 7, p = k % 7; return (p == 0 || p == 2) ? j : ((p == 1 || p == 3) ? j - 1 : j - 2); }
constexpr bool op_on(int k) { const int n = op_n(k); return n >= 0 && n < 64; }
constexpr int op_cost(int k) { const int p = k % 7; return !op_on(k) ? 0 : ((p == 1 || p == 3) ? 2 : 1); }
constexpr int NOPS = 66 * 7;
struct OpTab { short first[130]; };
constexpr OpTab make_optab(int nqk, int pvend) {
  OpTab t{};
  int tot = 0;
  for (int i = 0; i < NOPS; ++i) tot += op_cost(i);
  const int c1 = tot * nqk / 64;
  int c = 0, g = 0;
  for (int k = 0; k < NOPS; ++k) {
    const int gk = c < c1 ? QK0 + c * (64 - QK0) / c1 : 64 + (c - c1) * (pvend + 1) / (tot - c1);
    while (g <= gk) t.first[g++] = (short)k;
    c += op_cost(k);
  }
  while (g < 130) t.first[g++] = (short)NOPS;
  return t;
}
constexpr OpTab OPT0 = make_optab(NQK0, PVEND0), OPT1 = make_optab(NQK1, PVEND1);
template <int H> constexpr int op_first(int g) { return H ? OPT1.first[g] : OPT0.first[g]; }
template <int K> DI void micro_op(Hd& s) {
  if constexpr (op_on(K)) {
    constexpr int n = op_n(K), p = K % 7, ks = n >> 2, kb = ks >> 1, r = 8 * (ks & 1) + 2 * (n & 3);
    if constexpr (p == 0 || p == 2) { s.x[n][p >> 1] = __builtin_fmaf(s.S[kb][r + (p >> 1)], CEXP, s.mc); PIN(s.x[n][p >> 1]); }
    else if constexpr (p == 1 || p == 3) { s.e[n][p >> 1] = __builtin_amdgcn_exp2f(s.x[n][p >> 1]); PIN(s.e[n][p >> 1]); }
    else if constexpr (p == 4 || p == 5) { if constexpr (n == 0) s.l[p - 4] = s.e[n][p - 4]; else s.l[p - 4] += s.e[n][p - 4]; PIN(s.l[p - 4]); }
    else { unsigned w = cvtpk(s.e[n][0], s.e[n][1]); PIN(w); s.P[ks][n & 3] = w; }
  }
}
template <int K0, int... Ks> DI void micro_ops(Hd& s, std::integer_sequence<int, Ks...>) { (micro_op<K0 + Ks>(s), ...); }

template <int I, bool SCALAR> DI void pack_item(const f32x16 (&o)[4], float il, unsigned ow) {
  constexpr int db = I >> 2, g = I & 3;
  float a = o[db][4 * g] * il, b = o[db][4 * g + 1] * il, c = o[db][4 * g + 2] * il, d = o[db][4 * g + 3] * il;
  if constexpr (SCALAR) { PIN(a); PIN(b); PIN(c); PIN(d); }
  const u32x2 w = {cvtpk(a, b), cvtpk(c, d)};
  *(lds_u2p)(ow ^ (16u * I)) = w;
}
template <int I0, int... Is> DI void pack_items(const f32x16 (&o)[4], float il, unsigned ow, std::integer_sequence<int, Is...>) { (pack_item<I0 + Is, false>(o, il, ow), ...); }
DI unsigned stage_base(const Ctx& c) { const unsigned lane = opaque(c.lane), l32 = lane & 31u, h = lane >> 5; return RQO + c.wid * 8192u + l32 * 256u + 16u * (l32 & 7u) + 8u * h; }

template <int H> constexpr int bar_tile(int g) {
  if ((g + BOFF) % 16 != 0 || g + BOFF < 16 || g + BOFF > 112) return -1;
  const int j = (g + BOFF) / 16;
  if (H == 1) return j <= 3 ? 8 + j : (j == 4 ? 13 : (j == 5 ? 15 : -1));
  return j;
}
template <int H, int G> DI void gap(const Ctx& c, Hd& s, const bf16x8 (&q)[8], const Prev& pv) {
  constexpr int R = 8 * H;
  if constexpr (bar_tile<H>(G) >= 0) wait_bar<younger(0, bar_tile<H>(G), 1000 * H + G + 1)>();
  if constexpr (((G + BOFF) % 16) % 4 == 0) __builtin_amdgcn_s_setprio(3 - ((G + BOFF) % 16) / 4);
  events_at<1000 * H + G + 1>(c);
  if constexpr (G < 64) {
    if constexpr (G + PFK < 64) {
      if constexpr (((G + PFK) & 15) == 0) rebase<R + ((G + PFK) >> 4) - 1, R + ((G + PFK) >> 4)>(s.ka);
      k_read<H, G + PFK>(s);
    }
    if constexpr (G == 60) va_init(c, s, RING + (((R + 4) % 6) >= 3 ? 49152u : 0u));
    if constexpr (G + PFV >= 64) v_read<H, G + PFV - 64>(s);
    constexpr int kb = G >> 3, d0 = G & 7;
    if constexpr (d0 == 0) s.S[kb] = __builtin_amdgcn_mfma_f32_32x32x16_bf16(s.kf[G], q[d0], f32x16{}, 0, 0, 0);
    else                   s.S[kb] = __builtin_amdgcn_mfma_f32_32x32x16_bf16(s.kf[G], q[d0], s.S[kb], 0, 0, 0);
    PIN(s.S[kb]);
    if constexpr (G == 10) { s.mx[0] = fmax3(s.S[0][0], s.S[0][1], s.S[0][2]); s.mx[1] = fmax3(s.S[0][3], s.S[0][4], s.S[0][5]); PIN(s.mx[0]); PIN(s.mx[1]); }
    if constexpr (G == 11) { s.mx[0] = fmax3(s.mx[0], s.S[0][6], s.S[0][7]); s.mx[1] = fmax3(s.mx[1], s.S[0][8], s.S[0][9]); PIN(s.mx[0]); PIN(s.mx[1]); }
    if constexpr (G == 12) { s.mx[0] = fmax3(s.mx[0], s.S[0][10], s.S[0][11]); s.mx[1] = fmax3(s.mx[1], s.S[0][12], s.S[0][13]); PIN(s.mx[0]); PIN(s.mx[1]); }
    if constexpr (G == 13) { s.mx[0] = fmax3(s.mx[0], s.S[0][14], s.S[0][15]); s.mx[0] = fmax2(s.mx[0], s.mx[1]); PIN(s.mx[0]); }
    if constexpr (G == 14) { s.mc = -xhalf_max(s.mx[0]) * CEXP; PIN(s.mc); }
    if constexpr (H == 1 && G < 16) pack_item<G, true>(pv.o, pv.il, pv.ow);
  } else {
    constexpr int GP = G - 64, ks = pv_ks<H>(GP), db = pv_db<H>(GP);
    if constexpr (GP + PFV < 64) {
      rebase<pv_tile<H>(GP + PFV - 1), pv_tile<H>(GP + PFV)>(s.va);
      v_read<H, GP + PFV>(s);
    }
    const s16x8 vf = __builtin_shufflevector(s.vlo[GP], s.vhi[GP], 0, 1, 2, 3, 4, 5, 6, 7);
    if constexpr (ks == 0) s.o[db] = __builtin_amdgcn_mfma_f32_32x32x16_bf16(__builtin_bit_cast(bf16x8, vf), __builtin_bit_cast(bf16x8, s.P[0]), f32x16{}, 0, 0, 0);
    else                   s.o[db] = __builtin_amdgcn_mfma_f32_32x32x16_bf16(__builtin_bit_cast(bf16x8, vf), __builtin_bit_cast(bf16x8, s.P[ks]), s.o[db], 0, 0, 0);
    PIN(s.o[db]);
  }
  if constexpr (H == 1) {
    constexpr int GL = 64 + PVEND1 + 2;
    if constexpr (G == GL) { s.lt = xhalf_sum(s.l[0] + s.l[1]); PIN(s.lt); }
    if constexpr (G == GL + 1) { s.il = __builtin_amdgcn_rcpf(s.lt); PIN(s.il); s.ow = stage_base(c); }
    if constexpr (G >= 99 && G < 115 && ((G - 99) & 1) == 0) pack_item<(G - 99) / 2, true>(s.o, s.il, s.ow);
    if constexpr (G >= 116 && G < 128 && (G - 116) % 3 == 0) store_half<0, (G - 116) / 3>(c, c.hB);
  }
  if constexpr (H == 0 && G < 48) asm volatile("s_nop %0" :: "i"((G < 16 ? RAMP : (G < 32 ? RAMP / 2 : RAMP / 4)) - 1));
  micro_ops<op_first<H>(G)>(s, std::make_integer_sequence<int, op_first<H>(G + 1) - op_first<H>(G)>{});
  GAP();
}
template <int H, int... Gs> DI void gaps(const Ctx& c, Hd& s, const bf16x8 (&q)[8], const Prev& pv, std::integer_sequence<int, Gs...>) { (gap<H, Gs>(c, s, q, pv), ...); }

DI void fallback(const Ctx& c, unsigned hoff, f32x16 (&o)[4], float& l) {
  const unsigned lane = c.lane, l32 = lane & 31u, h = lane >> 5;
  const char* qp = c.pq + hoff + (c.wid * 32u + l32) * 256u + h * 16u;
  const char* kp = c.pk + hoff + l32 * 256u + h * 16u;
  const short* vp = (const short*)(c.pv + hoff) + l32;
  bf16x8 qf[8];
#pragma unroll
  for (int d = 0; d < 8; ++d) qf[d] = *(const bf16x8*)(qp + 32 * d);
  float m = -__builtin_inff();
#pragma nounroll
  for (int kb = 0; kb < 8; ++kb) {
    f32x16 a = {};
#pragma unroll
    for (int d = 0; d < 8; ++d) a = __builtin_amdgcn_mfma_f32_32x32x16_bf16(*(const bf16x8*)(kp + kb * 8192 + 32 * d), qf[d], a, 0, 0, 0);
#pragma unroll
    for (int r = 0; r < 16; ++r) m = fmax2(m, a[r]);
  }
  m = xhalf_max(m);
  float ls = 0.f;
#pragma unroll
  for (int db = 0; db < 4; ++db) o[db] = f32x16{};
#pragma nounroll
  for (int kb = 0; kb < 8; ++kb) {
    f32x16 a = {};
#pragma unroll
    for (int d = 0; d < 8; ++d) a = __builtin_amdgcn_mfma_f32_32x32x16_bf16(*(const bf16x8*)(kp + kb * 8192 + 32 * d), qf[d], a, 0, 0, 0);
#pragma unroll
    for (int sx = 0; sx < 2; ++sx) {
      float e[8];
#pragma unroll
      for (int j = 0; j < 8; ++j) { e[j] = __builtin_amdgcn_exp2f((a[8 * sx + j] - m) * CEXP); ls += e[j]; }
      const u32x4 p = {cvtpk(e[0], e[1]), cvtpk(e[2], e[3]), cvtpk(e[4], e[5]), cvtpk(e[6], e[7])};
#pragma unroll
      for (int db = 0; db < 4; ++db) {
        s16x8 vf;
#pragma unroll
        for (int j = 0; j < 8; ++j) vf[j] = vp[(kb * 32 + 16 * sx + 8 * (j >> 2) + 4 * h + (j & 3)) * 128 + 32 * db];
        o[db] = __builtin_amdgcn_mfma_f32_32x32x16_bf16(__builtin_bit_cast(bf16x8, vf), __builtin_bit_cast(bf16x8, p), o[db], 0, 0, 0);
      }
    }
  }
  l = xhalf_sum(ls);
}
#define UNSAFE_SUM(l) __builtin_expect(__any(!((l) <= LIM_HI && (l) >= LIM_LO)), 0)

template <int H>
DI void head(const Ctx& c, bf16x8 (&q)[8], Prev& pv) {
  constexpr int R = 8 * H;
  Hd s;
  { const unsigned ra = row_base(opaque(c.lane));
#pragma unroll
    for (int d0 = 0; d0 < 8; ++d0) s.ka[d0] = opaque((ra ^ (32u * d0)) + RING + ((R % 6) >= 3 ? 49152u : 0u)); }
  events_at<1000 * H>(c);
  GAP();
  wait_bar<younger(0, R, 1000 * H + 1)>();
  GAP();
  k_read<H, 0>(s); k_read<H, 1>(s); k_read<H, 2>(s); k_read<H, 3>(s);
  GAP();
  gaps<H>(c, s, q, pv, std::make_integer_sequence<int, 128>{});
  if constexpr (H == 0) {
    float l = xhalf_sum(s.l[0] + s.l[1]);
    if (UNSAFE_SUM(l)) fallback(c, c.hA, s.o, l);
#pragma unroll
    for (int db = 0; db < 4; ++db) pv.o[db] = s.o[db];
    pv.il = __builtin_amdgcn_rcpf(l); pv.ow = stage_base(c);
    GAP();
    events_at<999>(c);
    GAP();
    wait_vm<younger(1, 3, 1000)>();
    GAP();
    read_q(c, q);
  } else {
    float l = s.lt;
    if (UNSAFE_SUM(l)) {
      wait_vm<0>();
      fallback(c, c.hB, s.o, l);
      s.il = __builtin_amdgcn_rcpf(l);
      pack_items<0>(s.o, s.il, s.ow, std::make_integer_sequence<int, 8>{});
      store_half<0, 0>(c, c.hB); store_half<0, 1>(c, c.hB); store_half<0, 2>(c, c.hB); store_half<0, 3>(c, c.hB);
    }
    pack_items<8>(s.o, s.il, s.ow, std::make_integer_sequence<int, 8>{});
    store_half<1, 0>(c, c.hB); store_half<1, 1>(c, c.hB); store_half<1, 2>(c, c.hB); store_half<1, 3>(c, c.hB);
  }
  GAP();
}

__global__ __launch_bounds__(512, 2) void attn_fwd(const void* __restrict__ Q, const void* __restrict__ K, const void* __restrict__ V, void* __restrict__ O) {
  const unsigned tid = threadIdx.x, lane = tid & 63u;
  {
    unsigned long long pc; unsigned off, t, m;
    asm volatile("attn_fwd_code_begin:\n\t"
                 "s_getpc_b64 %0\n\t"
                 "v_lshlrev_b32 %1, 7, %4\n\t"
                 "v_lshrrev_b32 %2, 6, %4\n\t"
                 "s_mov_b32 %3, attn_fwd_code_end-attn_fwd_code_begin\n\t"
                 "v_cmp_gt_u32 vcc, %3, %1\n\t"
                 "v_readfirstlane_b32 %3, %2\n\t"
                 "s_nop 3\n\t"
                 "v_cndmask_b32 %1, 0, %1, vcc\n\t"
                 "s_lshl_b32 %3, %3, 8\n\t"
                 "s_add_u32 %3, %3, %5\n\t"
                 "s_mov_b32 m0, %3\n\t"
                 "s_nop 0\n\t"
                 "global_load_lds_dword %1, %0"
                 : "=&s"(pc), "=&v"(off), "=&v"(t), "=&s"(m) : "v"(tid), "i"(slot_of(5)) : "memory", "vcc", "scc");
    GAP();
  }
  Ctx c;
  c.lane = lane;
  c.wid = __builtin_amdgcn_readfirstlane(tid >> 6);
  const int nbytes = NHEADS * (int)HEADB;
  c.sq = __builtin_amdgcn_make_buffer_rsrc((void*)Q, (short)0, nbytes, 0x00020000);
  c.sk = __builtin_amdgcn_make_buffer_rsrc((void*)K, (short)0, nbytes, 0x00020000);
  c.sv = __builtin_amdgcn_make_buffer_rsrc((void*)V, (short)0, nbytes, 0x00020000);
  c.so = __builtin_amdgcn_make_buffer_rsrc((void*)O, (short)0, nbytes, 0x00020000);
  c.pq = (const char*)Q; c.pk = (const char*)K; c.pv = (const char*)V;
  {
    const unsigned r = lane >> 4, p = lane & 15u, v0 = r * 256u + 16u * (p ^ (r << 2));
#pragma unroll
    for (int j = 0; j < 4; ++j) c.vs[j] = opaque(v0 ^ (16u * j));
    const unsigned w2 = (32u * c.wid) & 48u;
    c.vkv[0] = opaque(v0 ^ w2); c.vkv[1] = opaque(v0 ^ (w2 + 16u));
  }
  { const unsigned b = blockIdx.x, h0 = (b & 7u) * 64u + (b >> 3) * 2u;
    c.hA = h0 * HEADB; c.hB = (h0 + 1u) * HEADB; }
  asm volatile("s_nop 4");
  load_piece<4, 0, 0, -1>(c); load_piece<4, 0, 1, -1>(c); load_piece<4, 1, 0, -1>(c); load_piece<4, 1, 1, -1>(c);
  load_piece<4, 2, 0, -1>(c); load_piece<4, 2, 1, -1>(c); load_piece<4, 3, 0, -1>(c); load_piece<4, 3, 1, -1>(c);
  load_piece<0, 0, 0, -1>(c); load_piece<0, 0, 1, -1>(c); load_piece<0, 1, 0, -1>(c); load_piece<0, 1, 1, -1>(c);
  bf16x8 q[8];
  GAP();
  wait_vm<4>();
  GAP();
  read_q(c, q);
  Prev pv;
  head<0>(c, q, pv);
  head<1>(c, q, pv);
  asm volatile("attn_fwd_code_end:");
}

extern "C" void kernel_launch(void* const* d_in, const int* in_sizes, int n_in, void* d_out, int out_size, void* d_ws, size_t ws_size, hipStream_t stream) {
  (void)in_sizes; (void)n_in; (void)out_size; (void)d_ws; (void)ws_size;
  (void)hipFuncSetAttribute((const void*)attn_fwd, hipFuncAttributeMaxDynamicSharedMemorySize, (int)LDS_BYTES);
  hipLaunchKernelGGL(attn_fwd, dim3(NWG), dim3(512), LDS_BYTES, stream, d_in[0], d_in[1], d_in[2], d_out);
}
```

```cpp
#include <hip/hip_runtime.h>
#include <hip/hip_bf16.h>
#include <utility>

typedef float    f32x16 __attribute__((ext_vector_type(16)));
typedef float    f32x2  __attribute__((ext_vector_type(2)));
typedef __bf16   bf16x8 __attribute__((ext_vector_type(8)));
typedef __bf16   bf16x2 __attribute__((ext_vector_type(2)));
typedef short    s16x4  __attribute__((ext_vector_type(4)));
typedef short    s16x8  __attribute__((ext_vector_type(8)));
typedef unsigned u32x4  __attribute__((ext_vector_type(4)));
typedef unsigned u32x2  __attribute__((ext_vector_type(2)));
typedef __attribute__((address_space(3))) const bf16x8* lds_b8p;
typedef __attribute__((address_space(3))) s16x4* lds_s4p;
typedef __attribute__((address_space(3))) u32x2* lds_u2p;
typedef __attribute__((address_space(3))) const u32x4* lds_u4p;

#define DI __device__ __forceinline__
#define GAP() __builtin_amdgcn_sched_barrier(0)
#define PIN(x) asm volatile("" : "+v"(x))

constexpr unsigned HEADB = 65536u;
constexpr unsigned RQO = 0u, RING = 65536u, LDS_BYTES = 163840u;
constexpr int NHEADS = 512, NWG = 256;
constexpr float CEXP = 0.08838834764831845f * 1.4426950408889634f;
constexpr float LIM_HI = 1073741824.0f, LIM_LO = 1.0f / LIM_HI;
constexpr int STORE_AUX = 18;
constexpr int BOFF = 8;
constexpr int PFK = 4, PFV = 3;
constexpr int QK0 = 15;
constexpr int NQK0 = 32, PVEND0 = 58;
constexpr int NQK1 = 44, PVEND1 = 27;
constexpr int RAMP = 12;

DI unsigned cvtpk(float lo, float hi) { f32x2 v = {lo, hi}; bf16x2 b = __builtin_convertvector(v, bf16x2); return __builtin_bit_cast(unsigned, b); }
DI float fmax2(float a, float b) { return __builtin_elementwise_maximum(a, b); }
DI float fmax3(float a, float b, float c) { return __builtin_elementwise_maximum(__builtin_elementwise_maximum(a, b), c); }
DI unsigned opaque(unsigned x) { asm volatile("" : "+v"(x)); return x; }
DI float xhalf_max(float m) { auto rr = __builtin_amdgcn_permlane32_swap(__float_as_uint(m), __float_as_uint(m), false, false); return fmax2(__uint_as_float(rr[0]), __uint_as_float(rr[1])); }
DI float xhalf_sum(float m) { auto rr = __builtin_amdgcn_permlane32_swap(__float_as_uint(m), __float_as_uint(m), false, false); return __uint_as_float(rr[0]) + __uint_as_float(rr[1]); }

template <int IMM> DI void dma1(unsigned m0v, unsigned v, __amdgpu_buffer_rsrc_t srd, unsigned soff) {
  asm volatile("s_mov_b32 m0, %0\n\ts_nop 0\n\tbuffer_load_dwordx4 %1, %2, %3 offen offset:%4 nt lds" :: "s"(m0v), "v"(v), "s"(srd), "s"(soff), "i"(IMM) : "memory");
}
template <int SEL, int IMM> DI void dma1_if(unsigned wid, unsigned m0v, unsigned v, __amdgpu_buffer_rsrc_t srd, unsigned soff) {
  asm volatile("s_cmp_eq_u32 %0, %5\n\ts_cbranch_scc0 1f\n\ts_mov_b32 m0, %1\n\ts_nop 0\n\t"
               "buffer_load_dwordx4 %2, %3, %4 offen offset:%6 nt lds\n1:"
               :: "s"(wid), "s"(m0v), "v"(v), "s"(srd), "s"(soff), "i"(SEL), "i"(IMM) : "memory", "scc");
}
template <int N> DI void wait_bar() { asm volatile("s_waitcnt vmcnt(%0)\n\ts_barrier" :: "i"(N) : "memory"); }
template <int N> DI void wait_vm() { asm volatile("s_waitcnt vmcnt(%0)" :: "i"(N) : "memory"); }

struct Ctx {
  __amdgpu_buffer_rsrc_t sq, sk, sv, so;
  const char *pq, *pk, *pv;
  unsigned wid, lane;
  unsigned vs[4], vkv[2];
  unsigned hA, hB;
};
constexpr unsigned slot_of(int i) { return RING + 16384u * (unsigned)(i % 6); }

struct Ev { int pos, len, kind, idx, half; };
#define U2(p0, p1, k, i) {p0, 0, k, i, 0}, {p1, 0, k, i, 1}
#define W(p, l, k, i, h) {p, l, k, i, h}
#define S1(p, i) {p, 0, 2, i, 0}
constexpr Ev EV[] = {
  U2(0, 0, 0, 2), W(1, 8, 0, 3, 0), W(9, 16, 0, 3, 1), W(9, 16, 0, 4, 0), W(25, 16, 0, 4, 1), W(25, 16, 0, 5, 0), W(25, 16, 0, 5, 1),
  W(41, 16, 0, 6, 0), W(41, 16, 0, 6, 1), W(41, 16, 0, 7, 0), W(57, 16, 0, 7, 1), W(57, 16, 1, 0, 0), W(73, 16, 1, 0, 1), W(73, 16, 1, 1, 0), W(73, 16, 1, 1, 1),
  W(89, 16, 1, 2, 0), W(89, 16, 1, 2, 1), W(105, 16, 1, 3, 0), W(105, 16, 1, 3, 1), W(105, 16, 0, 8, 0), W(121, 8, 0, 8, 1), U2(999, 999, 0, 9),
  W(1001, 8, 0, 10, 0), W(1001, 8, 0, 10, 1), W(1009, 16, 0, 11, 0), W(1009, 16, 0, 11, 1), W(1009, 16, 0, 12, 0),
  W(1025, 16, 0, 12, 1), W(1025, 16, 0, 13, 0), W(1025, 16, 0, 13, 1), W(1025, 16, 0, 14, 0), W(1041, 16, 0, 14, 1), W(1041, 16, 0, 15, 0), W(1041, 16, 0, 15, 1),
  S1(1066, 0), S1(1070, 1), S1(1074, 2), S1(1078, 3), S1(1082, 4), S1(1086, 5), S1(1090, 6), S1(1094, 7)};
constexpr int NEV = sizeof(EV) / sizeof(EV[0]);
constexpr int younger(int kind, int idx, int pos) {
  int e0 = -1;
  for (int e = 0; e < NEV; ++e) if (EV[e].kind == kind && EV[e].idx == idx) e0 = e;
  int n = 0;
  if (e0 < 0) n = (kind == 0 && idx == 0) ? 2 : 0;
  for (int e = e0 + 1; e < NEV; ++e) if (EV[e].pos < pos) ++n;
  return n;
}
constexpr bool ev_ok() {
  for (int e = 1; e < NEV; ++e) if (EV[e].pos < EV[e - 1].pos) return false;
  for (int e = 0; e < NEV; ++e) if (EV[e].len > 0) { const int g0 = (EV[e].pos - 1) % 1000, g1 = g0 + EV[e].len - 1; if ((g0 + BOFF) / 16 != (g1 + BOFF) / 16) return false; }
  return true;
}
static_assert(ev_ok(), "events in program order, windows inside one barrier interval");
constexpr int win_units(int e) { int n = 0; for (int x = 0; x < NEV; ++x) if (EV[x].pos == EV[e].pos && EV[x].len == EV[e].len) ++n; return n; }
constexpr int win_rank(int e) { int n = 0; for (int x = 0; x < e; ++x) if (EV[x].pos == EV[e].pos && EV[x].len == EV[e].len) ++n; return n; }

DI unsigned row_base(unsigned lane) { const unsigned l32 = lane & 31u, h = lane >> 5, f = ((l32 & 3u) << 2) | ((l32 >> 2) & 3u); return l32 * 256u + 16u * (f ^ h); }
DI void read_q(const Ctx& c, bf16x8 (&q)[8]) {
  const unsigned b = row_base(opaque(c.lane)) + RQO + c.wid * 8192u;
#pragma unroll
  for (int d0 = 0; d0 < 8; ++d0) q[d0] = *(lds_b8p)(b ^ (32u * d0));
}
template <int I> DI void store_rows(const Ctx& c, unsigned hoff) {
  const unsigned lane = opaque(c.lane), rr = lane >> 4, cc = lane & 15u;
  const u32x4 w = *(lds_u4p)(((RQO + c.wid * 8192u + rr * 256u + 16u * (cc ^ rr)) ^ (64u * (I & 1))) + 1024u * I);
  __builtin_amdgcn_raw_buffer_store_b128(w, c.so, hoff + c.wid * 8192u + lane * 16u + 1024u * I, 0, STORE_AUX);
}
template <int HF, int I> DI void store_half(const Ctx& c, unsigned hoff) {
  const unsigned lane = opaque(c.lane), rr = lane >> 3, cc = lane & 7u;
  const u32x4 w = *(lds_u4p)(RQO + c.wid * 8192u + rr * 256u + 16u * (cc ^ rr) + 128u * HF + 2048u * I);
  __builtin_amdgcn_raw_buffer_store_b128(w, c.so, hoff + c.wid * 8192u + rr * 256u + cc * 16u + 128u * HF + 2048u * I, 0, STORE_AUX);
}
template <int KIND, int X, int I, int WV> DI void load_piece(const Ctx& c) {
  if constexpr (KIND == 0) {
    const unsigned m0v = slot_of(X) + c.wid * 2048u, so = ((X & 8) ? c.hB : c.hA) + c.wid * 8192u + 2048u * (X & 3);
    if constexpr (WV < 0) dma1<1024 * I>(m0v, c.vkv[I], (X & 4) ? c.sv : c.sk, so); else dma1_if<WV, 1024 * I>(c.wid, m0v, c.vkv[I], (X & 4) ? c.sv : c.sk, so);
  } else {
    const unsigned m0v = RQO + c.wid * 8192u + 2048u * X, so = (KIND == 1 ? c.hB : c.hA) + c.wid * 8192u + 2048u * X;
    if constexpr (WV < 0) dma1<1024 * I>(m0v, c.vs[(2 * X + I) & 3], c.sq, so); else dma1_if<WV, 1024 * I>(c.wid, m0v, c.vs[(2 * X + I) & 3], c.sq, so);
  }
}
template <int E, int WV> DI void run_event(const Ctx& c) {
  if constexpr (EV[E].kind == 2) store_rows<EV[E].idx>(c, c.hA); else load_piece<EV[E].kind, EV[E].idx, EV[E].half, WV>(c);
}
template <int POS, int E, int... Ws> DI void window_step(const Ctx& c, std::integer_sequence<int, Ws...>) {
  constexpr int t = POS - EV[E].pos, k = win_units(E), r = win_rank(E), L = EV[E].len;
  ([&]() { if constexpr (((8 * r + Ws) * L) / (8 * k) == t) run_event<E, Ws>(c); }(), ...);
}
template <int POS, int E> DI void event_step(const Ctx& c) {
  if constexpr (EV[E].len == 0) { if constexpr (EV[E].pos == POS) run_event<E, -1>(c); }
  else if constexpr (POS >= EV[E].pos && POS < EV[E].pos + EV[E].len) window_step<POS, E>(c, std::make_integer_sequence<int, 8>{});
}
template <int POS, int... Es> DI void events_at_(const Ctx& c, std::integer_sequence<int, Es...>) { (event_step<POS, Es>(c), ...); }
template <int POS> DI void events_at(const Ctx& c) { events_at_<POS>(c, std::make_integer_sequence<int, NEV>{}); }

struct Hd {
  f32x16 S[8], o[4];
  bf16x8 kf[64];
  s16x4 vlo[64], vhi[64];
  u32x4 P[16];
  unsigned ka[8], va[8];
  float mx[2], l[2], mc;
  float x[64][2], e[64][2];
  float lt, il; unsigned ow;
};
struct Prev { u32x2 w[16]; unsigned ow; };

template <int H, int G> DI void k_read(Hd& s) {
  constexpr int I = 8 * H + (G >> 4);
  s.kf[G] = *(lds_b8p)(s.ka[G & 7] + 16384u * ((I % 6) % 3) + 8192u * ((G >> 3) & 1));
}
template <int H> constexpr int pv_ks(int gp) { return H ? (gp & 31) >> 1 : gp >> 2; }
template <int H> constexpr int pv_db(int gp) { return H ? 2 * (gp >> 5) + (gp & 1) : gp & 3; }
template <int H> constexpr int pv_tile(int gp) { return 8 * H + 4 + (pv_ks<H>(gp) >> 2); }
template <int H, int GP> DI void v_read(Hd& s) {
  constexpr int I = pv_tile<H>(GP), ks = pv_ks<H>(GP) & 3, db = pv_db<H>(GP);
  s.vlo[GP] = __builtin_amdgcn_ds_read_tr16_b64_v4i16((lds_s4p)(s.va[2 * db] + 16384u * ((I % 6) % 3) + 4096u * ks));
  s.vhi[GP] = __builtin_amdgcn_ds_read_tr16_b64_v4i16((lds_s4p)(s.va[2 * db + 1] + 16384u * ((I % 6) % 3) + 4096u * ks + 2048u));
}
template <int IPREV, int INEXT> DI void rebase(unsigned (&a)[8]) {
  if constexpr ((IPREV % 6) / 3 != (INEXT % 6) / 3) {
#pragma unroll
    for (int i = 0; i < 8; ++i) a[i] = opaque((INEXT % 6) >= 3 ? a[i] + 49152u : a[i] - 49152u);
  }
}
DI void va_init(const Ctx& c, Hd& s, unsigned base) {
  const unsigned lane = opaque(c.lane), h = lane >> 5, blk = (lane >> 4) & 1u, q4 = (lane & 15u) >> 2, p = lane & 3u;
  const unsigned vb = 256u * (4u * h + q4) + 16u * (4u * q4 + ((2u * blk + (p >> 1)) ^ h)) + 8u * (p & 1u);
#pragma unroll
  for (int i = 0; i < 8; ++i) s.va[i] = opaque((vb ^ (64u * (i >> 1)) ^ (32u * (i & 1))) + base);
}

constexpr int op_n(int k) { const int j = k / 7, p = k % 7; return (p == 0 || p == 2) ? j : ((p == 1 || p == 3) ? j - 1 : j - 2); }
constexpr bool op_on(int k) { const int n = op_n(k); return n >= 0 && n < 64; }
constexpr int op_cost(int k) { const int p = k % 7; return !op_on(k) ? 0 : ((p == 1 || p == 3) ? 2 : 1); }
constexpr int NOPS = 66 * 7;
struct OpTab { short first[130]; };
constexpr OpTab make_optab(int nqk, int pvend) {
  OpTab t{};
  int tot = 0;
  for (int i = 0; i < NOPS; ++i) tot += op_cost(i);
  const int c1 = tot * nqk / 64;
  int c = 0, g = 0;
  for (int k = 0; k < NOPS; ++k) {
    const int gk = c < c1 ? QK0 + c * (64 - QK0) / c1 : 64 + (c - c1) * (pvend + 1) / (tot - c1);
    while (g <= gk) t.first[g++] = (short)k;
    c += op_cost(k);
  }
  while (g < 130) t.first[g++] = (short)NOPS;
  return t;
}
constexpr OpTab OPT0 = make_optab(NQK0, PVEND0), OPT1 = make_optab(NQK1, PVEND1);
template <int H> constexpr int op_first(int g) { return H ? OPT1.first[g] : OPT0.first[g]; }
template <int K> DI void micro_op(Hd& s) {
  if constexpr (op_on(K)) {
    constexpr int n = op_n(K), p = K % 7, ks = n >> 2, kb = ks >> 1, r = 8 * (ks & 1) + 2 * (n & 3);
    if constexpr (p == 0 || p == 2) { s.x[n][p >> 1] = __builtin_fmaf(s.S[kb][r + (p >> 1)], CEXP, s.mc); PIN(s.x[n][p >> 1]); }
    else if constexpr (p == 1 || p == 3) { s.e[n][p >> 1] = __builtin_amdgcn_exp2f(s.x[n][p >> 1]); PIN(s.e[n][p >> 1]); }
    else if constexpr (p == 4 || p == 5) { if constexpr (n == 0) s.l[p - 4] = s.e[n][p - 4]; else s.l[p - 4] += s.e[n][p - 4]; PIN(s.l[p - 4]); }
    else { unsigned w = cvtpk(s.e[n][0], s.e[n][1]); PIN(w); s.P[ks][n & 3] = w; }
  }
}
template <int K0, int... Ks> DI void micro_ops(Hd& s, std::integer_sequence<int, Ks...>) { (micro_op<K0 + Ks>(s), ...); }

template <int I, bool SCALAR> DI void pack_item(const f32x16 (&o)[4], float il, unsigned ow) {
  constexpr int db = I >> 2, g = I & 3;
  float a = o[db][4 * g] * il, b = o[db][4 * g + 1] * il, c = o[db][4 * g + 2] * il, d = o[db][4 * g + 3] * il;
  if constexpr (SCALAR) { PIN(a); PIN(b); PIN(c); PIN(d); }
  const u32x2 w = {cvtpk(a, b), cvtpk(c, d)};
  *(lds_u2p)(ow ^ (16u * I)) = w;
}
template <int I0, int... Is> DI void pack_items(const f32x16 (&o)[4], float il, unsigned ow, std::integer_sequence<int, Is...>) { (pack_item<I0 + Is, false>(o, il, ow), ...); }
DI unsigned stage_base(const Ctx& c) { const unsigned lane = opaque(c.lane), l32 = lane & 31u, h = lane >> 5; return RQO + c.wid * 8192u + l32 * 256u + 16u * (l32 & 7u) + 8u * h; }

template <int H> constexpr int bar_tile(int g) {
  if ((g + BOFF) % 16 != 0 || g + BOFF < 16 || g + BOFF > 112) return -1;
  const int j = (g + BOFF) / 16;
  if (H == 1) return j <= 3 ? 8 + j : (j == 4 ? 13 : (j == 5 ? 15 : -1));
  return j;
}
template <int H, int G> DI void gap(const Ctx& c, Hd& s, const bf16x8 (&q)[8], const Prev& pv) {
  constexpr int R = 8 * H;
  if constexpr (bar_tile<H>(G) >= 0) wait_bar<younger(0, bar_tile<H>(G), 1000 * H + G + 1)>();
  if constexpr (((G + BOFF) % 16) % 4 == 0) __builtin_amdgcn_s_setprio(3 - ((G + BOFF) % 16) / 4);
  events_at<1000 * H + G + 1>(c);
  if constexpr (G < 64) {
    if constexpr (G + PFK < 64) {
      if constexpr (((G + PFK) & 15) == 0) rebase<R + ((G + PFK) >> 4) - 1, R + ((G + PFK) >> 4)>(s.ka);
      k_read<H, G + PFK>(s);
    }
    if constexpr (G == 60) va_init(c, s, RING + (((R + 4) % 6) >= 3 ? 49152u : 0u));
    if constexpr (G + PFV >= 64) v_read<H, G + PFV - 64>(s);
    constexpr int kb = G >> 3, d0 = G & 7;
    if constexpr (d0 == 0) s.S[kb] = __builtin_amdgcn_mfma_f32_32x32x16_bf16(s.kf[G], q[d0], f32x16{}, 0, 0, 0);
    else                   s.S[kb] = __builtin_amdgcn_mfma_f32_32x32x16_bf16(s.kf[G], q[d0], s.S[kb], 0, 0, 0);
    PIN(s.S[kb]);
    if constexpr (G == 10) { s.mx[0] = fmax3(s.S[0][0], s.S[0][1], s.S[0][2]); s.mx[1] = fmax3(s.S[0][3], s.S[0][4], s.S[0][5]); PIN(s.mx[0]); PIN(s.mx[1]); }
    if constexpr (G == 11) { s.mx[0] = fmax3(s.mx[0], s.S[0][6], s.S[0][7]); s.mx[1] = fmax3(s.mx[1], s.S[0][8], s.S[0][9]); PIN(s.mx[0]); PIN(s.mx[1]); }
    if constexpr (G == 12) { s.mx[0] = fmax3(s.mx[0], s.S[0][10], s.S[0][11]); s.mx[1] = fmax3(s.mx[1], s.S[0][12], s.S[0][13]); PIN(s.mx[0]); PIN(s.mx[1]); }
    if constexpr (G == 13) { s.mx[0] = fmax3(s.mx[0], s.S[0][14], s.S[0][15]); s.mx[0] = fmax2(s.mx[0], s.mx[1]); PIN(s.mx[0]); }
    if constexpr (G == 14) { s.mc = -xhalf_max(s.mx[0]) * CEXP; PIN(s.mc); }
    if constexpr (H == 1 && G < 16) *(lds_u2p)(pv.ow ^ (16u * G)) = pv.w[G];
  } else {
    constexpr int GP = G - 64, ks = pv_ks<H>(GP), db = pv_db<H>(GP);
    if constexpr (GP + PFV < 64) {
      rebase<pv_tile<H>(GP + PFV - 1), pv_tile<H>(GP + PFV)>(s.va);
      v_read<H, GP + PFV>(s);
    }
    const s16x8 vf = __builtin_shufflevector(s.vlo[GP], s.vhi[GP], 0, 1, 2, 3, 4, 5, 6, 7);
    if constexpr (ks == 0) s.o[db] = __builtin_amdgcn_mfma_f32_32x32x16_bf16(__builtin_bit_cast(bf16x8, vf), __builtin_bit_cast(bf16x8, s.P[0]), f32x16{}, 0, 0, 0);
    else                   s.o[db] = __builtin_amdgcn_mfma_f32_32x32x16_bf16(__builtin_bit_cast(bf16x8, vf), __builtin_bit_cast(bf16x8, s.P[ks]), s.o[db], 0, 0, 0);
    PIN(s.o[db]);
  }
  if constexpr (H == 1) {
    constexpr int GL = 64 + PVEND1 + 2;
    if constexpr (G == GL) { s.lt = xhalf_sum(s.l[0] + s.l[1]); PIN(s.lt); }
    if constexpr (G == GL + 1) { s.il = __builtin_amdgcn_rcpf(s.lt); PIN(s.il); s.ow = stage_base(c); }
    if constexpr (G >= 99 && G < 115 && ((G - 99) & 1) == 0) pack_item<(G - 99) / 2, true>(s.o, s.il, s.ow);
    if constexpr (G >= 116 && G < 128 && (G - 116) % 3 == 0) store_half<0, (G - 116) / 3>(c, c.hB);
  }
  if constexpr (H == 0 && G < 48) asm volatile("s_nop %0" :: "i"((G < 16 ? RAMP : (G < 32 ? RAMP / 2 : RAMP / 4)) - 1));
  micro_ops<op_first<H>(G)>(s, std::make_integer_sequence<int, op_first<H>(G + 1) - op_first<H>(G)>{});
  GAP();
}
template <int H, int... Gs> DI void gaps(const Ctx& c, Hd& s, const bf16x8 (&q)[8], const Prev& pv, std::integer_sequence<int, Gs...>) { (gap<H, Gs>(c, s, q, pv), ...); }

DI void fallback(const Ctx& c, unsigned hoff, f32x16 (&o)[4], float& l) {
  const unsigned lane = c.lane, l32 = lane & 31u, h = lane >> 5;
  const char* qp = c.pq + hoff + (c.wid * 32u + l32) * 256u + h * 16u;
  const char* kp = c.pk + hoff + l32 * 256u + h * 16u;
  const short* vp = (const short*)(c.pv + hoff) + l32;
  bf16x8 qf[8];
#pragma unroll
  for (int d = 0; d < 8; ++d) qf[d] = *(const bf16x8*)(qp + 32 * d);
  float m = -__builtin_inff();
#pragma nounroll
  for (int kb = 0; kb < 8; ++kb) {
    f32x16 a = {};
#pragma unroll
    for (int d = 0; d < 8; ++d) a = __builtin_amdgcn_mfma_f32_32x32x16_bf16(*(const bf16x8*)(kp + kb * 8192 + 32 * d), qf[d], a, 0, 0, 0);
#pragma unroll
    for (int r = 0; r < 16; ++r) m = fmax2(m, a[r]);
  }
  m = xhalf_max(m);
  float ls = 0.f;
#pragma unroll
  for (int db = 0; db < 4; ++db) o[db] = f32x16{};
#pragma nounroll
  for (int kb = 0; kb < 8; ++kb) {
    f32x16 a = {};
#pragma unroll
    for (int d = 0; d < 8; ++d) a = __builtin_amdgcn_mfma_f32_32x32x16_bf16(*(const bf16x8*)(kp + kb * 8192 + 32 * d), qf[d], a, 0, 0, 0);
#pragma unroll
    for (int sx = 0; sx < 2; ++sx) {
      float e[8];
#pragma unroll
      for (int j = 0; j < 8; ++j) { e[j] = __builtin_amdgcn_exp2f((a[8 * sx + j] - m) * CEXP); ls += e[j]; }
      const u32x4 p = {cvtpk(e[0], e[1]), cvtpk(e[2], e[3]), cvtpk(e[4], e[5]), cvtpk(e[6], e[7])};
#pragma unroll
      for (int db = 0; db < 4; ++db) {
        s16x8 vf;
#pragma unroll
        for (int j = 0; j < 8; ++j) vf[j] = vp[(kb * 32 + 16 * sx + 8 * (j >> 2) + 4 * h + (j & 3)) * 128 + 32 * db];
        o[db] = __builtin_amdgcn_mfma_f32_32x32x16_bf16(__builtin_bit_cast(bf16x8, vf), __builtin_bit_cast(bf16x8, p), o[db], 0, 0, 0);
      }
    }
  }
  l = xhalf_sum(ls);
}
#define UNSAFE_SUM(l) __builtin_expect(__any(!((l) <= LIM_HI && (l) >= LIM_LO)), 0)

template <int H>
DI void head(const Ctx& c, bf16x8 (&q)[8], Prev& pv) {
  constexpr int R = 8 * H;
  Hd s;
  { const unsigned ra = row_base(opaque(c.lane));
#pragma unroll
    for (int d0 = 0; d0 < 8; ++d0) s.ka[d0] = opaque((ra ^ (32u * d0)) + RING + ((R % 6) >= 3 ? 49152u : 0u)); }
  events_at<1000 * H>(c);
  GAP();
  wait_bar<younger(0, R, 1000 * H + 1)>();
  GAP();
  k_read<H, 0>(s); k_read<H, 1>(s); k_read<H, 2>(s); k_read<H, 3>(s);
  GAP();
  gaps<H>(c, s, q, pv, std::make_integer_sequence<int, 128>{});
  if constexpr (H == 0) {
    float l = xhalf_sum(s.l[0] + s.l[1]);
    if (UNSAFE_SUM(l)) fallback(c, c.hA, s.o, l);
    events_at<999>(c);
    GAP();
    const float il = __builtin_amdgcn_rcpf(l);
#pragma unroll
    for (int i = 0; i < 16; ++i) {
      const f32x16& o = s.o[i >> 2]; const int g = 4 * (i & 3);
      pv.w[i] = u32x2{cvtpk(o[g] * il, o[g + 1] * il), cvtpk(o[g + 2] * il, o[g + 3] * il)};
      PIN(pv.w[i]);
    }
    pv.ow = stage_base(c);
    GAP();
    wait_vm<younger(1, 3, 1000)>();
    GAP();
    read_q(c, q);
  } else {
    float l = s.lt;
    if (UNSAFE_SUM(l)) {
      wait_vm<0>();
      fallback(c, c.hB, s.o, l);
      s.il = __builtin_amdgcn_rcpf(l);
      pack_items<0>(s.o, s.il, s.ow, std::make_integer_sequence<int, 8>{});
      store_half<0, 0>(c, c.hB); store_half<0, 1>(c, c.hB); store_half<0, 2>(c, c.hB); store_half<0, 3>(c, c.hB);
    }
    pack_items<8>(s.o, s.il, s.ow, std::make_integer_sequence<int, 8>{});
    store_half<1, 0>(c, c.hB); store_half<1, 1>(c, c.hB); store_half<1, 2>(c, c.hB); store_half<1, 3>(c, c.hB);
  }
  GAP();
}

__global__ __launch_bounds__(512, 2) void attn_fwd(const void* __restrict__ Q, const void* __restrict__ K, const void* __restrict__ V, void* __restrict__ O) {
  const unsigned tid = threadIdx.x, lane = tid & 63u;
  {
    unsigned long long pc; unsigned off, t, m;
    asm volatile("attn_fwd_code_begin:\n\t"
                 "s_getpc_b64 %0\n\t"
                 "v_lshlrev_b32 %1, 7, %4\n\t"
                 "v_lshrrev_b32 %2, 6, %4\n\t"
                 "s_mov_b32 %3, attn_fwd_code_end-attn_fwd_code_begin\n\t"
                 "v_cmp_gt_u32 vcc, %3, %1\n\t"
                 "v_readfirstlane_b32 %3, %2\n\t"
                 "s_nop 3\n\t"
                 "v_cndmask_b32 %1, 0, %1, vcc\n\t"
                 "s_lshl_b32 %3, %3, 8\n\t"
                 "s_add_u32 %3, %3, %5\n\t"
                 "s_mov_b32 m0, %3\n\t"
                 "s_nop 0\n\t"
                 "global_load_lds_dword %1, %0"
                 : "=&s"(pc), "=&v"(off), "=&v"(t), "=&s"(m) : "v"(tid), "i"(slot_of(5)) : "memory", "vcc", "scc");
    GAP();
  }
  Ctx c;
  c.lane = lane;
  c.wid = __builtin_amdgcn_readfirstlane(tid >> 6);
  const int nbytes = NHEADS * (int)HEADB;
  c.sq = __builtin_amdgcn_make_buffer_rsrc((void*)Q, (short)0, nbytes, 0x00020000);
  c.sk = __builtin_amdgcn_make_buffer_rsrc((void*)K, (short)0, nbytes, 0x00020000);
  c.sv = __builtin_amdgcn_make_buffer_rsrc((void*)V, (short)0, nbytes, 0x00020000);
  c.so = __builtin_amdgcn_make_buffer_rsrc((void*)O, (short)0, nbytes, 0x00020000);
  c.pq = (const char*)Q; c.pk = (const char*)K; c.pv = (const char*)V;
  {
    const unsigned r = lane >> 4, p = lane & 15u, v0 = r * 256u + 16u * (p ^ (r << 2));
#pragma unroll
    for (int j = 0; j < 4; ++j) c.vs[j] = opaque(v0 ^ (16u * j));
    const unsigned w2 = (32u * c.wid) & 48u;
    c.vkv[0] = opaque(v0 ^ w2); c.vkv[1] = opaque(v0 ^ (w2 + 16u));
  }
  { const unsigned b = blockIdx.x, h0 = (b & 7u) * 64u + (b >> 3) * 2u;
    c.hA = h0 * HEADB; c.hB = (h0 + 1u) * HEADB; }
  asm volatile("s_nop 4");
  load_piece<4, 0, 0, -1>(c); load_piece<4, 0, 1, -1>(c); load_piece<4, 1, 0, -1>(c); load_piece<4, 1, 1, -1>(c);
  load_piece<4, 2, 0, -1>(c); load_piece<4, 2, 1, -1>(c); load_piece<4, 3, 0, -1>(c); load_piece<4, 3, 1, -1>(c);
  load_piece<0, 0, 0, -1>(c); load_piece<0, 0, 1, -1>(c); load_piece<0, 1, 0, -1>(c); load_piece<0, 1, 1, -1>(c);
  bf16x8 q[8];
  GAP();
  wait_vm<4>();
  GAP();
  read_q(c, q);
  Prev pv;
  head<0>(c, q, pv);
  head<1>(c, q, pv);
  asm volatile("attn_fwd_code_end:");
}

extern "C" void kernel_launch(void* const* d_in, const int* in_sizes, int n_in, void* d_out, int out_size, void* d_ws, size_t ws_size, hipStream_t stream) {
  (void)in_sizes; (void)n_in; (void)out_size; (void)d_ws; (void)ws_size;
  (void)hipFuncSetAttribute((const void*)attn_fwd, hipFuncAttributeMaxDynamicSharedMemorySize, (int)LDS_BYTES);
  hipLaunchKernelGGL(attn_fwd, dim3(NWG), dim3(512), LDS_BYTES, stream, d_in[0], d_in[1], d_in[2], d_out);
}
```

```cpp
#include <hip/hip_runtime.h>
#include <hip/hip_bf16.h>
#include <utility>

typedef float    f32x16 __attribute__((ext_vector_type(16)));
typedef float    f32x2  __attribute__((ext_vector_type(2)));
typedef __bf16   bf16x8 __attribute__((ext_vector_type(8)));
typedef __bf16   bf16x2 __attribute__((ext_vector_type(2)));
typedef short    s16x4  __attribute__((ext_vector_type(4)));
typedef short    s16x8  __attribute__((ext_vector_type(8)));
typedef unsigned u32x4  __attribute__((ext_vector_type(4)));
typedef unsigned u32x2  __attribute__((ext_vector_type(2)));
typedef __attribute__((address_space(3))) const bf16x8* lds_b8p;
typedef __attribute__((address_space(3))) s16x4* lds_s4p;
typedef __attribute__((address_space(3))) u32x2* lds_u2p;
typedef __attribute__((address_space(3))) const u32x4* lds_u4p;

#define DI __device__ __forceinline__
#define GAP() __builtin_amdgcn_sched_barrier(0)
#define PIN(x) asm volatile("" : "+v"(x))

constexpr unsigned HEADB = 65536u;
constexpr unsigned RQO = 0u, RING = 65536u, LDS_BYTES = 163840u;
constexpr int NHEADS = 512, NWG = 256;
constexpr float CEXP = 0.08838834764831845f * 1.4426950408889634f;
constexpr float LIM_HI = 1073741824.0f, LIM_LO = 1.0f / LIM_HI;
constexpr int STORE_AUX = 18;
constexpr int BOFF = 8;
constexpr int PFK = 4, PFV = 3;
constexpr int QK0 = 15;
constexpr int NQK0 = 32, PVEND0 = 58;
constexpr int NQK1 = 44, PVEND1 = 27;
constexpr int RAMP = 12;

DI unsigned cvtpk(float lo, float hi) { f32x2 v = {lo, hi}; bf16x2 b = __builtin_convertvector(v, bf16x2); return __builtin_bit_cast(unsigned, b); }
DI float fmax2(float a, float b) { return __builtin_elementwise_maximum(a, b); }
DI float fmax3(float a, float b, float c) { return __builtin_elementwise_maximum(__builtin_elementwise_maximum(a, b), c); }
DI unsigned opaque(unsigned x) { asm volatile("" : "+v"(x)); return x; }
DI float xhalf_max(float m) { auto rr = __builtin_amdgcn_permlane32_swap(__float_as_uint(m), __float_as_uint(m), false, false); return fmax2(__uint_as_float(rr[0]), __uint_as_float(rr[1])); }
DI float xhalf_sum(float m) { auto rr = __builtin_amdgcn_permlane32_swap(__float_as_uint(m), __float_as_uint(m), false, false); return __uint_as_float(rr[0]) + __uint_as_float(rr[1]); }

template <int IMM> DI void dma1(unsigned m0v, unsigned v, __amdgpu_buffer_rsrc_t srd, unsigned soff) {
  asm volatile("s_mov_b32 m0, %0\n\ts_nop 0\n\tbuffer_load_dwordx4 %1, %2, %3 offen offset:%4 nt lds" :: "s"(m0v), "v"(v), "s"(srd), "s"(soff), "i"(IMM) : "memory");
}
template <int SEL, int IMM> DI void dma1_if(unsigned wid, unsigned m0v, unsigned v, __amdgpu_buffer_rsrc_t srd, unsigned soff) {
  asm volatile("s_cmp_eq_u32 %0, %5\n\ts_cbranch_scc0 1f\n\ts_mov_b32 m0, %1\n\ts_nop 0\n\t"
               "buffer_load_dwordx4 %2, %3, %4 offen offset:%6 nt lds\n1:"
               :: "s"(wid), "s"(m0v), "v"(v), "s"(srd), "s"(soff), "i"(SEL), "i"(IMM) : "memory", "scc");
}
template <int N> DI void wait_bar() { asm volatile("s_waitcnt vmcnt(%0)\n\ts_barrier" :: "i"(N) : "memory"); }
template <int N> DI void wait_vm() { asm volatile("s_waitcnt vmcnt(%0)" :: "i"(N) : "memory"); }

struct Ctx {
  __amdgpu_buffer_rsrc_t sq, sk, sv, so;
  const char *pq, *pk, *pv;
  unsigned wid, lane;
  unsigned vs[4], vkv[2];
  unsigned hA, hB;
};
constexpr unsigned slot_of(int i) { return RING + 16384u * (unsigned)(i % 6); }

struct Ev { int pos, len, kind, idx, half; };
#define U2(p0, p1, k, i) {p0, 0, k, i, 0}, {p1, 0, k, i, 1}
#define W(p, l, k, i, h) {p, l, k, i, h}
#define S1(p, i) {p, 0, 2, i, 0}
constexpr Ev EV[] = {
  U2(0, 0, 0, 2), W(1, 8, 0, 3, 0), W(9, 16, 0, 3, 1), W(9, 16, 0, 4, 0), W(9, 16, 0, 4, 1), W(25, 16, 0, 5, 0), W(25, 16, 0, 5, 1), W(25, 16, 0, 6, 0),
  W(41, 16, 0, 6, 1), W(41, 16, 0, 7, 0), W(41, 16, 0, 7, 1), W(57, 16, 1, 0, 0), W(57, 16, 1, 0, 1), W(57, 16, 1, 1, 0), W(73, 16, 1, 1, 1), W(73, 16, 1, 2, 0), W(73, 16, 1, 2, 1),
  W(89, 16, 1, 3, 0), W(89, 16, 1, 3, 1), W(89, 16, 0, 8, 0), W(105, 16, 0, 8, 1), W(105, 16, 0, 9, 0), W(105, 16, 0, 9, 1), W(121, 8, 0, 10, 0), {999, 0, 0, 10, 1},
  W(1001, 8, 0, 11, 0), W(1001, 8, 0, 11, 1), W(1009, 16, 0, 12, 0), W(1009, 16, 0, 12, 1), W(1009, 16, 0, 13, 0),
  W(1025, 16, 0, 13, 1), W(1025, 16, 0, 14, 0), W(1025, 16, 0, 14, 1), W(1041, 16, 0, 15, 0), W(1041, 16, 0, 15, 1),
  S1(1066, 0), S1(1070, 1), S1(1074, 2), S1(1078, 3), S1(1082, 4), S1(1086, 5), S1(1090, 6), S1(1094, 7)};
constexpr int NEV = sizeof(EV) / sizeof(EV[0]);
constexpr int younger(int kind, int idx, int pos) {
  int e0 = -1;
  for (int e = 0; e < NEV; ++e) if (EV[e].kind == kind && EV[e].idx == idx) e0 = e;
  int n = 0;
  if (e0 < 0) n = (kind == 0 && idx == 0) ? 2 : 0;
  for (int e = e0 + 1; e < NEV; ++e) if (EV[e].pos < pos) ++n;
  return n;
}
constexpr bool ev_ok() {
  for (int e = 1; e < NEV; ++e) if (EV[e].pos < EV[e - 1].pos) return false;
  for (int e = 0; e < NEV; ++e) if (EV[e].len > 0) { const int g0 = (EV[e].pos - 1) % 1000, g1 = g0 + EV[e].len - 1; if ((g0 + BOFF) / 16 != (g1 + BOFF) / 16) return false; }
  return true;
}
static_assert(ev_ok(), "events in program order, windows inside one barrier interval");
constexpr int win_units(int e) { int n = 0; for (int x = 0; x < NEV; ++x) if (EV[x].pos == EV[e].pos && EV[x].len == EV[e].len) ++n; return n; }
constexpr int win_rank(int e) { int n = 0; for (int x = 0; x < e; ++x) if (EV[x].pos == EV[e].pos && EV[x].len == EV[e].len) ++n; return n; }

DI unsigned row_base(unsigned lane) { const unsigned l32 = lane & 31u, h = lane >> 5, f = ((l32 & 3u) << 2) | ((l32 >> 2) & 3u); return l32 * 256u + 16u * (f ^ h); }
DI void read_q(const Ctx& c, bf16x8 (&q)[8]) {
  const unsigned b = row_base(opaque(c.lane)) + RQO + c.wid * 8192u;
#pragma unroll
  for (int d0 = 0; d0 < 8; ++d0) q[d0] = *(lds_b8p)(b ^ (32u * d0));
}
template <int I> DI void store_rows(const Ctx& c, unsigned hoff) {
  const unsigned lane = opaque(c.lane), rr = lane >> 4, cc = lane & 15u;
  const u32x4 w = *(lds_u4p)(((RQO + c.wid * 8192u + rr * 256u + 16u * (cc ^ rr)) ^ (64u * (I & 1))) + 1024u * I);
  __builtin_amdgcn_raw_buffer_store_b128(w, c.so, hoff + c.wid * 8192u + lane * 16u + 1024u * I, 0, STORE_AUX);
}
template <int HF, int I> DI void store_half(const Ctx& c, unsigned hoff) {
  const unsigned lane = opaque(c.lane), rr = lane >> 3, cc = lane & 7u;
  const u32x4 w = *(lds_u4p)(RQO + c.wid * 8192u + rr * 256u + 16u * (cc ^ rr) + 128u * HF + 2048u * I);
  __builtin_amdgcn_raw_buffer_store_b128(w, c.so, hoff + c.wid * 8192u + rr * 256u + cc * 16u + 128u * HF + 2048u * I, 0, STORE_AUX);
}
template <int KIND, int X, int I, int WV> DI void load_piece(const Ctx& c) {
  if constexpr (KIND == 0) {
    const unsigned m0v = slot_of(X) + c.wid * 2048u, so = ((X & 8) ? c.hB : c.hA) + c.wid * 8192u + 2048u * (X & 3);
    if constexpr (WV < 0) dma1<1024 * I>(m0v, c.vkv[I], (X & 4) ? c.sv : c.sk, so); else dma1_if<WV, 1024 * I>(c.wid, m0v, c.vkv[I], (X & 4) ? c.sv : c.sk, so);
  } else {
    const unsigned m0v = RQO + c.wid * 8192u + 2048u * X, so = (KIND == 1 ? c.hB : c.hA) + c.wid * 8192u + 2048u * X;
    if constexpr (WV < 0) dma1<1024 * I>(m0v, c.vs[(2 * X + I) & 3], c.sq, so); else dma1_if<WV, 1024 * I>(c.wid, m0v, c.vs[(2 * X + I) & 3], c.sq, so);
  }
}
template <int E, int WV> DI void run_event(const Ctx& c) {
  if constexpr (EV[E].kind == 2) store_rows<EV[E].idx>(c, c.hA); else load_piece<EV[E].kind, EV[E].idx, EV[E].half, WV>(c);
}
template <int POS, int E, int... Ws> DI void window_step(const Ctx& c, std::integer_sequence<int, Ws...>) {
  constexpr int t = POS - EV[E].pos, k = win_units(E), r = win_rank(E), L = EV[E].len;
  ([&]() { if constexpr (((8 * r + Ws) * L) / (8 * k) == t) run_event<E, Ws>(c); }(), ...);
}
template <int POS, int E> DI void event_step(const Ctx& c) {
  if constexpr (EV[E].len == 0) { if constexpr (EV[E].pos == POS) run_event<E, -1>(c); }
  else if constexpr (POS >= EV[E].pos && POS < EV[E].pos + EV[E].len) window_step<POS, E>(c, std::make_integer_sequence<int, 8>{});
}
template <int POS, int... Es> DI void events_at_(const Ctx& c, std::integer_sequence<int, Es...>) { (event_step<POS, Es>(c), ...); }
template <int POS> DI void events_at(const Ctx& c) { events_at_<POS>(c, std::make_integer_sequence<int, NEV>{}); }

struct Hd {
  f32x16 S[8], o[4];
  bf16x8 kf[64];
  s16x4 vlo[64], vhi[64];
  u32x4 P[16];
  unsigned ka[8], va[8];
  float mx[2], l[2], mc;
  float x[64][2], e[64][2];
  float lt, il; unsigned ow;
};
struct Prev { u32x2 w[16]; unsigned ow; };

template <int H, int G> DI void k_read(Hd& s) {
  constexpr int I = 8 * H + (G >> 4);
  s.kf[G] = *(lds_b8p)(s.ka[G & 7] + 16384u * ((I % 6) % 3) + 8192u * ((G >> 3) & 1));
}
template <int H> constexpr int pv_ks(int gp) { return H ? (gp & 31) >> 1 : gp >> 2; }
template <int H> constexpr int pv_db(int gp) { return H ? 2 * (gp >> 5) + (gp & 1) : gp & 3; }
template <int H> constexpr int pv_tile(int gp) { return 8 * H + 4 + (pv_ks<H>(gp) >> 2); }
template <int H, int GP> DI void v_read(Hd& s) {
  constexpr int I = pv_tile<H>(GP), ks = pv_ks<H>(GP) & 3, db = pv_db<H>(GP);
  s.vlo[GP] = __builtin_amdgcn_ds_read_tr16_b64_v4i16((lds_s4p)(s.va[2 * db] + 16384u * ((I % 6) % 3) + 4096u * ks));
  s.vhi[GP] = __builtin_amdgcn_ds_read_tr16_b64_v4i16((lds_s4p)(s.va[2 * db + 1] + 16384u * ((I % 6) % 3) + 4096u * ks + 2048u));
}
template <int IPREV, int INEXT> DI void rebase(unsigned (&a)[8]) {
  if constexpr ((IPREV % 6) / 3 != (INEXT % 6) / 3) {
#pragma unroll
    for (int i = 0; i < 8; ++i) a[i] = opaque((INEXT % 6) >= 3 ? a[i] + 49152u : a[i] - 49152u);
  }
}
DI void va_init(const Ctx& c, Hd& s, unsigned base) {
  const unsigned lane = opaque(c.lane), h = lane >> 5, blk = (lane >> 4) & 1u, q4 = (lane & 15u) >> 2, p = lane & 3u;
  const unsigned vb = 256u * (4u * h + q4) + 16u * (4u * q4 + ((2u * blk + (p >> 1)) ^ h)) + 8u * (p & 1u);
#pragma unroll
  for (int i = 0; i < 8; ++i) s.va[i] = opaque((vb ^ (64u * (i >> 1)) ^ (32u * (i & 1))) + base);
}

constexpr int op_n(int k) { const int j = k / 7, p = k % 7; return (p == 0 || p == 2) ? j : ((p == 1 || p == 3) ? j - 1 : j - 2); }
constexpr bool op_on(int k) { const int n = op_n(k); return n >= 0 && n < 64; }
constexpr int op_cost(int k) { const int p = k % 7; return !op_on(k) ? 0 : ((p == 1 || p == 3) ? 2 : 1); }
constexpr int NOPS = 66 * 7;
struct OpTab { short first[130]; };
constexpr OpTab make_optab(int nqk, int pvend) {
  OpTab t{};
  int tot = 0;
  for (int i = 0; i < NOPS; ++i) tot += op_cost(i);
  const int c1 = tot * nqk / 64;
  int c = 0, g = 0;
  for (int k = 0; k < NOPS; ++k) {
    const int gk = c < c1 ? QK0 + c * (64 - QK0) / c1 : 64 + (c - c1) * (pvend + 1) / (tot - c1);
    while (g <= gk) t.first[g++] = (short)k;
    c += op_cost(k);
  }
  while (g < 130) t.first[g++] = (short)NOPS;
  return t;
}
constexpr OpTab OPT0 = make_optab(NQK0, PVEND0), OPT1 = make_optab(NQK1, PVEND1);
template <int H> constexpr int op_first(int g) { return H ? OPT1.first[g] : OPT0.first[g]; }
template <int K> DI void micro_op(Hd& s) {
  if constexpr (op_on(K)) {
    constexpr int n = op_n(K), p = K % 7, ks = n >> 2, kb = ks >> 1, r = 8 * (ks & 1) + 2 * (n & 3);
    if constexpr (p == 0 || p == 2) { s.x[n][p >> 1] = __builtin_fmaf(s.S[kb][r + (p >> 1)], CEXP, s.mc); PIN(s.x[n][p >> 1]); }
    else if constexpr (p == 1 || p == 3) { s.e[n][p >> 1] = __builtin_amdgcn_exp2f(s.x[n][p >> 1]); PIN(s.e[n][p >> 1]); }
    else if constexpr (p == 4 || p == 5) { if constexpr (n == 0) s.l[p - 4] = s.e[n][p - 4]; else s.l[p - 4] += s.e[n][p - 4]; PIN(s.l[p - 4]); }
    else { unsigned w = cvtpk(s.e[n][0], s.e[n][1]); PIN(w); s.P[ks][n & 3] = w; }
  }
}
template <int K0, int... Ks> DI void micro_ops(Hd& s, std::integer_sequence<int, Ks...>) { (micro_op<K0 + Ks>(s), ...); }

template <int I, bool SCALAR> DI void pack_item(const f32x16 (&o)[4], float il, unsigned ow) {
  constexpr int db = I >> 2, g = I & 3;
  float a = o[db][4 * g] * il, b = o[db][4 * g + 1] * il, c = o[db][4 * g + 2] * il, d = o[db][4 * g + 3] * il;
  if constexpr (SCALAR) { PIN(a); PIN(b); PIN(c); PIN(d); }
  const u32x2 w = {cvtpk(a, b), cvtpk(c, d)};
  *(lds_u2p)(ow ^ (16u * I)) = w;
}
template <int I0, int... Is> DI void pack_items(const f32x16 (&o)[4], float il, unsigned ow, std::integer_sequence<int, Is...>) { (pack_item<I0 + Is, false>(o, il, ow), ...); }
DI unsigned stage_base(const Ctx& c) { const unsigned lane = opaque(c.lane), l32 = lane & 31u, h = lane >> 5; return RQO + c.wid * 8192u + l32 * 256u + 16u * (l32 & 7u) + 8u * h; }

template <int H> constexpr int bar_tile(int g) {
  if ((g + BOFF) % 16 != 0 || g + BOFF < 16 || g + BOFF > 112) return -1;
  const int j = (g + BOFF) / 16;
  if (H == 1) return j <= 3 ? 8 + j : (j == 4 ? 13 : (j == 5 ? 15 : -1));
  return j;
}
template <int H, int G> DI void gap(const Ctx& c, Hd& s, const bf16x8 (&q)[8], const Prev& pv) {
  constexpr int R = 8 * H;
  if constexpr (bar_tile<H>(G) >= 0) wait_bar<younger(0, bar_tile<H>(G), 1000 * H + G + 1)>();
  if constexpr (((G + BOFF) % 16) % 4 == 0) __builtin_amdgcn_s_setprio(3 - ((G + BOFF) % 16) / 4);
  events_at<1000 * H + G + 1>(c);
  if constexpr (G < 64) {
    if constexpr (G + PFK < 64) {
      if constexpr (((G + PFK) & 15) == 0) rebase<R + ((G + PFK) >> 4) - 1, R + ((G + PFK) >> 4)>(s.ka);
      k_read<H, G + PFK>(s);
    }
    if constexpr (G == 60) va_init(c, s, RING + (((R + 4) % 6) >= 3 ? 49152u : 0u));
    if constexpr (G + PFV >= 64) v_read<H, G + PFV - 64>(s);
    constexpr int kb = G >> 3, d0 = G & 7;
    if constexpr (d0 == 0) s.S[kb] = __builtin_amdgcn_mfma_f32_32x32x16_bf16(s.kf[G], q[d0], f32x16{}, 0, 0, 0);
    else                   s.S[kb] = __builtin_amdgcn_mfma_f32_32x32x16_bf16(s.kf[G], q[d0], s.S[kb], 0, 0, 0);
    PIN(s.S[kb]);
    if constexpr (G == 10) { s.mx[0] = fmax3(s.S[0][0], s.S[0][1], s.S[0][2]); s.mx[1] = fmax3(s.S[0][3], s.S[0][4], s.S[0][5]); PIN(s.mx[0]); PIN(s.mx[1]); }
    if constexpr (G == 11) { s.mx[0] = fmax3(s.mx[0], s.S[0][6], s.S[0][7]); s.mx[1] = fmax3(s.mx[1], s.S[0][8], s.S[0][9]); PIN(s.mx[0]); PIN(s.mx[1]); }
    if constexpr (G == 12) { s.mx[0] = fmax3(s.mx[0], s.S[0][10], s.S[0][11]); s.mx[1] = fmax3(s.mx[1], s.S[0][12], s.S[0][13]); PIN(s.mx[0]); PIN(s.mx[1]); }
    if constexpr (G == 13) { s.mx[0] = fmax3(s.mx[0], s.S[0][14], s.S[0][15]); s.mx[0] = fmax2(s.mx[0], s.mx[1]); PIN(s.mx[0]); }
    if constexpr (G == 14) { s.mc = -xhalf_max(s.mx[0]) * CEXP; PIN(s.mc); }
    if constexpr (H == 1 && G < 16) *(lds_u2p)(pv.ow ^ (16u * G)) = pv.w[G];
  } else {
    constexpr int GP = G - 64, ks = pv_ks<H>(GP), db = pv_db<H>(GP);
    if constexpr (GP + PFV < 64) {
      rebase<pv_tile<H>(GP + PFV - 1), pv_tile<H>(GP + PFV)>(s.va);
      v_read<H, GP + PFV>(s);
    }
    const s16x8 vf = __builtin_shufflevector(s.vlo[GP], s.vhi[GP], 0, 1, 2, 3, 4, 5, 6, 7);
    if constexpr (ks == 0) s.o[db] = __builtin_amdgcn_mfma_f32_32x32x16_bf16(__builtin_bit_cast(bf16x8, vf), __builtin_bit_cast(bf16x8, s.P[0]), f32x16{}, 0, 0, 0);
    else                   s.o[db] = __builtin_amdgcn_mfma_f32_32x32x16_bf16(__builtin_bit_cast(bf16x8, vf), __builtin_bit_cast(bf16x8, s.P[ks]), s.o[db], 0, 0, 0);
    PIN(s.o[db]);
  }
  if constexpr (H == 1) {
    constexpr int GL = 64 + PVEND1 + 2;
    if constexpr (G == GL) { s.lt = xhalf_sum(s.l[0] + s.l[1]); PIN(s.lt); }
    if constexpr (G == GL + 1) { s.il = __builtin_amdgcn_rcpf(s.lt); PIN(s.il); s.ow = stage_base(c); }
    if constexpr (G >= 99 && G < 115 && ((G - 99) & 1) == 0) pack_item<(G - 99) / 2, true>(s.o, s.il, s.ow);
    if constexpr (G >= 116 && G < 128 && (G - 116) % 3 == 0) store_half<0, (G - 116) / 3>(c, c.hB);
  }
  if constexpr (H == 0 && G < 48) asm volatile("s_nop %0" :: "i"((G < 16 ? RAMP : (G < 32 ? RAMP / 2 : RAMP / 4)) - 1));
  micro_ops<op_first<H>(G)>(s, std::make_integer_sequence<int, op_first<H>(G + 1) - op_first<H>(G)>{});
  GAP();
}
template <int H, int... Gs> DI void gaps(const Ctx& c, Hd& s, const bf16x8 (&q)[8], const Prev& pv, std::integer_sequence<int, Gs...>) { (gap<H, Gs>(c, s, q, pv), ...); }

DI void fallback(const Ctx& c, unsigned hoff, f32x16 (&o)[4], float& l) {
  const unsigned lane = c.lane, l32 = lane & 31u, h = lane >> 5;
  const char* qp = c.pq + hoff + (c.wid * 32u + l32) * 256u + h * 16u;
  const char* kp = c.pk + hoff + l32 * 256u + h * 16u;
  const short* vp = (const short*)(c.pv + hoff) + l32;
  bf16x8 qf[8];
#pragma unroll
  for (int d = 0; d < 8; ++d) qf[d] = *(const bf16x8*)(qp + 32 * d);
  float m = -__builtin_inff();
#pragma nounroll
  for (int kb = 0; kb < 8; ++kb) {
    f32x16 a = {};
#pragma unroll
    for (int d = 0; d < 8; ++d) a = __builtin_amdgcn_mfma_f32_32x32x16_bf16(*(const bf16x8*)(kp + kb * 8192 + 32 * d), qf[d], a, 0, 0, 0);
#pragma unroll
    for (int r = 0; r < 16; ++r) m = fmax2(m, a[r]);
  }
  m = xhalf_max(m);
  float ls = 0.f;
#pragma unroll
  for (int db = 0; db < 4; ++db) o[db] = f32x16{};
#pragma nounroll
  for (int kb = 0; kb < 8; ++kb) {
    f32x16 a = {};
#pragma unroll
    for (int d = 0; d < 8; ++d) a = __builtin_amdgcn_mfma_f32_32x32x16_bf16(*(const bf16x8*)(kp + kb * 8192 + 32 * d), qf[d], a, 0, 0, 0);
#pragma unroll
    for (int sx = 0; sx < 2; ++sx) {
      float e[8];
#pragma unroll
      for (int j = 0; j < 8; ++j) { e[j] = __builtin_amdgcn_exp2f((a[8 * sx + j] - m) * CEXP); ls += e[j]; }
      const u32x4 p = {cvtpk(e[0], e[1]), cvtpk(e[2], e[3]), cvtpk(e[4], e[5]), cvtpk(e[6], e[7])};
#pragma unroll
      for (int db = 0; db < 4; ++db) {
        s16x8 vf;
#pragma unroll
        for (int j = 0; j < 8; ++j) vf[j] = vp[(kb * 32 + 16 * sx + 8 * (j >> 2) + 4 * h + (j & 3)) * 128 + 32 * db];
        o[db] = __builtin_amdgcn_mfma_f32_32x32x16_bf16(__builtin_bit_cast(bf16x8, vf), __builtin_bit_cast(bf16x8, p), o[db], 0, 0, 0);
      }
    }
  }
  l = xhalf_sum(ls);
}
#define UNSAFE_SUM(l) __builtin_expect(__any(!((l) <= LIM_HI && (l) >= LIM_LO)), 0)

template <int H>
DI void head(const Ctx& c, bf16x8 (&q)[8], Prev& pv) {
  constexpr int R = 8 * H;
  Hd s;
  { const unsigned ra = row_base(opaque(c.lane));
#pragma unroll
    for (int d0 = 0; d0 < 8; ++d0) s.ka[d0] = opaque((ra ^ (32u * d0)) + RING + ((R % 6) >= 3 ? 49152u : 0u)); }
  events_at<1000 * H>(c);
  GAP();
  wait_bar<younger(0, R, 1000 * H + 1)>();
  GAP();
  k_read<H, 0>(s); k_read<H, 1>(s); k_read<H, 2>(s); k_read<H, 3>(s);
  GAP();
  gaps<H>(c, s, q, pv, std::make_integer_sequence<int, 128>{});
  if constexpr (H == 0) {
    float l = xhalf_sum(s.l[0] + s.l[1]);
    if (UNSAFE_SUM(l)) fallback(c, c.hA, s.o, l);
    events_at<999>(c);
    GAP();
    const float il = __builtin_amdgcn_rcpf(l);
#pragma unroll
    for (int i = 0; i < 16; ++i) {
      const f32x16& o = s.o[i >> 2]; const int g = 4 * (i & 3);
      pv.w[i] = u32x2{cvtpk(o[g] * il, o[g + 1] * il), cvtpk(o[g + 2] * il, o[g + 3] * il)};
      PIN(pv.w[i]);
    }
    pv.ow = stage_base(c);
    GAP();
    wait_vm<younger(1, 3, 1000)>();
    GAP();
    read_q(c, q);
  } else {
    float l = s.lt;
    if (UNSAFE_SUM(l)) {
      wait_vm<0>();
      fallback(c, c.hB, s.o, l);
      s.il = __builtin_amdgcn_rcpf(l);
      pack_items<0>(s.o, s.il, s.ow, std::make_integer_sequence<int, 8>{});
      store_half<0, 0>(c, c.hB); store_half<0, 1>(c, c.hB); store_half<0, 2>(c, c.hB); store_half<0, 3>(c, c.hB);
    }
    pack_items<8>(s.o, s.il, s.ow, std::make_integer_sequence<int, 8>{});
    store_half<1, 0>(c, c.hB); store_half<1, 1>(c, c.hB); store_half<1, 2>(c, c.hB); store_half<1, 3>(c, c.hB);
  }
  GAP();
}

__global__ __launch_bounds__(512, 2) void attn_fwd(const void* __restrict__ Q, const void* __restrict__ K, const void* __restrict__ V, void* __restrict__ O) {
  const unsigned tid = threadIdx.x, lane = tid & 63u;
  {
    unsigned long long pc; unsigned off, t, m;
    asm volatile("attn_fwd_code_begin:\n\t"
                 "s_getpc_b64 %0\n\t"
                 "v_lshlrev_b32 %1, 7, %4\n\t"
                 "v_lshrrev_b32 %2, 6, %4\n\t"
                 "s_mov_b32 %3, attn_fwd_code_end-attn_fwd_code_begin\n\t"
                 "v_cmp_gt_u32 vcc, %3, %1\n\t"
                 "v_readfirstlane_b32 %3, %2\n\t"
                 "s_nop 3\n\t"
                 "v_cndmask_b32 %1, 0, %1, vcc\n\t"
                 "s_lshl_b32 %3, %3, 8\n\t"
                 "s_add_u32 %3, %3, %5\n\t"
                 "s_mov_b32 m0, %3\n\t"
                 "s_nop 0\n\t"
                 "global_load_lds_dword %1, %0"
                 : "=&s"(pc), "=&v"(off), "=&v"(t), "=&s"(m) : "v"(tid), "i"(slot_of(5)) : "memory", "vcc", "scc");
    GAP();
  }
  Ctx c;
  c.lane = lane;
  c.wid = __builtin_amdgcn_readfirstlane(tid >> 6);
  const int nbytes = NHEADS * (int)HEADB;
  c.sq = __builtin_amdgcn_make_buffer_rsrc((void*)Q, (short)0, nbytes, 0x00020000);
  c.sk = __builtin_amdgcn_make_buffer_rsrc((void*)K, (short)0, nbytes, 0x00020000);
  c.sv = __builtin_amdgcn_make_buffer_rsrc((void*)V, (short)0, nbytes, 0x00020000);
  c.so = __builtin_amdgcn_make_buffer_rsrc((void*)O, (short)0, nbytes, 0x00020000);
  c.pq = (const char*)Q; c.pk = (const char*)K; c.pv = (const char*)V;
  {
    const unsigned r = lane >> 4, p = lane & 15u, v0 = r * 256u + 16u * (p ^ (r << 2));
#pragma unroll
    for (int j = 0; j < 4; ++j) c.vs[j] = opaque(v0 ^ (16u * j));
    const unsigned w2 = (32u * c.wid) & 48u;
    c.vkv[0] = opaque(v0 ^ w2); c.vkv[1] = opaque(v0 ^ (w2 + 16u));
  }
  { const unsigned b = blockIdx.x, h0 = (b & 7u) * 64u + (b >> 3) * 2u;
    c.hA = h0 * HEADB; c.hB = (h0 + 1u) * HEADB; }
  asm volatile("s_nop 4");
  load_piece<4, 0, 0, -1>(c); load_piece<4, 0, 1, -1>(c); load_piece<4, 1, 0, -1>(c); load_piece<4, 1, 1, -1>(c);
  load_piece<4, 2, 0, -1>(c); load_piece<4, 2, 1, -1>(c); load_piece<4, 3, 0, -1>(c); load_piece<4, 3, 1, -1>(c);
  load_piece<0, 0, 0, -1>(c); load_piece<0, 0, 1, -1>(c); load_piece<0, 1, 0, -1>(c); load_piece<0, 1, 1, -1>(c);
  bf16x8 q[8];
  GAP();
  wait_vm<4>();
  GAP();
  read_q(c, q);
  Prev pv;
  head<0>(c, q, pv);
  head<1>(c, q, pv);
  asm volatile("attn_fwd_code_end:");
}

extern "C" void kernel_launch(void* const* d_in, const int* in_sizes, int n_in, void* d_out, int out_size, void* d_ws, size_t ws_size, hipStream_t stream) {
  (void)in_sizes; (void)n_in; (void)out_size; (void)d_ws; (void)ws_size;
  (void)hipFuncSetAttribute((const void*)attn_fwd, hipFuncAttributeMaxDynamicSharedMemorySize, (int)LDS_BYTES);
  hipLaunchKernelGGL(attn_fwd, dim3(NWG), dim3(512), LDS_BYTES, stream, d_in[0], d_in[1], d_in[2], d_out);
}
```

```cpp
#include <hip/hip_runtime.h>
#include <hip/hip_bf16.h>
#include <utility>

#pragma clang diagnostic ignored "-Wint-to-pointer-cast"

typedef float    f32x16 __attribute__((ext_vector_type(16)));
typedef float    f32x2  __attribute__((ext_vector_type(2)));
typedef __bf16   bf16x8 __attribute__((ext_vector_type(8)));
typedef __bf16   bf16x2 __attribute__((ext_vector_type(2)));
typedef short    s16x4  __attribute__((ext_vector_type(4)));
typedef short    s16x8  __attribute__((ext_vector_type(8)));
typedef unsigned u32x4  __attribute__((ext_vector_type(4)));
typedef unsigned u32x2  __attribute__((ext_vector_type(2)));
typedef __attribute__((address_space(3))) const bf16x8* lds_b8p;
typedef __attribute__((address_space(3))) s16x4* lds_s4p;
typedef __attribute__((address_space(3))) u32x2* lds_u2p;
typedef __attribute__((address_space(3))) const u32x4* lds_u4p;

#define DI __device__ __forceinline__
#define GAP() __builtin_amdgcn_sched_barrier(0)
#define PIN(x) asm volatile("" : "+v"(x))

constexpr unsigned HEADB = 65536u;
constexpr unsigned RQO = 0u, RING = 65536u, LDS_BYTES = 163840u;
constexpr int NHEADS = 512, NWG = 256;
constexpr float CEXP = 0.08838834764831845f * 1.4426950408889634f;
constexpr float LIM_HI = 1073741824.0f, LIM_LO = 1.0f / LIM_HI;
constexpr int STORE_AUX = 18;
constexpr int BOFF = 8;
constexpr int PFK = 4, PFV = 3;
constexpr int QK0 = 15;
constexpr int NQK0 = 32, PVEND0 = 58;
constexpr int NQK1 = 44, PVEND1 = 27;
constexpr int RAMP = 12;

DI unsigned cvtpk(float lo, float hi) { f32x2 v = {lo, hi}; bf16x2 b = __builtin_convertvector(v, bf16x2); return __builtin_bit_cast(unsigned, b); }
DI float fmax2(float a, float b) { return __builtin_elementwise_maximum(a, b); }
DI float fmax3(float a, float b, float c) { return __builtin_elementwise_maximum(__builtin_elementwise_maximum(a, b), c); }
DI unsigned opaque(unsigned x) { asm volatile("" : "+v"(x)); return x; }
DI float xhalf_max(float m) { auto rr = __builtin_amdgcn_permlane32_swap(__float_as_uint(m), __float_as_uint(m), false, false); return fmax2(__uint_as_float(rr[0]), __uint_as_float(rr[1])); }
DI float xhalf_sum(float m) { auto rr = __builtin_amdgcn_permlane32_swap(__float_as_uint(m), __float_as_uint(m), false, false); return __uint_as_float(rr[0]) + __uint_as_float(rr[1]); }

template <int IMM> DI void dma1(unsigned m0v, unsigned v, __amdgpu_buffer_rsrc_t srd, unsigned soff) {
  asm volatile("s_mov_b32 m0, %0\n\ts_nop 0\n\tbuffer_load_dwordx4 %1, %2, %3 offen offset:%4 nt lds" :: "s"(m0v), "v"(v), "s"(srd), "s"(soff), "i"(IMM) : "memory");
}
template <int SEL, int IMM> DI void dma1_if(unsigned wid, unsigned m0v, unsigned v, __amdgpu_buffer_rsrc_t srd, unsigned soff) {
  asm volatile("s_cmp_eq_u32 %0, %5\n\ts_cbranch_scc0 1f\n\ts_mov_b32 m0, %1\n\ts_nop 0\n\t"
               "buffer_load_dwordx4 %2, %3, %4 offen offset:%6 nt lds\n1:"
               :: "s"(wid), "s"(m0v), "v"(v), "s"(srd), "s"(soff), "i"(SEL), "i"(IMM) : "memory", "scc");
}
template <int N> DI void wait_bar() { asm volatile("s_waitcnt vmcnt(%0)\n\ts_barrier" :: "i"(N) : "memory"); }
template <int N> DI void wait_vm() { asm volatile("s_waitcnt vmcnt(%0)" :: "i"(N) : "memory"); }

struct Ctx {
  __amdgpu_buffer_rsrc_t sq, sk, sv, so;
  const char *pq, *pk, *pv;
  unsigned wid, lane;
  unsigned vs[4], vkv[2];
  unsigned hA, hB;
};
constexpr unsigned slot_of(int i) { return RING + 16384u * (unsigned)(i % 6); }

struct Ev { int pos, len, kind, idx, half; };
#define U2(p0, p1, k, i) {p0, 0, k, i, 0}, {p1, 0, k, i, 1}
#define W(p, l, k, i, h) {p, l, k, i, h}
#define S1(p, i) {p, 0, 2, i, 0}
constexpr Ev EV[] = {
  U2(0, 0, 0, 2), W(1, 8, 0, 3, 0), W(1, 8, 0, 3, 1), W(9, 16, 0, 4, 0), W(9, 16, 0, 4, 1), W(9, 16, 0, 5, 0), W(25, 16, 0, 5, 1), W(25, 16, 0, 6, 0), W(25, 16, 0, 6, 1),
  W(41, 16, 0, 7, 0), W(41, 16, 0, 7, 1), W(41, 16, 1, 0, 0), W(57, 16, 1, 0, 1), W(57, 16, 1, 1, 0), W(57, 16, 1, 1, 1), W(73, 16, 1, 2, 0), W(73, 16, 1, 2, 1), W(73, 16, 1, 3, 0),
  W(89, 16, 1, 3, 1), W(89, 16, 0, 8, 0), W(89, 16, 0, 8, 1), W(105, 16, 0, 9, 0), W(105, 16, 0, 9, 1), W(105, 16, 0, 10, 0), W(121, 8, 0, 10, 1), {999, 0, 0, 11, 0},
  W(1001, 8, 0, 11, 1), W(1001, 8, 0, 12, 0), W(1009, 16, 0, 12, 1), W(1009, 16, 0, 13, 0), W(1009, 16, 0, 13, 1),
  W(1025, 16, 0, 14, 0), W(1025, 16, 0, 14, 1), W(1041, 16, 0, 15, 0), W(1041, 16, 0, 15, 1),
  S1(1066, 0), S1(1070, 1), S1(1074, 2), S1(1078, 3), S1(1082, 4), S1(1086, 5), S1(1090, 6), S1(1094, 7)};
constexpr int NEV = sizeof(EV) / sizeof(EV[0]);
constexpr int younger(int kind, int idx, int pos) {
  int e0 = -1;
  for (int e = 0; e < NEV; ++e) if (EV[e].kind == kind && EV[e].idx == idx) e0 = e;
  int n = 0;
  if (e0 < 0) n = (kind == 0 && idx == 0) ? 2 : 0;
  for (int e = e0 + 1; e < NEV; ++e) if (EV[e].pos < pos) ++n;
  return n;
}
constexpr int bar_tile(int h, int g) {
  if ((g + BOFF) % 16 != 0 || g + BOFF < 16 || g + BOFF > 112) return -1;
  const int j = (g + BOFF) / 16;
  if (h == 1) return j <= 3 ? 8 + j : (j == 4 ? 13 : (j == 5 ? 15 : -1));
  return j;
}
constexpr bool ev_ok() {
  for (int e = 0; e < NEV; ++e) {
    if (e > 0 && EV[e].pos < EV[e - 1].pos) return false;
    const int h = EV[e].pos >= 1000, g0 = EV[e].pos - 1000 * h - 1, g1 = g0 + (EV[e].len > 0 ? EV[e].len - 1 : 0);
    if (EV[e].len > 0 && (g0 + BOFF) / 16 != (g1 + BOFF) / 16) return false;
    if (EV[e].kind != 0) continue;
    const int i = EV[e].idx;
    bool raw = false;
    if ((i & 7) == 0) raw = EV[e].pos + EV[e].len <= 1000 * (i >> 3) + 1;
    for (int g = 0; g < 128; ++g) { const int t = bar_tile(i >> 3, g); if (t >= i && t <= i + 1 && EV[e].pos + EV[e].len <= 1000 * (i >> 3) + g + 1 && (t == i || bar_tile(i >> 3, g - 16) < i)) raw = true; }
    if (!raw) return false;
    if (i >= 6) {
      const int o = i - 6, last = 1000 * (o >> 3) + 16 * (o & 7) + 15 + 1;
      bool war = (o < 8 && EV[e].pos > 1000);
      for (int hh = 0; hh < 2; ++hh) for (int g = 0; g < 128; ++g) if (bar_tile(hh, g) >= 0 && 1000 * hh + g + 1 > last && 1000 * hh + g + 1 <= EV[e].pos) war = true;
      if (!war) return false;
    }
  }
  return true;
}
static_assert(ev_ok(), "vector-memory program: order, windows, landing and slot reuse");
constexpr int win_units(int e) { int n = 0; for (int x = 0; x < NEV; ++x) if (EV[x].pos == EV[e].pos && EV[x].len == EV[e].len) ++n; return n; }
constexpr int win_rank(int e) { int n = 0; for (int x = 0; x < e; ++x) if (EV[x].pos == EV[e].pos && EV[x].len == EV[e].len) ++n; return n; }

DI unsigned row_base(unsigned lane) { const unsigned l32 = lane & 31u, h = lane >> 5, f = ((l32 & 3u) << 2) | ((l32 >> 2) & 3u); return l32 * 256u + 16u * (f ^ h); }
DI void read_q(const Ctx& c, bf16x8 (&q)[8]) {
  const unsigned b = row_base(opaque(c.lane)) + RQO + c.wid * 8192u;
#pragma unroll
  for (int d0 = 0; d0 < 8; ++d0) q[d0] = *(lds_b8p)(b ^ (32u * d0));
}
template <int I> DI void store_rows(const Ctx& c, unsigned hoff) {
  const unsigned lane = opaque(c.lane), rr = lane >> 4, cc = lane & 15u;
  const u32x4 w = *(lds_u4p)(((RQO + c.wid * 8192u + rr * 256u + 16u * (cc ^ rr)) ^ (64u * (I & 1))) + 1024u * I);
  __builtin_amdgcn_raw_buffer_store_b128(w, c.so, hoff + c.wid * 8192u + lane * 16u + 1024u * I, 0, STORE_AUX);
}
template <int HF, int I> DI void store_half(const Ctx& c, unsigned hoff) {
  const unsigned lane = opaque(c.lane), rr = lane >> 3, cc = lane & 7u;
  const u32x4 w = *(lds_u4p)(RQO + c.wid * 8192u + rr * 256u + 16u * (cc ^ rr) + 128u * HF + 2048u * I);
  __builtin_amdgcn_raw_buffer_store_b128(w, c.so, hoff + c.wid * 8192u + rr * 256u + cc * 16u + 128u * HF + 2048u * I, 0, STORE_AUX);
}
template <int KIND, int X, int I, int WV> DI void load_piece(const Ctx& c) {
  if constexpr (KIND == 0) {
    const unsigned m0v = slot_of(X) + c.wid * 2048u, so = ((X & 8) ? c.hB : c.hA) + c.wid * 8192u + 2048u * (X & 3);
    if constexpr (WV < 0) dma1<1024 * I>(m0v, c.vkv[I], (X & 4) ? c.sv : c.sk, so); else dma1_if<WV, 1024 * I>(c.wid, m0v, c.vkv[I], (X & 4) ? c.sv : c.sk, so);
  } else {
    const unsigned m0v = RQO + c.wid * 8192u + 2048u * X, so = (KIND == 1 ? c.hB : c.hA) + c.wid * 8192u + 2048u * X;
    if constexpr (WV < 0) dma1<1024 * I>(m0v, c.vs[(2 * X + I) & 3], c.sq, so); else dma1_if<WV, 1024 * I>(c.wid, m0v, c.vs[(2 * X + I) & 3], c.sq, so);
  }
}
template <int E, int WV> DI void run_event(const Ctx& c) {
  if constexpr (EV[E].kind == 2) store_rows<EV[E].idx>(c, c.hA); else load_piece<EV[E].kind, EV[E].idx, EV[E].half, WV>(c);
}
template <int POS, int E, int... Ws> DI void window_step(const Ctx& c, std::integer_sequence<int, Ws...>) {
  constexpr int t = POS - EV[E].pos, k = win_units(E), r = win_rank(E), L = EV[E].len;
  ([&]() { if constexpr (((8 * r + Ws) * L) / (8 * k) == t) run_event<E, Ws>(c); }(), ...);
}
template <int POS, int E> DI void event_step(const Ctx& c) {
  if constexpr (EV[E].len == 0) { if constexpr (EV[E].pos == POS) run_event<E, -1>(c); }
  else if constexpr (POS >= EV[E].pos && POS < EV[E].pos + EV[E].len) window_step<POS, E>(c, std::make_integer_sequence<int, 8>{});
}
template <int POS, int... Es> DI void events_at_(const Ctx& c, std::integer_sequence<int, Es...>) { (event_step<POS, Es>(c), ...); }
template <int POS> DI void events_at(const Ctx& c) { events_at_<POS>(c, std::make_integer_sequence<int, NEV>{}); }

struct Hd {
  f32x16 S[8], o[4];
  bf16x8 kf[64];
  s16x4 vlo[64], vhi[64];
  u32x4 P[16];
  unsigned ka[8], va[8];
  float mx[2], l[2], mc;
  float x[64][2], e[64][2];
  float lt, il; unsigned ow;
};
struct Prev { u32x2 w[16]; unsigned ow; };

template <int H, int G> DI void k_read(Hd& s) {
  constexpr int I = 8 * H + (G >> 4);
  s.kf[G] = *(lds_b8p)(s.ka[G & 7] + 16384u * ((I % 6) % 3) + 8192u * ((G >> 3) & 1));
}
template <int H> constexpr int pv_ks(int gp) { return H ? (gp & 31) >> 1 : gp >> 2; }
template <int H> constexpr int pv_db(int gp) { return H ? 2 * (gp >> 5) + (gp & 1) : gp & 3; }
template <int H> constexpr int pv_tile(int gp) { return 8 * H + 4 + (pv_ks<H>(gp) >> 2); }
template <int H, int GP> DI void v_read(Hd& s) {
  constexpr int I = pv_tile<H>(GP), ks = pv_ks<H>(GP) & 3, db = pv_db<H>(GP);
  s.vlo[GP] = __builtin_amdgcn_ds_read_tr16_b64_v4i16((lds_s4p)(s.va[2 * db] + 16384u * ((I % 6) % 3) + 4096u * ks));
  s.vhi[GP] = __builtin_amdgcn_ds_read_tr16_b64_v4i16((lds_s4p)(s.va[2 * db + 1] + 16384u * ((I % 6) % 3) + 4096u * ks + 2048u));
}
template <int IPREV, int INEXT> DI void rebase(unsigned (&a)[8]) {
  if constexpr ((IPREV % 6) / 3 != (INEXT % 6) / 3) {
#pragma unroll
    for (int i = 0; i < 8; ++i) a[i] = opaque((INEXT % 6) >= 3 ? a[i] + 49152u : a[i] - 49152u);
  }
}
DI void va_init(const Ctx& c, Hd& s, unsigned base) {
  const unsigned lane = opaque(c.lane), h = lane >> 5, blk = (lane >> 4) & 1u, q4 = (lane & 15u) >> 2, p = lane & 3u;
  const unsigned vb = 256u * (4u * h + q4) + 16u * (4u * q4 + ((2u * blk + (p >> 1)) ^ h)) + 8u * (p & 1u);
#pragma unroll
  for (int i = 0; i < 8; ++i) s.va[i] = opaque((vb ^ (64u * (i >> 1)) ^ (32u * (i & 1))) + base);
}

constexpr int op_n(int k) { const int j = k / 7, p = k % 7; return (p == 0 || p == 2) ? j : ((p == 1 || p == 3) ? j - 1 : j - 2); }
constexpr bool op_on(int k) { const int n = op_n(k); return n >= 0 && n < 64; }
constexpr int op_cost(int k) { const int p = k % 7; return !op_on(k) ? 0 : ((p == 1 || p == 3) ? 2 : 1); }
constexpr int NOPS = 66 * 7;
struct OpTab { short first[130]; };
constexpr OpTab make_optab(int nqk, int pvend) {
  OpTab t{};
  int tot = 0;
  for (int i = 0; i < NOPS; ++i) tot += op_cost(i);
  const int c1 = tot * nqk / 64;
  int c = 0, g = 0;
  for (int k = 0; k < NOPS; ++k) {
    const int gk = c < c1 ? QK0 + c * (64 - QK0) / c1 : 64 + (c - c1) * (pvend + 1) / (tot - c1);
    while (g <= gk) t.first[g++] = (short)k;
    c += op_cost(k);
  }
  while (g < 130) t.first[g++] = (short)NOPS;
  return t;
}
constexpr OpTab OPT0 = make_optab(NQK0, PVEND0), OPT1 = make_optab(NQK1, PVEND1);
template <int H> constexpr int op_first(int g) { return H ? OPT1.first[g] : OPT0.first[g]; }
template <int K> DI void micro_op(Hd& s) {
  if constexpr (op_on(K)) {
    constexpr int n = op_n(K), p = K % 7, ks = n >> 2, kb = ks >> 1, r = 8 * (ks & 1) + 2 * (n & 3);
    if constexpr (p == 0 || p == 2) { s.x[n][p >> 1] = __builtin_fmaf(s.S[kb][r + (p >> 1)], CEXP, s.mc); PIN(s.x[n][p >> 1]); }
    else if constexpr (p == 1 || p == 3) { s.e[n][p >> 1] = __builtin_amdgcn_exp2f(s.x[n][p >> 1]); PIN(s.e[n][p >> 1]); }
    else if constexpr (p == 4 || p == 5) { if constexpr (n == 0) s.l[p - 4] = s.e[n][p - 4]; else s.l[p - 4] += s.e[n][p - 4]; PIN(s.l[p - 4]); }
    else { unsigned w = cvtpk(s.e[n][0], s.e[n][1]); PIN(w); s.P[ks][n & 3] = w; }
  }
}
template <int K0, int... Ks> DI void micro_ops(Hd& s, std::integer_sequence<int, Ks...>) { (micro_op<K0 + Ks>(s), ...); }

template <int I, bool SCALAR> DI void pack_item(const f32x16 (&o)[4], float il, unsigned ow) {
  constexpr int db = I >> 2, g = I & 3;
  float a = o[db][4 * g] * il, b = o[db][4 * g + 1] * il, c = o[db][4 * g + 2] * il, d = o[db][4 * g + 3] * il;
  if constexpr (SCALAR) { PIN(a); PIN(b); PIN(c); PIN(d); }
  const u32x2 w = {cvtpk(a, b), cvtpk(c, d)};
  *(lds_u2p)(ow ^ (16u * I)) = w;
}
template <int I0, int... Is> DI void pack_items(const f32x16 (&o)[4], float il, unsigned ow, std::integer_sequence<int, Is...>) { (pack_item<I0 + Is, false>(o, il, ow), ...); }
DI unsigned stage_base(const Ctx& c) { const unsigned lane = opaque(c.lane), l32 = lane & 31u, h = lane >> 5; return RQO + c.wid * 8192u + l32 * 256u + 16u * (l32 & 7u) + 8u * h; }

template <int H, int G> DI void gap(const Ctx& c, Hd& s, const bf16x8 (&q)[8], const Prev& pv) {
  constexpr int R = 8 * H;
  if constexpr (bar_tile(H, G) >= 0) wait_bar<younger(0, bar_tile(H, G), 1000 * H + G + 1)>();
  if constexpr (((G + BOFF) % 16) % 4 == 0) __builtin_amdgcn_s_setprio(3 - ((G + BOFF) % 16) / 4);
  events_at<1000 * H + G + 1>(c);
  if constexpr (G < 64) {
    if constexpr (G + PFK < 64) {
      if constexpr (((G + PFK) & 15) == 0) rebase<R + ((G + PFK) >> 4) - 1, R + ((G + PFK) >> 4)>(s.ka);
      k_read<H, G + PFK>(s);
    }
    if constexpr (G == 60) va_init(c, s, RING + (((R + 4) % 6) >= 3 ? 49152u : 0u));
    if constexpr (G + PFV >= 64) v_read<H, G + PFV - 64>(s);
    constexpr int kb = G >> 3, d0 = G & 7;
    if constexpr (d0 == 0) s.S[kb] = __builtin_amdgcn_mfma_f32_32x32x16_bf16(s.kf[G], q[d0], f32x16{}, 0, 0, 0);
    else                   s.S[kb] = __builtin_amdgcn_mfma_f32_32x32x16_bf16(s.kf[G], q[d0], s.S[kb], 0, 0, 0);
    PIN(s.S[kb]);
    if constexpr (G == 10) { s.mx[0] = fmax3(s.S[0][0], s.S[0][1], s.S[0][2]); s.mx[1] = fmax3(s.S[0][3], s.S[0][4], s.S[0][5]); PIN(s.mx[0]); PIN(s.mx[1]); }
    if constexpr (G == 11) { s.mx[0] = fmax3(s.mx[0], s.S[0][6], s.S[0][7]); s.mx[1] = fmax3(s.mx[1], s.S[0][8], s.S[0][9]); PIN(s.mx[0]); PIN(s.mx[1]); }
    if constexpr (G == 12) { s.mx[0] = fmax3(s.mx[0], s.S[0][10], s.S[0][11]); s.mx[1] = fmax3(s.mx[1], s.S[0][12], s.S[0][13]); PIN(s.mx[0]); PIN(s.mx[1]); }
    if constexpr (G == 13) { s.mx[0] = fmax3(s.mx[0], s.S[0][14], s.S[0][15]); s.mx[0] = fmax2(s.mx[0], s.mx[1]); PIN(s.mx[0]); }
    if constexpr (G == 14) { s.mc = -xhalf_max(s.mx[0]) * CEXP; PIN(s.mc); }
    if constexpr (H == 1 && G < 16) *(lds_u2p)(pv.ow ^ (16u * G)) = pv.w[G];
  } else {
    constexpr int GP = G - 64, ks = pv_ks<H>(GP), db = pv_db<H>(GP);
    if constexpr (GP + PFV < 64) {
      rebase<pv_tile<H>(GP + PFV - 1), pv_tile<H>(GP + PFV)>(s.va);
      v_read<H, GP + PFV>(s);
    }
    const s16x8 vf = __builtin_shufflevector(s.vlo[GP], s.vhi[GP], 0, 1, 2, 3, 4, 5, 6, 7);
    if constexpr (ks == 0) s.o[db] = __builtin_amdgcn_mfma_f32_32x32x16_bf16(__builtin_bit_cast(bf16x8, vf), __builtin_bit_cast(bf16x8, s.P[0]), f32x16{}, 0, 0, 0);
    else                   s.o[db] = __builtin_amdgcn_mfma_f32_32x32x16_bf16(__builtin_bit_cast(bf16x8, vf), __builtin_bit_cast(bf16x8, s.P[ks]), s.o[db], 0, 0, 0);
    PIN(s.o[db]);
  }
  if constexpr (H == 1) {
    constexpr int GL = 64 + PVEND1 + 2;
    if constexpr (G == GL) { s.lt = xhalf_sum(s.l[0] + s.l[1]); PIN(s.lt); }
    if constexpr (G == GL + 1) { s.il = __builtin_amdgcn_rcpf(s.lt); PIN(s.il); s.ow = stage_base(c); }
    if constexpr (G >= 99 && G < 115 && ((G - 99) & 1) == 0) pack_item<(G - 99) / 2, true>(s.o, s.il, s.ow);
    if constexpr (G >= 116 && G < 128 && (G - 116) % 3 == 0) store_half<0, (G - 116) / 3>(c, c.hB);
  }
  if constexpr (H == 0 && G < 48) asm volatile("s_nop %0" :: "i"((G < 16 ? RAMP : (G < 32 ? RAMP / 2 : RAMP / 4)) - 1));
  micro_ops<op_first<H>(G)>(s, std::make_integer_sequence<int, op_first<H>(G + 1) - op_first<H>(G)>{});
  GAP();
}
template <int H, int... Gs> DI void gaps(const Ctx& c, Hd& s, const bf16x8 (&q)[8], const Prev& pv, std::integer_sequence<int, Gs...>) { (gap<H, Gs>(c, s, q, pv), ...); }

DI void fallback(const Ctx& c, unsigned hoff, f32x16 (&o)[4], float& l) {
  const unsigned lane = c.lane, l32 = lane & 31u, h = lane >> 5;
  const char* qp = c.pq + hoff + (c.wid * 32u + l32) * 256u + h * 16u;
  const char* kp = c.pk + hoff + l32 * 256u + h * 16u;
  const short* vp = (const short*)(c.pv + hoff) + l32;
  bf16x8 qf[8];
#pragma unroll
  for (int d = 0; d < 8; ++d) qf[d] = *(const bf16x8*)(qp + 32 * d);
  float m = -__builtin_inff();
#pragma nounroll
  for (int kb = 0; kb < 8; ++kb) {
    f32x16 a = {};
#pragma unroll
    for (int d = 0; d < 8; ++d) a = __builtin_amdgcn_mfma_f32_32x32x16_bf16(*(const bf16x8*)(kp + kb * 8192 + 32 * d), qf[d], a, 0, 0, 0);
#pragma unroll
    for (int r = 0; r < 16; ++r) m = fmax2(m, a[r]);
  }
  m = xhalf_max(m);
  float ls = 0.f;
#pragma unroll
  for (int db = 0; db < 4; ++db) o[db] = f32x16{};
#pragma nounroll
  for (int kb = 0; kb < 8; ++kb) {
    f32x16 a = {};
#pragma unroll
    for (int d = 0; d < 8; ++d) a = __builtin_amdgcn_mfma_f32_32x32x16_bf16(*(const bf16x8*)(kp + kb * 8192 + 32 * d), qf[d], a, 0, 0, 0);
#pragma unroll
    for (int sx = 0; sx < 2; ++sx) {
      float e[8];
#pragma unroll
      for (int j = 0; j < 8; ++j) { e[j] = __builtin_amdgcn_exp2f((a[8 * sx + j] - m) * CEXP); ls += e[j]; }
      const u32x4 p = {cvtpk(e[0], e[1]), cvtpk(e[2], e[3]), cvtpk(e[4], e[5]), cvtpk(e[6], e[7])};
#pragma unroll
      for (int db = 0; db < 4; ++db) {
        s16x8 vf;
#pragma unroll
        for (int j = 0; j < 8; ++j) vf[j] = vp[(kb * 32 + 16 * sx + 8 * (j >> 2) + 4 * h + (j & 3)) * 128 + 32 * db];
        o[db] = __builtin_amdgcn_mfma_f32_32x32x16_bf16(__builtin_bit_cast(bf16x8, vf), __builtin_bit_cast(bf16x8, p), o[db], 0, 0, 0);
      }
    }
  }
  l = xhalf_sum(ls);
}
#define UNSAFE_SUM(l) __builtin_expect(__any(!((l) <= LIM_HI && (l) >= LIM_LO)), 0)

template <int H>
DI void head(const Ctx& c, bf16x8 (&q)[8], Prev& pv) {
  constexpr int R = 8 * H;
  Hd s;
  { const unsigned ra = row_base(opaque(c.lane));
#pragma unroll
    for (int d0 = 0; d0 < 8; ++d0) s.ka[d0] = opaque((ra ^ (32u * d0)) + RING + ((R % 6) >= 3 ? 49152u : 0u)); }
  events_at<1000 * H>(c);
  GAP();
  wait_bar<younger(0, R, 1000 * H + 1)>();
  GAP();
  k_read<H, 0>(s); k_read<H, 1>(s); k_read<H, 2>(s); k_read<H, 3>(s);
  GAP();
  gaps<H>(c, s, q, pv, std::make_integer_sequence<int, 128>{});
  if constexpr (H == 0) {
    float l = xhalf_sum(s.l[0] + s.l[1]);
    if (UNSAFE_SUM(l)) fallback(c, c.hA, s.o, l);
    events_at<999>(c);
    GAP();
    const float il = __builtin_amdgcn_rcpf(l);
#pragma unroll
    for (int i = 0; i < 16; ++i) {
      const f32x16& o = s.o[i >> 2]; const int g = 4 * (i & 3);
      pv.w[i] = u32x2{cvtpk(o[g] * il, o[g + 1] * il), cvtpk(o[g + 2] * il, o[g + 3] * il)};
      PIN(pv.w[i]);
    }
    pv.ow = stage_base(c);
    GAP();
    wait_vm<younger(1, 3, 1000)>();
    GAP();
    read_q(c, q);
  } else {
    float l = s.lt;
    if (UNSAFE_SUM(l)) {
      wait_vm<0>();
      fallback(c, c.hB, s.o, l);
      s.il = __builtin_amdgcn_rcpf(l);
      pack_items<0>(s.o, s.il, s.ow, std::make_integer_sequence<int, 8>{});
      store_half<0, 0>(c, c.hB); store_half<0, 1>(c, c.hB); store_half<0, 2>(c, c.hB); store_half<0, 3>(c, c.hB);
    }
    pack_items<8>(s.o, s.il, s.ow, std::make_integer_sequence<int, 8>{});
    store_half<1, 0>(c, c.hB); store_half<1, 1>(c, c.hB); store_half<1, 2>(c, c.hB); store_half<1, 3>(c, c.hB);
  }
  GAP();
}

__global__ __launch_bounds__(512, 2) void attn_fwd(const void* __restrict__ Q, const void* __restrict__ K, const void* __restrict__ V, void* __restrict__ O) {
  const unsigned tid = threadIdx.x, lane = tid & 63u;
  {
    unsigned long long pc; unsigned off, t, m;
    asm volatile("attn_fwd_code_begin:\n\t"
                 "s_getpc_b64 %0\n\t"
                 "v_lshlrev_b32 %1, 7, %4\n\t"
                 "v_lshrrev_b32 %2, 6, %4\n\t"
                 "s_mov_b32 %3, attn_fwd_code_end-attn_fwd_code_begin\n\t"
                 "v_cmp_gt_u32 vcc, %3, %1\n\t"
                 "v_readfirstlane_b32 %3, %2\n\t"
                 "s_nop 3\n\t"
                 "v_cndmask_b32 %1, 0, %1, vcc\n\t"
                 "s_lshl_b32 %3, %3, 8\n\t"
                 "s_add_u32 %3, %3, %5\n\t"
                 "s_mov_b32 m0, %3\n\t"
                 "s_nop 0\n\t"
                 "global_load_lds_dword %1, %0"
                 : "=&s"(pc), "=&v"(off), "=&v"(t), "=&s"(m) : "v"(tid), "i"(slot_of(5)) : "memory", "vcc", "scc");
    GAP();
  }
  Ctx c;
  c.lane = lane;
  c.wid = __builtin_amdgcn_readfirstlane(tid >> 6);
  const int nbytes = NHEADS * (int)HEADB;
  c.sq = __builtin_amdgcn_make_buffer_rsrc((void*)Q, (short)0, nbytes, 0x00020000);
  c.sk = __builtin_amdgcn_make_buffer_rsrc((void*)K, (short)0, nbytes, 0x00020000);
  c.sv = __builtin_amdgcn_make_buffer_rsrc((void*)V, (short)0, nbytes, 0x00020000);
  c.so = __builtin_amdgcn_make_buffer_rsrc((void*)O, (short)0, nbytes, 0x00020000);
  c.pq = (const char*)Q; c.pk = (const char*)K; c.pv = (const char*)V;
  {
    const unsigned r = lane >> 4, p = lane & 15u, v0 = r * 256u + 16u * (p ^ (r << 2));
#pragma unroll
    for (int j = 0; j < 4; ++j) c.vs[j] = opaque(v0 ^ (16u * j));
    const unsigned w2 = (32u * c.wid) & 48u;
    c.vkv[0] = opaque(v0 ^ w2); c.vkv[1] = opaque(v0 ^ (w2 + 16u));
  }
  { const unsigned b = blockIdx.x, h0 = (b & 7u) * 64u + (b >> 3) * 2u;
    c.hA = h0 * HEADB; c.hB = (h0 + 1u) * HEADB; }
  asm volatile("s_nop 4");
  load_piece<4, 0, 0, -1>(c); load_piece<4, 0, 1, -1>(c); load_piece<4, 1, 0, -1>(c); load_piece<4, 1, 1, -1>(c);
  load_piece<4, 2, 0, -1>(c); load_piece<4, 2, 1, -1>(c); load_piece<4, 3, 0, -1>(c); load_piece<4, 3, 1, -1>(c);
  load_piece<0, 0, 0, -1>(c); load_piece<0, 0, 1, -1>(c); load_piece<0, 1, 0, -1>(c); load_piece<0, 1, 1, -1>(c);
  bf16x8 q[8];
  GAP();
  wait_vm<4>();
  GAP();
  read_q(c, q);
  Prev pv;
  head<0>(c, q, pv);
  head<1>(c, q, pv);
  asm volatile("attn_fwd_code_end:");
}

extern "C" void kernel_launch(void* const* d_in, const int* in_sizes, int n_in, void* d_out, int out_size, void* d_ws, size_t ws_size, hipStream_t stream) {
  (void)in_sizes; (void)n_in; (void)out_size; (void)d_ws; (void)ws_size;
  (void)hipFuncSetAttribute((const void*)attn_fwd, hipFuncAttributeMaxDynamicSharedMemorySize, (int)LDS_BYTES);
  hipLaunchKernelGGL(attn_fwd, dim3(NWG), dim3(512), LDS_BYTES, stream, d_in[0], d_in[1], d_in[2], d_out);
}
```

```cpp
#include <hip/hip_runtime.h>
#include <hip/hip_bf16.h>
#include <utility>

#pragma clang diagnostic ignored "-Wint-to-pointer-cast"

typedef float    f32x16 __attribute__((ext_vector_type(16)));
typedef float    f32x2  __attribute__((ext_vector_type(2)));
typedef __bf16   bf16x8 __attribute__((ext_vector_type(8)));
typedef __bf16   bf16x2 __attribute__((ext_vector_type(2)));
typedef short    s16x4  __attribute__((ext_vector_type(4)));
typedef short    s16x8  __attribute__((ext_vector_type(8)));
typedef unsigned u32x4  __attribute__((ext_vector_type(4)));
typedef unsigned u32x2  __attribute__((ext_vector_type(2)));
typedef __attribute__((address_space(3))) const bf16x8* lds_b8p;
typedef __attribute__((address_space(3))) s16x4* lds_s4p;
typedef __attribute__((address_space(3))) u32x2* lds_u2p;
typedef __attribute__((address_space(3))) const u32x4* lds_u4p;

#define DI __device__ __forceinline__
#define GAP() __builtin_amdgcn_sched_barrier(0)
#define PIN(x) asm volatile("" : "+v"(x))

constexpr unsigned HEADB = 65536u;
constexpr unsigned RQO = 0u, RING = 65536u, LDS_BYTES = 163840u;
constexpr int NHEADS = 512, NWG = 256;
constexpr float CEXP = 0.08838834764831845f * 1.4426950408889634f;
constexpr float LIM_HI = 1073741824.0f, LIM_LO = 1.0f / LIM_HI;
constexpr int STORE_AUX = 18;
constexpr int BOFF = 8;
constexpr int PFK = 4, PFV = 3;
constexpr int QK0 = 15;
constexpr int NQK0 = 32, PVEND0 = 58;
constexpr int NQK1 = 44, PVEND1 = 27;
constexpr int RAMP = 12;

DI unsigned cvtpk(float lo, float hi) { f32x2 v = {lo, hi}; bf16x2 b = __builtin_convertvector(v, bf16x2); return __builtin_bit_cast(unsigned, b); }
DI float fmax2(float a, float b) { return __builtin_elementwise_maximum(a, b); }
DI float fmax3(float a, float b, float c) { return __builtin_elementwise_maximum(__builtin_elementwise_maximum(a, b), c); }
DI unsigned opaque(unsigned x) { asm volatile("" : "+v"(x)); return x; }
DI float xhalf_max(float m) { auto rr = __builtin_amdgcn_permlane32_swap(__float_as_uint(m), __float_as_uint(m), false, false); return fmax2(__uint_as_float(rr[0]), __uint_as_float(rr[1])); }
DI float xhalf_sum(float m) { auto rr = __builtin_amdgcn_permlane32_swap(__float_as_uint(m), __float_as_uint(m), false, false); return __uint_as_float(rr[0]) + __uint_as_float(rr[1]); }

template <int IMM> DI void dma1(unsigned m0v, unsigned v, __amdgpu_buffer_rsrc_t srd, unsigned soff) {
  asm volatile("s_mov_b32 m0, %0\n\ts_nop 0\n\tbuffer_load_dwordx4 %1, %2, %3 offen offset:%4 nt lds" :: "s"(m0v), "v"(v), "s"(srd), "s"(soff), "i"(IMM) : "memory");
}
template <int SEL, int IMM> DI void dma1_if(unsigned wid, unsigned m0v, unsigned v, __amdgpu_buffer_rsrc_t srd, unsigned soff) {
  asm volatile("s_cmp_eq_u32 %0, %5\n\ts_cbranch_scc0 1f\n\ts_mov_b32 m0, %1\n\ts_nop 0\n\t"
               "buffer_load_dwordx4 %2, %3, %4 offen offset:%6 nt lds\n1:"
               :: "s"(wid), "s"(m0v), "v"(v), "s"(srd), "s"(soff), "i"(SEL), "i"(IMM) : "memory", "scc");
}
template <int N> DI void wait_bar() { asm volatile("s_waitcnt vmcnt(%0)\n\ts_barrier" :: "i"(N) : "memory"); }
template <int N> DI void wait_vm() { asm volatile("s_waitcnt vmcnt(%0)" :: "i"(N) : "memory"); }

struct Ctx {
  __amdgpu_buffer_rsrc_t sq, sk, sv, so;
  const char *pq, *pk, *pv;
  unsigned wid, lane;
  unsigned vs[4], vkv[2];
  unsigned hA, hB;
};
constexpr unsigned slot_of(int i) { return RING + 16384u * (unsigned)(i % 6); }

struct Ev { int pos, len, kind, idx, half; };
#define U2(p0, p1, k, i) {p0, 0, k, i, 0}, {p1, 0, k, i, 1}
#define W(p, l, k, i, h) {p, l, k, i, h}
#define S1(p, i) {p, 0, 2, i, 0}
constexpr Ev EV[] = {
  U2(0, 0, 0, 2), W(1, 8, 0, 3, 0), W(9, 16, 0, 3, 1), W(9, 16, 0, 4, 0), W(9, 16, 0, 4, 1), W(25, 16, 0, 5, 0), W(25, 16, 0, 5, 1), W(25, 16, 0, 6, 0),
  W(41, 16, 0, 6, 1), W(41, 16, 0, 7, 0), W(41, 16, 0, 7, 1), W(57, 16, 1, 0, 0), W(57, 16, 1, 0, 1), W(57, 16, 1, 1, 0), W(73, 16, 1, 1, 1), W(73, 16, 1, 2, 0), W(73, 16, 1, 2, 1),
  W(89, 16, 1, 3, 0), W(89, 16, 1, 3, 1), W(89, 16, 0, 8, 0), W(105, 16, 0, 8, 1), W(105, 16, 0, 9, 0), W(105, 16, 0, 9, 1), W(121, 8, 0, 10, 0), {999, 0, 0, 10, 1},
  W(1001, 8, 0, 11, 0), W(1001, 8, 0, 11, 1), W(1009, 16, 0, 12, 0), W(1009, 16, 0, 12, 1), W(1009, 16, 0, 13, 0),
  W(1025, 16, 0, 13, 1), W(1025, 16, 0, 14, 0), W(1025, 16, 0, 14, 1), W(1041, 16, 0, 15, 0), W(1041, 16, 0, 15, 1),
  S1(1066, 0), S1(1070, 1), S1(1074, 2), S1(1078, 3), S1(1082, 4), S1(1086, 5), S1(1090, 6), S1(1094, 7)};
constexpr int NEV = sizeof(EV) / sizeof(EV[0]);
constexpr int younger(int kind, int idx, int pos) {
  int e0 = -1;
  for (int e = 0; e < NEV; ++e) if (EV[e].kind == kind && EV[e].idx == idx) e0 = e;
  int n = 0;
  if (e0 < 0) n = (kind == 0 && idx == 0) ? 2 : 0;
  for (int e = e0 + 1; e < NEV; ++e) if (EV[e].pos < pos) ++n;
  return n;
}
constexpr int bar_tile(int h, int g) {
  if ((g + BOFF) % 16 != 0 || g + BOFF < 16 || g + BOFF > 112) return -1;
  const int j = (g + BOFF) / 16;
  if (h == 1) return j <= 3 ? 8 + j : (j == 4 ? 13 : (j == 5 ? 15 : -1));
  return j;
}
constexpr bool ev_ok() {
  for (int e = 0; e < NEV; ++e) {
    if (e > 0 && EV[e].pos < EV[e - 1].pos) return false;
    const int h = EV[e].pos >= 1000, g0 = EV[e].pos - 1000 * h - 1, g1 = g0 + (EV[e].len > 0 ? EV[e].len - 1 : 0);
    if (EV[e].len > 0 && (g0 + BOFF) / 16 != (g1 + BOFF) / 16) return false;
    if (EV[e].kind != 0) continue;
    const int i = EV[e].idx;
    bool raw = false;
    if ((i & 7) == 0) raw = EV[e].pos + EV[e].len <= 1000 * (i >> 3) + 1;
    for (int g = 0; g < 128; ++g) { const int t = bar_tile(i >> 3, g); if (t >= i && t <= i + 1 && EV[e].pos + EV[e].len <= 1000 * (i >> 3) + g + 1 && (t == i || bar_tile(i >> 3, g - 16) < i)) raw = true; }
    if (!raw) return false;
    if (i >= 6) {
      const int o = i - 6, last = 1000 * (o >> 3) + 16 * (o & 7) + 15 + 1;
      bool war = (o < 8 && EV[e].pos > 1000);
      for (int hh = 0; hh < 2; ++hh) for (int g = 0; g < 128; ++g) if (bar_tile(hh, g) >= 0 && 1000 * hh + g + 1 > last && 1000 * hh + g + 1 <= EV[e].pos) war = true;
      if (!war) return false;
    }
  }
  return true;
}
static_assert(ev_ok(), "vector-memory program: order, windows, landing and slot reuse");
constexpr int win_units(int e) { int n = 0; for (int x = 0; x < NEV; ++x) if (EV[x].pos == EV[e].pos && EV[x].len == EV[e].len) ++n; return n; }
constexpr int win_rank(int e) { int n = 0; for (int x = 0; x < e; ++x) if (EV[x].pos == EV[e].pos && EV[x].len == EV[e].len) ++n; return n; }

DI unsigned row_base(unsigned lane) { const unsigned l32 = lane & 31u, h = lane >> 5, f = ((l32 & 3u) << 2) | ((l32 >> 2) & 3u); return l32 * 256u + 16u * (f ^ h); }
DI void read_q(const Ctx& c, bf16x8 (&q)[8]) {
  const unsigned b = row_base(opaque(c.lane)) + RQO + c.wid * 8192u;
#pragma unroll
  for (int d0 = 0; d0 < 8; ++d0) q[d0] = *(lds_b8p)(b ^ (32u * d0));
}
template <int I> DI void store_rows(const Ctx& c, unsigned hoff) {
  const unsigned lane = opaque(c.lane), rr = lane >> 4, cc = lane & 15u;
  const u32x4 w = *(lds_u4p)(((RQO + c.wid * 8192u + rr * 256u + 16u * (cc ^ rr)) ^ (64u * (I & 1))) + 1024u * I);
  __builtin_amdgcn_raw_buffer_store_b128(w, c.so, hoff + c.wid * 8192u + lane * 16u + 1024u * I, 0, STORE_AUX);
}
template <int HF, int I> DI void store_half(const Ctx& c, unsigned hoff) {
  const unsigned lane = opaque(c.lane), rr = lane >> 3, cc = lane & 7u;
  const u32x4 w = *(lds_u4p)(RQO + c.wid * 8192u + rr * 256u + 16u * (cc ^ rr) + 128u * HF + 2048u * I);
  __builtin_amdgcn_raw_buffer_store_b128(w, c.so, hoff + c.wid * 8192u + rr * 256u + cc * 16u + 128u * HF + 2048u * I, 0, STORE_AUX);
}
template <int KIND, int X, int I, int WV> DI void load_piece(const Ctx& c) {
  if constexpr (KIND == 0) {
    const unsigned m0v = slot_of(X) + c.wid * 2048u, so = ((X & 8) ? c.hB : c.hA) + c.wid * 8192u + 2048u * (X & 3);
    if constexpr (WV < 0) dma1<1024 * I>(m0v, c.vkv[I], (X & 4) ? c.sv : c.sk, so); else dma1_if<WV, 1024 * I>(c.wid, m0v, c.vkv[I], (X & 4) ? c.sv : c.sk, so);
  } else {
    const unsigned m0v = RQO + c.wid * 8192u + 2048u * X, so = (KIND == 1 ? c.hB : c.hA) + c.wid * 8192u + 2048u * X;
    if constexpr (WV < 0) dma1<1024 * I>(m0v, c.vs[(2 * X + I) & 3], c.sq, so); else dma1_if<WV, 1024 * I>(c.wid, m0v, c.vs[(2 * X + I) & 3], c.sq, so);
  }
}
template <int E, int WV> DI void run_event(const Ctx& c) {
  if constexpr (EV[E].kind == 2) store_rows<EV[E].idx>(c, c.hA); else load_piece<EV[E].kind, EV[E].idx, EV[E].half, WV>(c);
}
template <int POS, int E, int... Ws> DI void window_step(const Ctx& c, std::integer_sequence<int, Ws...>) {
  constexpr int t = POS - EV[E].pos, k = win_units(E), r = win_rank(E), L = EV[E].len;
  ([&]() { if constexpr (((8 * r + Ws) * L) / (8 * k) == t) run_event<E, Ws>(c); }(), ...);
}
template <int POS, int E> DI void event_step(const Ctx& c) {
  if constexpr (EV[E].len == 0) { if constexpr (EV[E].pos == POS) run_event<E, -1>(c); }
  else if constexpr (POS >= EV[E].pos && POS < EV[E].pos + EV[E].len) window_step<POS, E>(c, std::make_integer_sequence<int, 8>{});
}
template <int POS, int... Es> DI void events_at_(const Ctx& c, std::integer_sequence<int, Es...>) { (event_step<POS, Es>(c), ...); }
template <int POS> DI void events_at(const Ctx& c) { events_at_<POS>(c, std::make_integer_sequence<int, NEV>{}); }

struct Hd {
  f32x16 S[8], o[4];
  bf16x8 kf[64];
  s16x4 vlo[64], vhi[64];
  u32x4 P[16];
  unsigned ka[8], va[8];
  float mx[2], l[2], mc;
  float x[64][2], e[64][2];
  float lt, il; unsigned ow;
};
struct Prev { u32x2 w[16]; unsigned ow; };

template <int H, int G> DI void k_read(Hd& s) {
  constexpr int I = 8 * H + (G >> 4);
  s.kf[G] = *(lds_b8p)(s.ka[G & 7] + 16384u * ((I % 6) % 3) + 8192u * ((G >> 3) & 1));
}
template <int H> constexpr int pv_ks(int gp) { return H ? (gp & 31) >> 1 : gp >> 2; }
template <int H> constexpr int pv_db(int gp) { return H ? 2 * (gp >> 5) + (gp & 1) : gp & 3; }
template <int H> constexpr int pv_tile(int gp) { return 8 * H + 4 + (pv_ks<H>(gp) >> 2); }
template <int H, int GP> DI void v_read(Hd& s) {
  constexpr int I = pv_tile<H>(GP), ks = pv_ks<H>(GP) & 3, db = pv_db<H>(GP);
  s.vlo[GP] = __builtin_amdgcn_ds_read_tr16_b64_v4i16((lds_s4p)(s.va[2 * db] + 16384u * ((I % 6) % 3) + 4096u * ks));
  s.vhi[GP] = __builtin_amdgcn_ds_read_tr16_b64_v4i16((lds_s4p)(s.va[2 * db + 1] + 16384u * ((I % 6) % 3) + 4096u * ks + 2048u));
}
template <int IPREV, int INEXT> DI void rebase(unsigned (&a)[8]) {
  if constexpr ((IPREV % 6) / 3 != (INEXT % 6) / 3) {
#pragma unroll
    for (int i = 0; i < 8; ++i) a[i] = opaque((INEXT % 6) >= 3 ? a[i] + 49152u : a[i] - 49152u);
  }
}
DI void va_init(const Ctx& c, Hd& s, unsigned base) {
  const unsigned lane = opaque(c.lane), h = lane >> 5, blk = (lane >> 4) & 1u, q4 = (lane & 15u) >> 2, p = lane & 3u;
  const unsigned vb = 256u * (4u * h + q4) + 16u * (4u * q4 + ((2u * blk + (p >> 1)) ^ h)) + 8u * (p & 1u);
#pragma unroll
  for (int i = 0; i < 8; ++i) s.va[i] = opaque((vb ^ (64u * (i >> 1)) ^ (32u * (i & 1))) + base);
}

constexpr int op_n(int k) { const int j = k / 7, p = k % 7; return (p == 0 || p == 2) ? j : ((p == 1 || p == 3) ? j - 1 : j - 2); }
constexpr bool op_on(int k) { const int n = op_n(k); return n >= 0 && n < 64; }
constexpr int op_cost(int k) { const int p = k % 7; return !op_on(k) ? 0 : ((p == 1 || p == 3) ? 2 : 1); }
constexpr int NOPS = 66 * 7;
struct OpTab { short first[130]; };
constexpr OpTab make_optab(int nqk, int pvend) {
  OpTab t{};
  int tot = 0;
  for (int i = 0; i < NOPS; ++i) tot += op_cost(i);
  const int c1 = tot * nqk / 64;
  int c = 0, g = 0;
  for (int k = 0; k < NOPS; ++k) {
    const int gk = c < c1 ? QK0 + c * (64 - QK0) / c1 : 64 + (c - c1) * (pvend + 1) / (tot - c1);
    while (g <= gk) t.first[g++] = (short)k;
    c += op_cost(k);
  }
  while (g < 130) t.first[g++] = (short)NOPS;
  return t;
}
constexpr OpTab OPT0 = make_optab(NQK0, PVEND0), OPT1 = make_optab(NQK1, PVEND1);
template <int H> constexpr int op_first(int g) { return H ? OPT1.first[g] : OPT0.first[g]; }
template <int K> DI void micro_op(Hd& s) {
  if constexpr (op_on(K)) {
    constexpr int n = op_n(K), p = K % 7, ks = n >> 2, kb = ks >> 1, r = 8 * (ks & 1) + 2 * (n & 3);
    if constexpr (p == 0 || p == 2) { s.x[n][p >> 1] = __builtin_fmaf(s.S[kb][r + (p >> 1)], CEXP, s.mc); PIN(s.x[n][p >> 1]); }
    else if constexpr (p == 1 || p == 3) { s.e[n][p >> 1] = __builtin_amdgcn_exp2f(s.x[n][p >> 1]); PIN(s.e[n][p >> 1]); }
    else if constexpr (p == 4 || p == 5) { if constexpr (n == 0) s.l[p - 4] = s.e[n][p - 4]; else s.l[p - 4] += s.e[n][p - 4]; PIN(s.l[p - 4]); }
    else { unsigned w = cvtpk(s.e[n][0], s.e[n][1]); PIN(w); s.P[ks][n & 3] = w; }
  }
}
template <int K0, int... Ks> DI void micro_ops(Hd& s, std::integer_sequence<int, Ks...>) { (micro_op<K0 + Ks>(s), ...); }

template <int I, bool SCALAR> DI void pack_item(const f32x16 (&o)[4], float il, unsigned ow) {
  constexpr int db = I >> 2, g = I & 3;
  float a = o[db][4 * g] * il, b = o[db][4 * g + 1] * il, c = o[db][4 * g + 2] * il, d = o[db][4 * g + 3] * il;
  if constexpr (SCALAR) { PIN(a); PIN(b); PIN(c); PIN(d); }
  const u32x2 w = {cvtpk(a, b), cvtpk(c, d)};
  *(lds_u2p)(ow ^ (16u * I)) = w;
}
template <int I0, int... Is> DI void pack_items(const f32x16 (&o)[4], float il, unsigned ow, std::integer_sequence<int, Is...>) { (pack_item<I0 + Is, false>(o, il, ow), ...); }
DI unsigned stage_base(const Ctx& c) { const unsigned lane = opaque(c.lane), l32 = lane & 31u, h = lane >> 5; return RQO + c.wid * 8192u + l32 * 256u + 16u * (l32 & 7u) + 8u * h; }

template <int H, int G> DI void gap(const Ctx& c, Hd& s, const bf16x8 (&q)[8], const Prev& pv) {
  constexpr int R = 8 * H;
  if constexpr (bar_tile(H, G) >= 0) wait_bar<younger(0, bar_tile(H, G), 1000 * H + G + 1)>();
  if constexpr (((G + BOFF) % 16) % 4 == 0) __builtin_amdgcn_s_setprio(3 - ((G + BOFF) % 16) / 4);
  events_at<1000 * H + G + 1>(c);
  if constexpr (G < 64) {
    if constexpr (G + PFK < 64) {
      if constexpr (((G + PFK) & 15) == 0) rebase<R + ((G + PFK) >> 4) - 1, R + ((G + PFK) >> 4)>(s.ka);
      k_read<H, G + PFK>(s);
    }
    if constexpr (G == 60) va_init(c, s, RING + (((R + 4) % 6) >= 3 ? 49152u : 0u));
    if constexpr (G + PFV >= 64) v_read<H, G + PFV - 64>(s);
    constexpr int kb = G >> 3, d0 = G & 7;
    if constexpr (d0 == 0) s.S[kb] = __builtin_amdgcn_mfma_f32_32x32x16_bf16(s.kf[G], q[d0], f32x16{}, 0, 0, 0);
    else                   s.S[kb] = __builtin_amdgcn_mfma_f32_32x32x16_bf16(s.kf[G], q[d0], s.S[kb], 0, 0, 0);
    PIN(s.S[kb]);
    if constexpr (G == 10) { s.mx[0] = fmax3(s.S[0][0], s.S[0][1], s.S[0][2]); s.mx[1] = fmax3(s.S[0][3], s.S[0][4], s.S[0][5]); PIN(s.mx[0]); PIN(s.mx[1]); }
    if constexpr (G == 11) { s.mx[0] = fmax3(s.mx[0], s.S[0][6], s.S[0][7]); s.mx[1] = fmax3(s.mx[1], s.S[0][8], s.S[0][9]); PIN(s.mx[0]); PIN(s.mx[1]); }
    if constexpr (G == 12) { s.mx[0] = fmax3(s.mx[0], s.S[0][10], s.S[0][11]); s.mx[1] = fmax3(s.mx[1], s.S[0][12], s.S[0][13]); PIN(s.mx[0]); PIN(s.mx[1]); }
    if constexpr (G == 13) { s.mx[0] = fmax3(s.mx[0], s.S[0][14], s.S[0][15]); s.mx[0] = fmax2(s.mx[0], s.mx[1]); PIN(s.mx[0]); }
    if constexpr (G == 14) { s.mc = -xhalf_max(s.mx[0]) * CEXP; PIN(s.mc); }
    if constexpr (H == 1 && G < 16) *(lds_u2p)(pv.ow ^ (16u * G)) = pv.w[G];
  } else {
    constexpr int GP = G - 64, ks = pv_ks<H>(GP), db = pv_db<H>(GP);
    if constexpr (GP + PFV < 64) {
      rebase<pv_tile<H>(GP + PFV - 1), pv_tile<H>(GP + PFV)>(s.va);
      v_read<H, GP + PFV>(s);
    }
    const s16x8 vf = __builtin_shufflevector(s.vlo[GP], s.vhi[GP], 0, 1, 2, 3, 4, 5, 6, 7);
    if constexpr (ks == 0) s.o[db] = __builtin_amdgcn_mfma_f32_32x32x16_bf16(__builtin_bit_cast(bf16x8, vf), __builtin_bit_cast(bf16x8, s.P[0]), f32x16{}, 0, 0, 0);
    else                   s.o[db] = __builtin_amdgcn_mfma_f32_32x32x16_bf16(__builtin_bit_cast(bf16x8, vf), __builtin_bit_cast(bf16x8, s.P[ks]), s.o[db], 0, 0, 0);
    PIN(s.o[db]);
  }
  if constexpr (H == 1) {
    constexpr int GL = 64 + PVEND1 + 2;
    if constexpr (G == GL) { s.lt = xhalf_sum(s.l[0] + s.l[1]); PIN(s.lt); }
    if constexpr (G == GL + 1) { s.il = __builtin_amdgcn_rcpf(s.lt); PIN(s.il); s.ow = stage_base(c); }
    if constexpr (G >= 99 && G < 115 && ((G - 99) & 1) == 0) pack_item<(G - 99) / 2, true>(s.o, s.il, s.ow);
    if constexpr (G >= 116 && G < 128 && (G - 116) % 3 == 0) store_half<0, (G - 116) / 3>(c, c.hB);
  }
  if constexpr (H == 0 && G < 48) asm volatile("s_nop %0" :: "i"((G < 16 ? RAMP : (G < 32 ? RAMP / 2 : RAMP / 4)) - 1));
  micro_ops<op_first<H>(G)>(s, std::make_integer_sequence<int, op_first<H>(G + 1) - op_first<H>(G)>{});
  GAP();
}
template <int H, int... Gs> DI void gaps(const Ctx& c, Hd& s, const bf16x8 (&q)[8], const Prev& pv, std::integer_sequence<int, Gs...>) { (gap<H, Gs>(c, s, q, pv), ...); }

DI void fallback(const Ctx& c, unsigned hoff, f32x16 (&o)[4], float& l) {
  const unsigned lane = c.lane, l32 = lane & 31u, h = lane >> 5;
  const char* qp = c.pq + hoff + (c.wid * 32u + l32) * 256u + h * 16u;
  const char* kp = c.pk + hoff + l32 * 256u + h * 16u;
  const short* vp = (const short*)(c.pv + hoff) + l32;
  bf16x8 qf[8];
#pragma unroll
  for (int d = 0; d < 8; ++d) qf[d] = *(const bf16x8*)(qp + 32 * d);
  float m = -__builtin_inff();
#pragma nounroll
  for (int kb = 0; kb < 8; ++kb) {
    f32x16 a = {};
#pragma unroll
    for (int d = 0; d < 8; ++d) a = __builtin_amdgcn_mfma_f32_32x32x16_bf16(*(const bf16x8*)(kp + kb * 8192 + 32 * d), qf[d], a, 0, 0, 0);
#pragma unroll
    for (int r = 0; r < 16; ++r) m = fmax2(m, a[r]);
  }
  m = xhalf_max(m);
  float ls = 0.f;
#pragma unroll
  for (int db = 0; db < 4; ++db) o[db] = f32x16{};
#pragma nounroll
  for (int kb = 0; kb < 8; ++kb) {
    f32x16 a = {};
#pragma unroll
    for (int d = 0; d < 8; ++d) a = __builtin_amdgcn_mfma_f32_32x32x16_bf16(*(const bf16x8*)(kp + kb * 8192 + 32 * d), qf[d], a, 0, 0, 0);
#pragma unroll
    for (int sx = 0; sx < 2; ++sx) {
      float e[8];
#pragma unroll
      for (int j = 0; j < 8; ++j) { e[j] = __builtin_amdgcn_exp2f((a[8 * sx + j] - m) * CEXP); ls += e[j]; }
      const u32x4 p = {cvtpk(e[0], e[1]), cvtpk(e[2], e[3]), cvtpk(e[4], e[5]), cvtpk(e[6], e[7])};
#pragma unroll
      for (int db = 0; db < 4; ++db) {
        s16x8 vf;
#pragma unroll
        for (int j = 0; j < 8; ++j) vf[j] = vp[(kb * 32 + 16 * sx + 8 * (j >> 2) + 4 * h + (j & 3)) * 128 + 32 * db];
        o[db] = __builtin_amdgcn_mfma_f32_32x32x16_bf16(__builtin_bit_cast(bf16x8, vf), __builtin_bit_cast(bf16x8, p), o[db], 0, 0, 0);
      }
    }
  }
  l = xhalf_sum(ls);
}
#define UNSAFE_SUM(l) __builtin_expect(__any(!((l) <= LIM_HI && (l) >= LIM_LO)), 0)

template <int H>
DI void head(const Ctx& c, bf16x8 (&q)[8], Prev& pv) {
  constexpr int R = 8 * H;
  Hd s;
  { const unsigned ra = row_base(opaque(c.lane));
#pragma unroll
    for (int d0 = 0; d0 < 8; ++d0) s.ka[d0] = opaque((ra ^ (32u * d0)) + RING + ((R % 6) >= 3 ? 49152u : 0u)); }
  events_at<1000 * H>(c);
  GAP();
  wait_bar<younger(0, R, 1000 * H + 1)>();
  GAP();
  k_read<H, 0>(s); k_read<H, 1>(s); k_read<H, 2>(s); k_read<H, 3>(s);
  GAP();
  gaps<H>(c, s, q, pv, std::make_integer_sequence<int, 128>{});
  if constexpr (H == 0) {
    float l = xhalf_sum(s.l[0] + s.l[1]);
    if (UNSAFE_SUM(l)) fallback(c, c.hA, s.o, l);
    events_at<999>(c);
    GAP();
    const float il = __builtin_amdgcn_rcpf(l);
#pragma unroll
    for (int i = 0; i < 16; ++i) {
      const f32x16& o = s.o[i >> 2]; const int g = 4 * (i & 3);
      pv.w[i] = u32x2{cvtpk(o[g] * il, o[g + 1] * il), cvtpk(o[g + 2] * il, o[g + 3] * il)};
      PIN(pv.w[i]);
    }
    pv.ow = stage_base(c);
    GAP();
    wait_vm<younger(1, 3, 1000)>();
    GAP();
    read_q(c, q);
  } else {
    float l = s.lt;
    if (UNSAFE_SUM(l)) {
      wait_vm<0>();
      fallback(c, c.hB, s.o, l);
      s.il = __builtin_amdgcn_rcpf(l);
      pack_items<0>(s.o, s.il, s.ow, std::make_integer_sequence<int, 8>{});
      store_half<0, 0>(c, c.hB); store_half<0, 1>(c, c.hB); store_half<0, 2>(c, c.hB); store_half<0, 3>(c, c.hB);
    }
    pack_items<8>(s.o, s.il, s.ow, std::make_integer_sequence<int, 8>{});
    store_half<1, 0>(c, c.hB); store_half<1, 1>(c, c.hB); store_half<1, 2>(c, c.hB); store_half<1, 3>(c, c.hB);
  }
  GAP();
}

__global__ __launch_bounds__(512, 2) void attn_fwd(const void* __restrict__ Q, const void* __restrict__ K, const void* __restrict__ V, void* __restrict__ O) {
  const unsigned tid = threadIdx.x, lane = tid & 63u;
  {
    unsigned long long pc; unsigned off, t, m;
    asm volatile("attn_fwd_code_begin:\n\t"
                 "s_getpc_b64 %0\n\t"
                 "v_lshlrev_b32 %1, 7, %4\n\t"
                 "v_lshrrev_b32 %2, 6, %4\n\t"
                 "s_mov_b32 %3, attn_fwd_code_end-attn_fwd_code_begin\n\t"
                 "v_cmp_gt_u32 vcc, %3, %1\n\t"
                 "v_readfirstlane_b32 %3, %2\n\t"
                 "s_nop 3\n\t"
                 "v_cndmask_b32 %1, 0, %1, vcc\n\t"
                 "s_lshl_b32 %3, %3, 8\n\t"
                 "s_add_u32 %3, %3, %5\n\t"
                 "s_mov_b32 m0, %3\n\t"
                 "s_nop 0\n\t"
                 "global_load_lds_dword %1, %0"
                 : "=&s"(pc), "=&v"(off), "=&v"(t), "=&s"(m) : "v"(tid), "i"(slot_of(5)) : "memory", "vcc", "scc");
    GAP();
  }
  Ctx c;
  c.lane = lane;
  c.wid = __builtin_amdgcn_readfirstlane(tid >> 6);
  const int nbytes = NHEADS * (int)HEADB;
  c.sq = __builtin_amdgcn_make_buffer_rsrc((void*)Q, (short)0, nbytes, 0x00020000);
  c.sk = __builtin_amdgcn_make_buffer_rsrc((void*)K, (short)0, nbytes, 0x00020000);
  c.sv = __builtin_amdgcn_make_buffer_rsrc((void*)V, (short)0, nbytes, 0x00020000);
  c.so = __builtin_amdgcn_make_buffer_rsrc((void*)O, (short)0, nbytes, 0x00020000);
  c.pq = (const char*)Q; c.pk = (const char*)K; c.pv = (const char*)V;
  {
    const unsigned r = lane >> 4, p = lane & 15u, v0 = r * 256u + 16u * (p ^ (r << 2));
#pragma unroll
    for (int j = 0; j < 4; ++j) c.vs[j] = opaque(v0 ^ (16u * j));
    const unsigned w2 = (32u * c.wid) & 48u;
    c.vkv[0] = opaque(v0 ^ w2); c.vkv[1] = opaque(v0 ^ (w2 + 16u));
  }
  { const unsigned b = blockIdx.x, h0 = (b & 7u) * 64u + (b >> 3) * 2u;
    c.hA = h0 * HEADB; c.hB = (h0 + 1u) * HEADB; }
  asm volatile("s_nop 4");
  load_piece<4, 0, 0, -1>(c); load_piece<4, 0, 1, -1>(c); load_piece<4, 1, 0, -1>(c); load_piece<4, 1, 1, -1>(c);
  load_piece<4, 2, 0, -1>(c); load_piece<4, 2, 1, -1>(c); load_piece<4, 3, 0, -1>(c); load_piece<4, 3, 1, -1>(c);
  load_piece<0, 0, 0, -1>(c); load_piece<0, 0, 1, -1>(c); load_piece<0, 1, 0, -1>(c); load_piece<0, 1, 1, -1>(c);
  bf16x8 q[8];
  GAP();
  wait_vm<4>();
  GAP();
  read_q(c, q);
  Prev pv;
  head<0>(c, q, pv);
  head<1>(c, q, pv);
  asm volatile("attn_fwd_code_end:");
}

extern "C" void kernel_launch(void* const* d_in, const int* in_sizes, int n_in, void* d_out, int out_size, void* d_ws, size_t ws_size, hipStream_t stream) {
  (void)in_sizes; (void)n_in; (void)out_size; (void)d_ws; (void)ws_size;
  (void)hipFuncSetAttribute((const void*)attn_fwd, hipFuncAttributeMaxDynamicSharedMemorySize, (int)LDS_BYTES);
  hipLaunchKernelGGL(attn_fwd, dim3(NWG), dim3(512), LDS_BYTES, stream, d_in[0], d_in[1], d_in[2], d_out);
}
```
